```python
import torch

def _softmax(x, axis=-1):
    return torch.softmax(x, dim=axis)
def _gelu(x):
    return torch.nn.functional.gelu(x, approximate="tanh")
def _silu(x):
    return torch.nn.functional.silu(x)
def _sigmoid(x):
    return torch.sigmoid(x)
def _layernorm(x, axis=-1, eps=1e-5):
    m = x.mean(dim=axis, keepdim=True)
    v = x.var(dim=axis, keepdim=True, unbiased=False)
    return (x - m) / torch.sqrt(v + eps)
def _scatter_add(tgt, idx, src, axis=0):
    return tgt.clone().index_add_(axis, idx, src)
def _segment_sum(src, seg, n):
    out = torch.zeros((n,) + src.shape[1:], dtype=src.dtype)
    return out.index_add_(0, seg, src)
def _pad(x, pw):
    flat = tuple(v for pair in reversed(pw) for v in pair)
    return torch.nn.functional.pad(x, flat)

kernel_name = 'standalone__gqa_softmax__near_ridge'

def setup_inputs():

    _w = (lambda a: (lambda t: t.to(torch.bfloat16) if t.is_floating_point() else t)(torch.as_tensor(a)))
    B, Hq, Hkv, Sq, Sk, D = 32, 32, 32, 256, 512, 128
    return {
        "q": _w((lambda *s: torch.randn(*s, dtype=torch.bfloat16) * 0.8)(B, Hq, Sq, D)),
        "k": _w((lambda *s: torch.randn(*s, dtype=torch.bfloat16) * 0.8)(B, Hkv, Sk, D)),
        "v": _w((lambda *s: torch.randn(*s, dtype=torch.bfloat16) * 0.05)(B, Hkv, Sk, D)),
    }

def reference(q, k, v):
    B, Hq, Sq, D = q.shape
    Hkv = k.shape[1]
    rep = Hq // Hkv
    k = (lambda x, n, axis: torch.repeat_interleave(x, n, dim=axis))(k, rep, axis=1)
    v = (lambda x, n, axis: torch.repeat_interleave(x, n, dim=axis))(v, rep, axis=1)
    scale = D ** -0.5
    scores = torch.einsum("bhqd,bhkd->bhqk", q, k) * scale
    p = _softmax(scores, axis=-1)
    return torch.einsum("bhqk,bhkd->bhqd", p, v)
```

```cpp
#include <hip/hip_runtime.h>
#include <hip/hip_bf16.h>
#include <cstdio>

using bf16 = __hip_bfloat16;
constexpr int   D = 128, NW = 8, QBLK = 32, KVBLK = 64;
constexpr float SCALE = 0.088388347648318440f;
#define ATTN_THR 8.f
constexpr float THR = ATTN_THR;
#define ATTN_LAYOUT 0
constexpr int LAYOUT = ATTN_LAYOUT;
#define ATTN_SDEPTH 2
constexpr int SDEPTH = ATTN_SDEPTH;
static_assert(SDEPTH == 1 || SDEPTH == 2, "SDEPTH is 1 or 2");
#define ATTN_B 32
#define ATTN_HQ 32
#define ATTN_HKV 32
#define ATTN_S 256
#define ATTN_SKV 512
constexpr int B = ATTN_B, HQ = ATTN_HQ, HKV = ATTN_HKV, S = ATTN_S, SKV = ATTN_SKV;
constexpr int LDQ = LAYOUT ? HQ * D : D, LDK = LAYOUT ? HKV * D : D, LDO = LDQ;
static_assert(S % (QBLK * NW) == 0 && SKV % (2 * KVBLK) == 0 && HQ % HKV == 0, "S % 256 == 0, SKV % 128 == 0 (even tile count), HQ % HKV == 0");
constexpr size_t SHM_V = KVBLK * D * 2, SHM_K = KVBLK * D * 2, SHM_ATTN = 2 * SHM_V + 2 * SHM_K + NW * 64 * 4;

using bf16x8 = __attribute__((ext_vector_type(8))) short;
using s16x4  = __attribute__((ext_vector_type(4))) short;
using f32x16 = __attribute__((ext_vector_type(16))) float;
using f32x8  = __attribute__((ext_vector_type(8))) float;
using u32x4  = __attribute__((ext_vector_type(4))) unsigned;
#define KSWZ(row, colB) ((row) * 256 + ((colB) ^ (((row) & 7) << 4)))
#define SBAR() __builtin_amdgcn_sched_barrier(0)
__device__ __forceinline__ int crow(int r, int hi) { return (r & 3) + 8 * (r >> 2) + 4 * hi; }
__device__ __forceinline__ unsigned cvtpk(float lo, float hi) {
  unsigned r; asm volatile("v_cvt_pk_bf16_f32 %0, %1, %2" : "=v"(r) : "v"(lo), "v"(hi)); return r;
}
template <typename TIn> struct Stage;
template <> struct Stage<bf16>  { using T = bf16x8;
  __device__ static __forceinline__ T ld8(const bf16* p) { return *reinterpret_cast<const bf16x8*>(p); }
  __device__ static __forceinline__ bf16x8 tobf(T x) { return x; } };
template <> struct Stage<float> { using T = f32x8;
  __device__ static __forceinline__ T ld8(const float* p) { return *reinterpret_cast<const f32x8*>(p); }
  __device__ static __forceinline__ bf16x8 tobf(T x) {
    u32x4 w = {cvtpk(x[0], x[1]), cvtpk(x[2], x[3]), cvtpk(x[4], x[5]), cvtpk(x[6], x[7])}; return *reinterpret_cast<bf16x8*>(&w); } };

__device__ __forceinline__ void partialSM(f32x16& p0, f32x16& p1, float& m_reg, float& mn, float& alpha) {
  constexpr float C = SCALE * 1.4426950408889634f;
  float pmax = p0[0]; for (int r = 1; r < 16; ++r) pmax = fmaxf(pmax, p0[r]); for (int r = 0; r < 16; ++r) pmax = fmaxf(pmax, p1[r]);
  { auto rr = __builtin_amdgcn_permlane32_swap(__float_as_uint(pmax), __float_as_uint(pmax), false, false);
    pmax = fmaxf(__uint_as_float(rr[0]), __uint_as_float(rr[1])); }
  if (__builtin_expect(__all(pmax - m_reg <= THR / SCALE), 1)) { mn = m_reg; alpha = 1.f; }
  else { mn = fmaxf(m_reg, pmax); alpha = __builtin_amdgcn_exp2f((m_reg - mn) * C); m_reg = mn; }
  float mnC = -mn * C;
  for (int r = 0; r < 16; ++r) p0[r] = fmaf(p0[r], C, mnC); for (int r = 0; r < 16; ++r) p1[r] = fmaf(p1[r], C, mnC);
  for (int r = 0; r < 16; ++r) p0[r] = __builtin_amdgcn_exp2f(p0[r]);
}
__device__ __forceinline__ void finishSM(f32x16& p0, f32x16& p1, float alpha, float& l_reg, bf16x8& pa0, bf16x8& pa1, bf16x8& pa2, bf16x8& pa3) {
  for (int r = 0; r < 16; ++r) p1[r] = __builtin_amdgcn_exp2f(p1[r]);
  float ps = 0; for (int r = 0; r < 16; ++r) ps += p0[r]; for (int r = 0; r < 16; ++r) ps += p1[r];
  { auto rr = __builtin_amdgcn_permlane32_swap(__float_as_uint(ps), __float_as_uint(ps), false, false);
    ps = __uint_as_float(rr[0]) + __uint_as_float(rr[1]); }
  l_reg = l_reg * alpha + ps;
#define PK4(P, BASE, OUT) do { unsigned a0 = cvtpk(P[BASE + 0], P[BASE + 1]), a1 = cvtpk(P[BASE + 2], P[BASE + 3]);   \
    unsigned b0 = cvtpk(P[BASE + 4], P[BASE + 5]), b1 = cvtpk(P[BASE + 6], P[BASE + 7]);                              \
    auto r0 = __builtin_amdgcn_permlane32_swap(a0, b0, false, false); auto r1 = __builtin_amdgcn_permlane32_swap(a1, b1, false, false); \
    u32x4 w = {r0[0], r1[0], r0[1], r1[1]}; OUT = *reinterpret_cast<bf16x8*>(&w); } while (0)
  PK4(p0, 0, pa0); PK4(p0, 8, pa1); PK4(p1, 0, pa2); PK4(p1, 8, pa3);
#undef PK4
}
__device__ __forceinline__ void qkt(f32x16& p0, f32x16& p1, const bf16* Ks, const bf16x8* qr, int r32, int hi) {
  p0 = f32x16{}; p1 = f32x16{};
  for (int d0 = 0; d0 < 8; ++d0) { int cb = (d0 * 16 + hi * 8) * 2;
    bf16x8 b0 = *reinterpret_cast<const bf16x8*>((const char*)Ks + KSWZ(r32, cb));
    bf16x8 b1 = *reinterpret_cast<const bf16x8*>((const char*)Ks + KSWZ(32 + r32, cb));
    p0 = __builtin_amdgcn_mfma_f32_32x32x16_bf16(b0, qr[d0], p0, 0, 0, 0);
    p1 = __builtin_amdgcn_mfma_f32_32x32x16_bf16(b1, qr[d0], p1, 0, 0, 0); }
}
__device__ __forceinline__ int v_st(int k, int c) { const int kk = (k & ~0xC) | ((k & 4) << 1) | ((k & 8) >> 1); return ((kk >> 3) * 4 + (c >> 5)) * 512 + ((kk & 7) * 32 + (c & 31)) * 2; }
__device__ __forceinline__ int v_rd_base(int lane) { return ((lane & 3) << 3) | (((lane >> 2) & 3) << 6) | (((lane >> 4) & 1) << 5) | (((lane >> 5) & 1) << 8); }
constexpr int v_rd_off(int d0, int ks, int half) { return d0 * 512 + ks * 4096 + half * 2048; }
template <int OFF> __device__ __forceinline__ s16x4 tr_read(int vb) {
  s16x4 r; asm volatile("ds_read_b64_tr_b16 %0, %1 offset:%2" : "=&v"(r) : "v"(vb), "i"(OFF) : "memory"); return r;
}
template <int D0> __device__ __forceinline__ void pv_one(f32x16& od, int vb, bf16x8 pa0, bf16x8 pa1, bf16x8 pa2, bf16x8 pa3) {
  const s16x4 l0 = tr_read<v_rd_off(D0, 0, 0)>(vb), h0 = tr_read<v_rd_off(D0, 0, 1)>(vb), l1 = tr_read<v_rd_off(D0, 1, 0)>(vb), h1 = tr_read<v_rd_off(D0, 1, 1)>(vb);
  const s16x4 l2 = tr_read<v_rd_off(D0, 2, 0)>(vb), h2 = tr_read<v_rd_off(D0, 2, 1)>(vb), l3 = tr_read<v_rd_off(D0, 3, 0)>(vb), h3 = tr_read<v_rd_off(D0, 3, 1)>(vb);
  asm volatile("s_waitcnt lgkmcnt(0)" ::: "memory"); SBAR();
#define PK(L, H) (bf16x8){L[0], L[1], L[2], L[3], H[0], H[1], H[2], H[3]}
  od = __builtin_amdgcn_mfma_f32_32x32x16_bf16(pa0, PK(l0, h0), od, 0, 0, 0);
  od = __builtin_amdgcn_mfma_f32_32x32x16_bf16(pa1, PK(l1, h1), od, 0, 0, 0);
  od = __builtin_amdgcn_mfma_f32_32x32x16_bf16(pa2, PK(l2, h2), od, 0, 0, 0);
  od = __builtin_amdgcn_mfma_f32_32x32x16_bf16(pa3, PK(l3, h3), od, 0, 0, 0);
#undef PK
}
__device__ __forceinline__ void pv_d0(f32x16* o, int vb, bf16x8 pa0, bf16x8 pa1, bf16x8 pa2, bf16x8 pa3) {
  pv_one<0>(o[0], vb, pa0, pa1, pa2, pa3); pv_one<1>(o[1], vb, pa0, pa1, pa2, pa3); pv_one<2>(o[2], vb, pa0, pa1, pa2, pa3); pv_one<3>(o[3], vb, pa0, pa1, pa2, pa3);
}

template <typename TQ>
__device__ __forceinline__ void attn_dense_body(const TQ* __restrict__ Qb, const bf16* __restrict__ Kh, const bf16* __restrict__ Vh,
                                                bf16* __restrict__ Ob, int seq, char* lds) {
  using St = Stage<bf16>; using SQ = Stage<TQ>;
  const int tid = threadIdx.x, wid = tid >> 6, lane = tid & 63, r32 = lane & 31, hi = lane >> 5;
  bf16* V_lds = (bf16*)lds; bf16* K_lds = (bf16*)(lds + 2 * SHM_V);
  float* ws = (float*)(lds + 2 * SHM_V + 2 * SHM_K) + wid * 64; float* li_l = ws; float* al_l = ws + 32;
  float m_reg = -1e30f, l_reg = 0; f32x16 o[4] = {}; bf16x8 qr[8];
  const TQ* Qw = Qb + (long)(wid * QBLK + r32) * LDQ + hi * 8;
#pragma unroll
  for (int d0 = 0; d0 < 8; ++d0) qr[d0] = SQ::tobf(SQ::ld8(Qw + d0 * 16));
  const int sr = tid >> 4, sc = (tid & 15) * 8, vst0 = v_st(sr, sc), vst1 = v_st(32 + sr, sc);
  const int vb0 = (int)(uintptr_t)V_lds + v_rd_base(lane);
  struct { typename St::T vs0, vs1, ks0, ks1; } sr_[SDEPTH];
#define SLOAD(i, k0) do { sr_[i].vs0 = St::ld8(&Vh[(long)((k0) + sr) * LDK + sc]); sr_[i].vs1 = St::ld8(&Vh[(long)((k0) + 32 + sr) * LDK + sc]); \
    sr_[i].ks0 = St::ld8(&Kh[(long)((k0) + sr) * LDK + sc]); sr_[i].ks1 = St::ld8(&Kh[(long)((k0) + 32 + sr) * LDK + sc]); } while (0)
#define SWRITE(b, i) do { *(bf16x8*)((char*)V_lds + (b) * SHM_V + vst0) = St::tobf(sr_[i].vs0);          \
    *(bf16x8*)((char*)V_lds + (b) * SHM_V + vst1) = St::tobf(sr_[i].vs1); int kc = sc * 2;               \
    *(bf16x8*)((char*)K_lds + (b) * SHM_K + KSWZ(sr, kc)) = St::tobf(sr_[i].ks0);                       \
    *(bf16x8*)((char*)K_lds + (b) * SHM_K + KSWZ(32 + sr, kc)) = St::tobf(sr_[i].ks1); } while (0)
#define SWAIT() do { if constexpr (SDEPTH == 2) asm volatile("s_waitcnt vmcnt(4)" ::: "memory"); else asm volatile("s_waitcnt vmcnt(0)" ::: "memory"); } while (0)
#define RESC(a) do { if (__any((a) < 1.f)) { if (hi == 0) al_l[r32] = (a); asm volatile("s_waitcnt lgkmcnt(0)" ::: "memory"); \
    for (int d = 0; d < 4; ++d) for (int r = 0; r < 16; ++r) o[d][r] *= al_l[crow(r, hi)]; } } while (0)
  f32x16 pA0, pA1, pB0, pB1; float mnA, mnB, alA, alB; bf16x8 pa0, pa1, pa2, pa3; const int NT = seq / KVBLK;
  constexpr int SE = 0, SO = SDEPTH - 1;
  SLOAD(SE, 0); asm volatile("s_waitcnt vmcnt(0)" ::: "memory"); SWRITE(0, SE); __syncthreads();
  qkt(pA0, pA1, K_lds, qr, r32, hi); partialSM(pA0, pA1, m_reg, mnA, alA);
  SLOAD(SO, KVBLK); if constexpr (SDEPTH == 2) { if (2 < NT) SLOAD(SE, 2 * KVBLK); }
  SWAIT(); SWRITE(1, SO); __syncthreads();
  for (int j = 1; j + 1 < NT; j += 2) {
    SBAR(); qkt(pB0, pB1, (bf16*)((char*)K_lds + SHM_K), qr, r32, hi);
    finishSM(pA0, pA1, alA, l_reg, pa0, pa1, pa2, pa3); SBAR();
    SLOAD(SO, (j + SDEPTH) * KVBLK); SBAR();
    pv_d0(o, vb0, pa0, pa1, pa2, pa3); partialSM(pB0, pB1, m_reg, mnB, alB);
    __syncthreads(); SWAIT(); SWRITE(0, SE);
    RESC(alB); __syncthreads();
    SBAR(); qkt(pA0, pA1, K_lds, qr, r32, hi);
    finishSM(pB0, pB1, alB, l_reg, pa0, pa1, pa2, pa3); SBAR();
    if (SDEPTH == 1 || j + 3 < NT) SLOAD(SE, (j + 1 + SDEPTH) * KVBLK); SBAR();
    pv_d0(o, vb0 + (int)SHM_V, pa0, pa1, pa2, pa3); partialSM(pA0, pA1, m_reg, mnA, alA);
    __syncthreads(); SWAIT(); SWRITE(1, SO);
    RESC(alA); __syncthreads();
  }
  SBAR(); qkt(pB0, pB1, (bf16*)((char*)K_lds + SHM_K), qr, r32, hi);
  finishSM(pA0, pA1, alA, l_reg, pa0, pa1, pa2, pa3); SBAR();
  pv_d0(o, vb0, pa0, pa1, pa2, pa3); partialSM(pB0, pB1, m_reg, mnB, alB);
  __syncthreads(); RESC(alB);
  finishSM(pB0, pB1, alB, l_reg, pa0, pa1, pa2, pa3); SBAR();
  pv_d0(o, vb0 + (int)SHM_V, pa0, pa1, pa2, pa3);
  if (hi == 0) li_l[r32] = l_reg; asm volatile("s_waitcnt lgkmcnt(0)" ::: "memory");
  float rli[16];
#pragma unroll
  for (int r = 0; r < 16; ++r) rli[r] = __builtin_amdgcn_rcpf(li_l[crow(r, hi)]);
  bf16* Ow = Ob + (long)(wid * QBLK) * LDO;
#pragma unroll
  for (int r = 0; r < 16; ++r) { int orow = crow(r, hi);
    for (int d0 = 0; d0 < 4; ++d0) Ow[(long)orow * LDO + d0 * 32 + r32] = __float2bfloat16(o[d0][r] * rli[r]); }
#undef SLOAD
#undef SWRITE
#undef SWAIT
#undef RESC
}

template <typename TQ>
__global__ __launch_bounds__(64 * NW, 1)
void attn_fwd_dense(const TQ* __restrict__ Q, const bf16* __restrict__ K, const bf16* __restrict__ V, bf16* __restrict__ O, int sq, int skv, int GQA) {
  extern __shared__ __attribute__((aligned(16))) char shm[];
  const long b = blockIdx.z, h = blockIdx.y, Hq = gridDim.y, Hkv = Hq / GQA, kvh = h / GQA, i0 = (long)blockIdx.x * (QBLK * NW);
  if constexpr (LAYOUT == 0) {
    const long q0 = ((b * Hq + h) * sq + i0) * D, k0 = (b * Hkv + kvh) * skv * (long)D;
    attn_dense_body<TQ>(Q + q0, K + k0, V + k0, O + q0, skv, shm);
  } else {
    const long q0 = ((b * sq + i0) * HQ + h) * (long)D, k0 = (b * skv * HKV + kvh) * (long)D;
    attn_dense_body<TQ>(Q + q0, K + k0, V + k0, O + q0, skv, shm);
  }
}

extern "C" void kernel_launch(void* const* d_in, const int* in_sizes, int n_in, void* d_out, int out_size, void* d_ws, size_t ws_size, hipStream_t stream) {
  hipFuncSetAttribute((const void*)attn_fwd_dense<bf16>, hipFuncAttributeMaxDynamicSharedMemorySize, (int)SHM_ATTN);
  hipLaunchKernelGGL(attn_fwd_dense<bf16>, dim3(S / (QBLK * NW), HQ, B), dim3(64 * NW), SHM_ATTN, stream,
                     (const bf16*)d_in[0], (const bf16*)d_in[1], (const bf16*)d_in[2], (bf16*)d_out, S, SKV, HQ / HKV);
}
```

```cpp
#include <hip/hip_runtime.h>
#include <hip/hip_bf16.h>
#include <cstdint>

typedef short    bf16x8 __attribute__((ext_vector_type(8)));
typedef short    s16x4  __attribute__((ext_vector_type(4)));
typedef float    f32x16 __attribute__((ext_vector_type(16)));
typedef unsigned u32x4  __attribute__((ext_vector_type(4)));
typedef __attribute__((address_space(3))) char lds_char;
typedef __attribute__((address_space(3))) const bf16x8 lds_bf16x8;
typedef __attribute__((address_space(3))) u32x4 lds_u32x4;
typedef __attribute__((address_space(3))) s16x4 lds_s16x4;

#define THR 0.0f
#define STPOL 16
#define LDPOL "nt"
#define DI __device__ __forceinline__
#define SBAR() __builtin_amdgcn_sched_barrier(0)
#define MFMA(a, b, c) __builtin_amdgcn_mfma_f32_32x32x16_bf16(a, b, c, 0, 0, 0)

constexpr int NHEADS = 1024, NWG = 256, HPW = NHEADS / NWG;
constexpr unsigned TILEB = 16384, KOFF = 0, VOFF = 3 * TILEB, QOFF = 6 * TILEB, LDSB = QOFF + 8 * 8192;
constexpr unsigned QHB = 256 * 128 * 2, KHB = 512 * 128 * 2;
constexpr float CS = 0.088388347648318440f * 1.4426950408889634f;

typedef float  f32x2  __attribute__((ext_vector_type(2)));
typedef __bf16 bf16x2 __attribute__((ext_vector_type(2)));
DI unsigned cvtpk(float lo, float hi) { f32x2 v = {lo, hi}; return __builtin_bit_cast(unsigned, __builtin_convertvector(v, bf16x2)); }
template <int N> DI void wait_bar() { asm volatile("s_waitcnt vmcnt(%0) lgkmcnt(0)\n\ts_barrier" :: "i"(N) : "memory"); }
DI void bar_only() { asm volatile("s_waitcnt lgkmcnt(0)\n\ts_barrier" ::: "memory"); }
template <int N> DI void wait_vm() { asm volatile("s_waitcnt vmcnt(%0)" :: "i"(N) : "memory"); }
template <int OFF> DI void dma(unsigned m0v, unsigned voff, __amdgpu_buffer_rsrc_t srd, unsigned soff) {
  asm volatile("s_mov_b32 m0, %0\n\ts_nop 0\n\tbuffer_load_dwordx4 %1, %2, %3 offen offset:%4 " LDPOL " lds"
               :: "s"(m0v), "v"(voff), "s"(srd), "s"(soff), "i"(OFF) : "memory");
}
DI lds_char* opq(unsigned a) { asm volatile("" : "+v"(a)); return (lds_char*)(size_t)a; }
DI bf16x8 ldsr128(lds_char* a) { return *(lds_bf16x8*)a; }
DI s16x4 ldstr(lds_char* a) { return __builtin_amdgcn_ds_read_tr16_b64_v4i16((lds_s16x4*)a); }
constexpr int exops(int j) { j = (j + 8) & 7; return j == 0 ? 8 : (j >= 1 && j <= 4) ? 2 : 0; }

struct St {
  f32x16 o[4]; f32x16 s0, s1; bf16x8 q[8]; u32x4 p[4];
  float m, l;
};

template <bool DOQK, bool DOPV, bool ZERO> DI void seg_m(St& c, unsigned ka0_, unsigned ka1_, unsigned va0_, unsigned va1_) {
  lds_char *ka0 = opq(ka0_), *ka1 = opq(ka1_), *va0 = opq(va0_), *va1 = opq(va1_);
  constexpr int L = 4, M0 = DOQK ? 0 : 16, M1 = DOPV ? 32 : 16;
  bf16x8 f[32];
  auto rd = [&](int m) {
    if (m < 16) { const int d0 = m >> 1, kb = m & 1; f[m] = ldsr128(((d0 & 1) ? ka1 : ka0) + 8192 * kb + 256 * (d0 >> 1)); }
    else { const int s = (m - 16) >> 2, db = (m - 16) & 3;
      const s16x4 lo = ldstr(va0 + 4096 * s + 256 * db), hi = ldstr(va1 + 4096 * s + 2048 + 256 * db);
      f[m] = __builtin_shufflevector(lo, hi, 0, 1, 2, 3, 4, 5, 6, 7); }
  };
  __builtin_amdgcn_s_setprio(1);
#pragma unroll
  for (int m = M0; m < M0 + L; ++m) rd(m);
  SBAR();
#pragma unroll
  for (int m = M0; m < M1; ++m) {
    if (m + L < M1) rd(m + L);
    if (m < 16) { const int d0 = m >> 1, kb = m & 1;
      if (kb == 0) c.s0 = MFMA(f[m], c.q[d0], d0 == 0 ? f32x16{} : c.s0); else c.s1 = MFMA(f[m], c.q[d0], d0 == 0 ? f32x16{} : c.s1); }
    else { const int s = (m - 16) >> 2, db = (m - 16) & 3;
      c.o[db] = MFMA(f[m], __builtin_bit_cast(bf16x8, c.p[s]), (ZERO && s == 0) ? f32x16{} : c.o[db]); }
    SBAR();
  }
  __builtin_amdgcn_s_setprio(0);
}

DI float swapmax(float x) { auto r = __builtin_amdgcn_permlane32_swap(__float_as_uint(x), __float_as_uint(x), false, false); return __builtin_fmaxf(__uint_as_float(r[0]), __uint_as_float(r[1])); }
DI float swapsum(float x) { auto r = __builtin_amdgcn_permlane32_swap(__float_as_uint(x), __float_as_uint(x), false, false); return __uint_as_float(r[0]) + __uint_as_float(r[1]); }
#define MX3(a, b, c) __builtin_fmaxf(__builtin_fmaxf((a), (b)), (c))

template <bool FIRST> DI void softmax(St& c) {
  float a = MX3(c.s0[0], c.s0[1], c.s1[0]), b = MX3(c.s0[2], c.s0[3], c.s1[1]); a = MX3(a, c.s1[2], c.s1[3]);
#pragma unroll
  for (int r = 4; r < 16; r += 4) { a = MX3(a, c.s0[r], c.s0[r + 1]); b = MX3(b, c.s0[r + 2], c.s0[r + 3]); a = MX3(a, c.s1[r], c.s1[r + 1]); b = MX3(b, c.s1[r + 2], c.s1[r + 3]); }
  const float rm = swapmax(__builtin_fmaxf(a, b)) * CS;
  if (FIRST) { c.m = rm; c.l = 0.f; }
  else if (__any(rm > c.m + THR)) {
    const float mn = __builtin_fmaxf(c.m, rm), al = __builtin_amdgcn_exp2f(c.m - mn);
    c.m = mn; c.l *= al;
#pragma unroll
    for (int d = 0; d < 4; ++d)
#pragma unroll
      for (int r = 0; r < 16; ++r) c.o[d][r] *= al;
  }
  const float nm = -c.m; float l0 = 0.f, l1 = 0.f;
#pragma unroll
  for (int r = 0; r < 16; ++r) { c.s0[r] = __builtin_amdgcn_exp2f(__builtin_fmaf(c.s0[r], CS, nm)); c.s1[r] = __builtin_amdgcn_exp2f(__builtin_fmaf(c.s1[r], CS, nm)); }
#pragma unroll
  for (int r = 0; r < 16; ++r) { l0 += c.s0[r]; l1 += c.s1[r]; }
  c.l += l0 + l1;
#pragma unroll
  for (int e = 0; e < 4; ++e) {
    c.p[0][e] = cvtpk(c.s0[2 * e], c.s0[2 * e + 1]); c.p[1][e] = cvtpk(c.s0[8 + 2 * e], c.s0[8 + 2 * e + 1]);
    c.p[2][e] = cvtpk(c.s1[2 * e], c.s1[2 * e + 1]); c.p[3][e] = cvtpk(c.s1[8 + 2 * e], c.s1[8 + 2 * e + 1]); }
}

DI void epilogue(St& c, float l, unsigned stage, unsigned lane, __amdgpu_buffer_rsrc_t so, unsigned soff) {
  asm volatile("" : "+v"(lane));
  const float il = __builtin_amdgcn_rcpf(swapsum(l));
  const unsigned r = lane & 31, hi = lane >> 5;
  lds_char* wb[4];
#pragma unroll
  for (int x = 0; x < 4; ++x) wb[x] = opq(stage + 256 * r + 16 * ((4 * (x >> 1) + 2 * hi + (x & 1)) ^ (r & 7)));
#pragma unroll
  for (int db = 0; db < 4; ++db)
#pragma unroll
    for (int e = 0; e < 2; ++e) {
      u32x4 w;
#pragma unroll
      for (int i = 0; i < 4; ++i) w[i] = cvtpk(c.o[db][8 * e + 2 * i] * il, c.o[db][8 * e + 2 * i + 1] * il);
      *(lds_u32x4*)(wb[2 * (db & 1) + e] + 128 * (db >> 1)) = w;
    }
  lds_char* rb[2];
#pragma unroll
  for (int x = 0; x < 2; ++x) rb[x] = opq(stage + 256 * (lane >> 4) + 16 * ((lane & 15) ^ (4 * x + (lane >> 4))));
  unsigned vo = lane * 16; asm volatile("" : "+v"(vo));
#pragma unroll
  for (int j = 0; j < 8; ++j) {
    const u32x4 v = *(lds_u32x4*)(rb[j & 1] + 1024 * j);
    __builtin_amdgcn_raw_buffer_store_b128(v, so, vo + 1024 * (j & 3), soff + 4096 * (j >> 2), STPOL);
  }
}

__global__ __launch_bounds__(512, 2) void attn_fwd(const char* __restrict__ Q, const char* __restrict__ K, const char* __restrict__ V, char* __restrict__ O) {
  __shared__ __attribute__((aligned(16))) char lds[LDSB];
  const unsigned lane = threadIdx.x & 63, w = __builtin_amdgcn_readfirstlane(threadIdx.x >> 6), grp = w >> 2, w4 = w & 3;
  const unsigned r = lane & 31, hi = lane >> 5;
  const unsigned ldsb = (unsigned)(size_t)(lds_char*)lds;
  unsigned voff[4];
#pragma unroll
  for (int x = 0; x < 4; ++x) voff[x] = 256 * ((lane >> 2) & 3) + 64 * (lane >> 4) + 16 * ((lane & 3) ^ x);
  unsigned kb_[2], vb_[2];
#pragma unroll
  for (int par = 0; par < 2; ++par) {
    kb_[par] = 1024 * (r >> 2) + 64 * (r & 3) + 16 * ((2 * par + hi) ^ ((r >> 2) & 3));
    const unsigned g16 = (lane >> 4) & 1, qq = (lane >> 2) & 3, p = lane & 3;
    vb_[par] = 1024 * hi + 64 * qq + 16 * ((2 * (p & 1) + g16) ^ (2 * par + hi)) + 8 * (p >> 1);
  }
  const unsigned stage = ldsb + QOFF + 8192 * w;
  const char* KV = grp ? K : V;
  const unsigned ring = ldsb + (grp ? KOFF : VOFF) + 4096 * w4;
  const unsigned blk = blockIdx.x;
  const __amdgpu_buffer_rsrc_t so = __builtin_amdgcn_make_buffer_rsrc((void*)O, (short)0, NHEADS * QHB, 0x20000);
  auto ldtile = [&](int t, unsigned slot) {
    const unsigned tt = t < 8 * HPW ? t : 0;
    const __amdgpu_buffer_rsrc_t srd = __builtin_amdgcn_make_buffer_rsrc((void*)KV, (short)0, t < 8 * HPW ? NHEADS * KHB : 0, 0x20000);
    const unsigned soff = (blk + NWG * (tt >> 3)) * KHB + (tt & 7) * TILEB + 4096 * w4, m0v = ring + slot;
    dma<0>(m0v, voff[0], srd, soff); dma<1024>(m0v, voff[1], srd, soff); dma<2048>(m0v, voff[2], srd, soff); dma<3072>(m0v, voff[3], srd, soff);
  };
  auto ldq2 = [&](int h, int i) {
    const unsigned hh = h < HPW ? h : 0;
    const __amdgpu_buffer_rsrc_t srd = __builtin_amdgcn_make_buffer_rsrc((void*)Q, (short)0, h < HPW ? NHEADS * QHB : 0, 0x20000);
    const unsigned soff = (blk + NWG * hh) * QHB + 8192 * w + 2048 * i, m0v = stage + 2048 * i;
    dma<0>(m0v, voff[(2 * i) & 3], srd, soff); dma<1024>(m0v, voff[(2 * i + 1) & 3], srd, soff);
  };
  St c;
  auto rdq = [&]() {
    lds_char *qa0 = opq(stage + kb_[0]), *qa1 = opq(stage + kb_[1]);
#pragma unroll
    for (int d0 = 0; d0 < 8; ++d0) c.q[d0] = ldsr128(((d0 & 1) ? qa1 : qa0) + 256 * (d0 >> 1));
  };
  asm volatile("s_nop 4" ::: "memory");
  if (grp) { ldtile(0, 0); ldq2(0, 0); ldq2(0, 1); ldq2(0, 2); ldq2(0, 3); ldtile(1, TILEB); ldtile(2, 2 * TILEB); wait_bar<16>(); wait_vm<8>(); rdq(); bar_only(); }
  else     { ldq2(0, 0); ldq2(0, 1); ldq2(0, 2); ldq2(0, 3); ldtile(0, 0); wait_vm<4>(); rdq(); bar_only(); }
  unsigned sl0 = 0, sl1 = TILEB, sl2 = 2 * TILEB;
  float lprev = 0.f;
  for (int h = 0; h < HPW; ++h) {
#pragma unroll
    for (int j = 0; j < 8; ++j) {
      const int t = 8 * h + j;
      {
        const unsigned ka0 = ldsb + KOFF + sl0 + kb_[0], ka1 = ldsb + KOFF + sl0 + kb_[1], va0 = ldsb + VOFF + sl2 + vb_[0], va1 = ldsb + VOFF + sl2 + vb_[1];
        if (j == 0) { if (h == 0) seg_m<true, false, false>(c, ka0, ka1, va0, va1); else seg_m<true, true, false>(c, ka0, ka1, va0, va1); }
        else if (j == 1) seg_m<true, true, true>(c, ka0, ka1, va0, va1);
        else seg_m<true, true, false>(c, ka0, ka1, va0, va1);
      }
      if (grp) {
        constexpr int dummy = 0; (void)dummy;
        if ((j == 1 || j == 2) && h == 0) { if (j == 1) wait_bar<4 + exops(7)>(); else wait_bar<4 + exops(1)>(); }
        else switch (j) { case 0: wait_bar<exops(-2) + 4 + exops(-1)>(); break; case 1: wait_bar<exops(-1) + 4 + exops(0)>(); break; case 2: wait_bar<exops(0) + 4 + exops(1)>(); break;
          case 3: wait_bar<exops(1) + 4 + exops(2)>(); break; case 4: wait_bar<exops(2) + 4 + exops(3)>(); break; case 5: wait_bar<exops(3) + 4 + exops(4)>(); break;
          case 6: wait_bar<exops(4) + 4 + exops(5)>(); break; default: wait_bar<exops(5) + 4 + exops(6)>(); break; }
      } else bar_only();
      if (grp) ldtile(t + 3, sl0); else ldtile(t + 1, sl1);
      if (j >= 1 && j <= 4) ldq2(h + 1, j - 1);
      if (j == 0) {
        lprev = c.l;
        softmax<true>(c);
        if (h > 0) epilogue(c, lprev, stage, lane, so, (blk + NWG * (h - 1)) * QHB + 8192 * w);
      } else softmax<false>(c);
      if (j == 7) { wait_vm<12>(); rdq(); }
      if (!grp) {
        if ((j == 0 || j == 1) && h == 0) { if (j == 0) wait_bar<4 + exops(7)>(); else wait_bar<4 + exops(1)>(); }
        else switch (j) { case 0: wait_bar<exops(-1) + 4 + exops(0)>(); break; case 1: wait_bar<exops(0) + 4 + exops(1)>(); break; case 2: wait_bar<exops(1) + 4 + exops(2)>(); break;
          case 3: wait_bar<exops(2) + 4 + exops(3)>(); break; case 4: wait_bar<exops(3) + 4 + exops(4)>(); break; case 5: wait_bar<exops(4) + 4 + exops(5)>(); break;
          case 6: wait_bar<exops(5) + 4 + exops(6)>(); break; default: wait_bar<exops(6) + 4 + exops(7)>(); break; }
      } else if (!(j == 7 && h == HPW - 1)) bar_only();
      { const unsigned x = sl0; sl0 = sl1; sl1 = sl2; sl2 = x; }
    }
  }
  {
    const unsigned va0 = ldsb + VOFF + sl2 + vb_[0], va1 = ldsb + VOFF + sl2 + vb_[1];
    seg_m<false, true, false>(c, 0, 0, va0, va1);
    epilogue(c, c.l, stage, lane, so, (blk + NWG * (HPW - 1)) * QHB + 8192 * w);
  }
}

extern "C" void kernel_launch(void* const* d_in, const int* in_sizes, int n_in, void* d_out, int out_size, void* d_ws, size_t ws_size, hipStream_t stream) {
  hipLaunchKernelGGL(attn_fwd, dim3(NWG), dim3(512), 0, stream, (const char*)d_in[0], (const char*)d_in[1], (const char*)d_in[2], (char*)d_out);
}
```

```cpp
#include <hip/hip_runtime.h>
#include <hip/hip_bf16.h>
#include <cstdint>

typedef short    bf16x8 __attribute__((ext_vector_type(8)));
typedef short    s16x4  __attribute__((ext_vector_type(4)));
typedef float    f32x16 __attribute__((ext_vector_type(16)));
typedef unsigned u32x4  __attribute__((ext_vector_type(4)));
typedef __attribute__((address_space(3))) char lds_char;
typedef __attribute__((address_space(3))) const bf16x8 lds_bf16x8;
typedef __attribute__((address_space(3))) u32x4 lds_u32x4;
typedef __attribute__((address_space(3))) s16x4 lds_s16x4;

#define THR 8.0f
#define STPOL 16
#define LDPOL "nt"
#define DI __device__ __forceinline__
#define SBAR() __builtin_amdgcn_sched_barrier(0)
#define MFMA(a, b, c) __builtin_amdgcn_mfma_f32_32x32x16_bf16(a, b, c, 0, 0, 0)

constexpr int NHEADS = 1024, NWG = 256, HPW = NHEADS / NWG;
constexpr unsigned TILEB = 16384, KOFF = 0, VOFF = 3 * TILEB, QOFF = 6 * TILEB, LDSB = QOFF + 8 * 8192;
constexpr unsigned QHB = 256 * 128 * 2, KHB = 512 * 128 * 2;
constexpr float CS = 0.088388347648318440f * 1.4426950408889634f;

typedef float  f32x2  __attribute__((ext_vector_type(2)));
typedef __bf16 bf16x2 __attribute__((ext_vector_type(2)));
DI unsigned cvtpk(float lo, float hi) { f32x2 v = {lo, hi}; return __builtin_bit_cast(unsigned, __builtin_convertvector(v, bf16x2)); }
template <int N> DI void wait_bar() { asm volatile("s_waitcnt vmcnt(%0) lgkmcnt(0)\n\ts_barrier" :: "i"(N) : "memory"); }
DI void bar_only() { asm volatile("s_waitcnt lgkmcnt(0)\n\ts_barrier" ::: "memory"); }
template <int N> DI void wait_vm() { asm volatile("s_waitcnt vmcnt(%0)" :: "i"(N) : "memory"); }
template <int OFF> DI void dma(unsigned m0v, unsigned voff, __amdgpu_buffer_rsrc_t srd, unsigned soff) {
  asm volatile("s_mov_b32 m0, %0\n\ts_nop 0\n\tbuffer_load_dwordx4 %1, %2, %3 offen offset:%4 " LDPOL " lds"
               :: "s"(m0v), "v"(voff), "s"(srd), "s"(soff), "i"(OFF) : "memory");
}
DI lds_char* opq(unsigned a) { asm volatile("" : "+v"(a)); return (lds_char*)(size_t)a; }
DI bf16x8 ldsr128(lds_char* a) { return *(lds_bf16x8*)a; }
DI s16x4 ldstr(lds_char* a) { return __builtin_amdgcn_ds_read_tr16_b64_v4i16((lds_s16x4*)a); }
constexpr int exops(int j) { j = (j + 8) & 7; return j == 0 ? 8 : (j >= 1 && j <= 4) ? 2 : 0; }

struct St {
  f32x16 o[4]; f32x16 s0, s1; bf16x8 q[8]; u32x4 p[4];
  float m, l;
};

template <bool DOQK, bool DOPV, bool ZERO> DI void seg_m(St& c, unsigned ka0_, unsigned ka1_, unsigned va0_, unsigned va1_) {
  lds_char *ka0 = opq(ka0_), *ka1 = opq(ka1_), *va0 = opq(va0_), *va1 = opq(va1_);
  constexpr int L = 4, M0 = DOQK ? 0 : 16, M1 = DOPV ? 32 : 16;
  bf16x8 f[32];
  auto rd = [&](int m) {
    if (m < 16) { const int d0 = m >> 1, kb = m & 1; f[m] = ldsr128(((d0 & 1) ? ka1 : ka0) + 8192 * kb + 256 * (d0 >> 1)); }
    else { const int s = (m - 16) >> 2, db = (m - 16) & 3;
      const s16x4 lo = ldstr(va0 + 4096 * s + 256 * db), hi = ldstr(va1 + 4096 * s + 2048 + 256 * db);
      f[m] = __builtin_shufflevector(lo, hi, 0, 1, 2, 3, 4, 5, 6, 7); }
  };

#pragma unroll
  for (int m = M0; m < M0 + L; ++m) rd(m);
  SBAR();
#pragma unroll
  for (int m = M0; m < M1; ++m) {
    if (m + L < M1) rd(m + L);
    if (m < 16) { const int d0 = m >> 1, kb = m & 1;
      if (kb == 0) c.s0 = MFMA(f[m], c.q[d0], d0 == 0 ? f32x16{} : c.s0); else c.s1 = MFMA(f[m], c.q[d0], d0 == 0 ? f32x16{} : c.s1); }
    else { const int s = (m - 16) >> 2, db = (m - 16) & 3;
      c.o[db] = MFMA(f[m], __builtin_bit_cast(bf16x8, c.p[s]), (ZERO && s == 0) ? f32x16{} : c.o[db]); }
    SBAR();
  }

}

DI float swapmax(float x) { auto r = __builtin_amdgcn_permlane32_swap(__float_as_uint(x), __float_as_uint(x), false, false); return __builtin_fmaxf(__uint_as_float(r[0]), __uint_as_float(r[1])); }
DI float swapsum(float x) { auto r = __builtin_amdgcn_permlane32_swap(__float_as_uint(x), __float_as_uint(x), false, false); return __uint_as_float(r[0]) + __uint_as_float(r[1]); }
#define MX3(a, b, c) __builtin_fmaxf(__builtin_fmaxf((a), (b)), (c))

template <bool FIRST> DI void softmax(St& c) {
  float a = MX3(c.s0[0], c.s0[1], c.s1[0]), b = MX3(c.s0[2], c.s0[3], c.s1[1]); a = MX3(a, c.s1[2], c.s1[3]);
#pragma unroll
  for (int r = 4; r < 16; r += 4) { a = MX3(a, c.s0[r], c.s0[r + 1]); b = MX3(b, c.s0[r + 2], c.s0[r + 3]); a = MX3(a, c.s1[r], c.s1[r + 1]); b = MX3(b, c.s1[r + 2], c.s1[r + 3]); }
  const float rm = swapmax(__builtin_fmaxf(a, b)) * CS;
  if (FIRST) { c.m = rm; c.l = 0.f; }
  else if (__any(rm > c.m + THR)) {
    const float mn = __builtin_fmaxf(c.m, rm), al = __builtin_amdgcn_exp2f(c.m - mn);
    c.m = mn; c.l *= al;
#pragma unroll
    for (int d = 0; d < 4; ++d)
#pragma unroll
      for (int r = 0; r < 16; ++r) c.o[d][r] *= al;
  }
  const float nm = -c.m; float l0 = 0.f, l1 = 0.f;
#pragma unroll
  for (int r = 0; r < 16; ++r) { c.s0[r] = __builtin_amdgcn_exp2f(__builtin_fmaf(c.s0[r], CS, nm)); c.s1[r] = __builtin_amdgcn_exp2f(__builtin_fmaf(c.s1[r], CS, nm)); }
#pragma unroll
  for (int r = 0; r < 16; ++r) { l0 += c.s0[r]; l1 += c.s1[r]; }
  c.l += l0 + l1;
#pragma unroll
  for (int e = 0; e < 4; ++e) {
    c.p[0][e] = cvtpk(c.s0[2 * e], c.s0[2 * e + 1]); c.p[1][e] = cvtpk(c.s0[8 + 2 * e], c.s0[8 + 2 * e + 1]);
    c.p[2][e] = cvtpk(c.s1[2 * e], c.s1[2 * e + 1]); c.p[3][e] = cvtpk(c.s1[8 + 2 * e], c.s1[8 + 2 * e + 1]); }
  asm volatile("" : "+v"(c.p[0]), "+v"(c.p[1]), "+v"(c.p[2]), "+v"(c.p[3]), "+v"(c.l));
}

DI void epilogue(St& c, float l, unsigned stage, unsigned lane, __amdgpu_buffer_rsrc_t so, unsigned soff) {
  asm volatile("" : "+v"(lane));
  const float il = __builtin_amdgcn_rcpf(swapsum(l));
  const unsigned r = lane & 31, hi = lane >> 5;
  lds_char* wb[4];
#pragma unroll
  for (int x = 0; x < 4; ++x) wb[x] = opq(stage + 256 * r + 16 * ((4 * (x >> 1) + 2 * hi + (x & 1)) ^ (r & 7)));
#pragma unroll
  for (int db = 0; db < 4; ++db)
#pragma unroll
    for (int e = 0; e < 2; ++e) {
      u32x4 w;
#pragma unroll
      for (int i = 0; i < 4; ++i) w[i] = cvtpk(c.o[db][8 * e + 2 * i] * il, c.o[db][8 * e + 2 * i + 1] * il);
      *(lds_u32x4*)(wb[2 * (db & 1) + e] + 128 * (db >> 1)) = w;
    }
  lds_char* rb[2];
#pragma unroll
  for (int x = 0; x < 2; ++x) rb[x] = opq(stage + 256 * (lane >> 4) + 16 * ((lane & 15) ^ (4 * x + (lane >> 4))));
  unsigned vo = lane * 16; asm volatile("" : "+v"(vo));
#pragma unroll
  for (int j = 0; j < 8; ++j) {
    const u32x4 v = *(lds_u32x4*)(rb[j & 1] + 1024 * j);
    __builtin_amdgcn_raw_buffer_store_b128(v, so, vo + 1024 * (j & 3), soff + 4096 * (j >> 2), STPOL);
  }
}

__global__ __launch_bounds__(512, 2) void attn_fwd(const char* __restrict__ Q, const char* __restrict__ K, const char* __restrict__ V, char* __restrict__ O
  ) {
  __shared__ __attribute__((aligned(16))) char lds[LDSB];
  const unsigned lane = threadIdx.x & 63, w = __builtin_amdgcn_readfirstlane(threadIdx.x >> 6), grp = w >> 2, w4 = w & 3;
  const unsigned r = lane & 31, hi = lane >> 5;
  const unsigned ldsb = (unsigned)(size_t)(lds_char*)lds;
  unsigned voff[4];
#pragma unroll
  for (int x = 0; x < 4; ++x) voff[x] = 256 * ((lane >> 2) & 3) + 64 * (lane >> 4) + 16 * ((lane & 3) ^ x);
  unsigned kb_[2], vb_[2];
#pragma unroll
  for (int par = 0; par < 2; ++par) {
    kb_[par] = 1024 * (r >> 2) + 64 * (r & 3) + 16 * ((2 * par + hi) ^ ((r >> 2) & 3));
    const unsigned g16 = (lane >> 4) & 1, qq = (lane >> 2) & 3, p = lane & 3;
    vb_[par] = 1024 * hi + 64 * qq + 16 * ((2 * (p & 1) + g16) ^ (2 * par + hi)) + 8 * (p >> 1);
  }
  const unsigned stage = ldsb + QOFF + 8192 * w;
  const char* KV = grp ? K : V;
  const unsigned ring = ldsb + (grp ? KOFF : VOFF) + 4096 * w4;
  const unsigned blk = blockIdx.x;
  const __amdgpu_buffer_rsrc_t so = __builtin_amdgcn_make_buffer_rsrc((void*)O, (short)0, NHEADS * QHB, 0x20000);
  auto ldtile = [&](int t, unsigned slot) {
    const unsigned tt = t < 8 * HPW ? t : 0;
    const __amdgpu_buffer_rsrc_t srd = __builtin_amdgcn_make_buffer_rsrc((void*)KV, (short)0, t < 8 * HPW ? NHEADS * KHB : 0, 0x20000);
    const unsigned soff = (blk + NWG * (tt >> 3)) * KHB + (tt & 7) * TILEB + 4096 * w4, m0v = ring + slot;
    dma<0>(m0v, voff[0], srd, soff); dma<1024>(m0v, voff[1], srd, soff); dma<2048>(m0v, voff[2], srd, soff); dma<3072>(m0v, voff[3], srd, soff);
  };
  auto ldq2 = [&](int h, int i) {
    const unsigned hh = h < HPW ? h : 0;
    const __amdgpu_buffer_rsrc_t srd = __builtin_amdgcn_make_buffer_rsrc((void*)Q, (short)0, h < HPW ? NHEADS * QHB : 0, 0x20000);
    const unsigned soff = (blk + NWG * hh) * QHB + 8192 * w + 2048 * i, m0v = stage + 2048 * i;
    dma<0>(m0v, voff[(2 * i) & 3], srd, soff); dma<1024>(m0v, voff[(2 * i + 1) & 3], srd, soff);
  };
  St c;
  auto rdq = [&]() {
    lds_char *qa0 = opq(stage + kb_[0]), *qa1 = opq(stage + kb_[1]);
#pragma unroll
    for (int d0 = 0; d0 < 8; ++d0) c.q[d0] = ldsr128(((d0 & 1) ? qa1 : qa0) + 256 * (d0 >> 1));
  };
  asm volatile("s_nop 4" ::: "memory");
  if (grp) { ldtile(0, 0); ldq2(0, 0); ldq2(0, 1); ldq2(0, 2); ldq2(0, 3); ldtile(1, TILEB); ldtile(2, 2 * TILEB); wait_bar<16>(); wait_vm<8>(); rdq(); bar_only(); }
  else     { ldq2(0, 0); ldq2(0, 1); ldq2(0, 2); ldq2(0, 3); ldtile(0, 0); wait_vm<4>(); rdq(); bar_only(); }
  unsigned sl0 = 0, sl1 = TILEB, sl2 = 2 * TILEB;
  float lprev = 0.f;
#define TS(i)
  for (int h = 0; h < HPW; ++h) {
#pragma unroll
    for (int j = 0; j < 8; ++j) {
      const int t = 8 * h + j;
      {
        const unsigned ka0 = ldsb + KOFF + sl0 + kb_[0], ka1 = ldsb + KOFF + sl0 + kb_[1], va0 = ldsb + VOFF + sl2 + vb_[0], va1 = ldsb + VOFF + sl2 + vb_[1];
        if (j == 0) { if (h == 0) seg_m<true, false, false>(c, ka0, ka1, va0, va1); else seg_m<true, true, false>(c, ka0, ka1, va0, va1); }
        else if (j == 1) seg_m<true, true, true>(c, ka0, ka1, va0, va1);
        else seg_m<true, true, false>(c, ka0, ka1, va0, va1);
      }
      TS(0);
      if (grp) {
        constexpr int dummy = 0; (void)dummy;
        if ((j == 1 || j == 2) && h == 0) { if (j == 1) wait_bar<4 + exops(7)>(); else wait_bar<4 + exops(1)>(); }
        else switch (j) { case 0: wait_bar<exops(-2) + 4 + exops(-1)>(); break; case 1: wait_bar<exops(-1) + 4 + exops(0)>(); break; case 2: wait_bar<exops(0) + 4 + exops(1)>(); break;
          case 3: wait_bar<exops(1) + 4 + exops(2)>(); break; case 4: wait_bar<exops(2) + 4 + exops(3)>(); break; case 5: wait_bar<exops(3) + 4 + exops(4)>(); break;
          case 6: wait_bar<exops(4) + 4 + exops(5)>(); break; default: wait_bar<exops(5) + 4 + exops(6)>(); break; }
      } else bar_only();
      TS(1);
      if (grp) ldtile(t + 3, sl0); else ldtile(t + 1, sl1);
      if (j >= 1 && j <= 4) ldq2(h + 1, j - 1);
      if (j == 0) {
        lprev = c.l;
        softmax<true>(c);
        if (h > 0) epilogue(c, lprev, stage, lane, so, (blk + NWG * (h - 1)) * QHB + 8192 * w);
      } else softmax<false>(c);
      if (j == 7) { wait_vm<12>(); rdq(); }
      TS(2);
      if (!grp) {
        if ((j == 0 || j == 1) && h == 0) { if (j == 0) wait_bar<4 + exops(7)>(); else wait_bar<4 + exops(1)>(); }
        else switch (j) { case 0: wait_bar<exops(-1) + 4 + exops(0)>(); break; case 1: wait_bar<exops(0) + 4 + exops(1)>(); break; case 2: wait_bar<exops(1) + 4 + exops(2)>(); break;
          case 3: wait_bar<exops(2) + 4 + exops(3)>(); break; case 4: wait_bar<exops(3) + 4 + exops(4)>(); break; case 5: wait_bar<exops(4) + 4 + exops(5)>(); break;
          case 6: wait_bar<exops(5) + 4 + exops(6)>(); break; default: wait_bar<exops(6) + 4 + exops(7)>(); break; }
      } else if (!(j == 7 && h == HPW - 1)) bar_only();
      TS(3);
      { const unsigned x = sl0; sl0 = sl1; sl1 = sl2; sl2 = x; }
    }
  }
  {
    const unsigned va0 = ldsb + VOFF + sl2 + vb_[0], va1 = ldsb + VOFF + sl2 + vb_[1];
    seg_m<false, true, false>(c, 0, 0, va0, va1);
    epilogue(c, c.l, stage, lane, so, (blk + NWG * (HPW - 1)) * QHB + 8192 * w);
  }
}

extern "C" void kernel_launch(void* const* d_in, const int* in_sizes, int n_in, void* d_out, int out_size, void* d_ws, size_t ws_size, hipStream_t stream) {
  hipLaunchKernelGGL(attn_fwd, dim3(NWG), dim3(512), 0, stream, (const char*)d_in[0], (const char*)d_in[1], (const char*)d_in[2], (char*)d_out
  );
}
```

```cpp
#include <hip/hip_runtime.h>
#include <stdint.h>

__device__ __forceinline__ float dot128(const uint4 (&q)[16], const uint4* __restrict__ kr) {
  float s = 0.f;
#pragma unroll
  for (int c = 0; c < 16; ++c) {
    const uint4 kk = kr[c]; const unsigned kw[4] = {kk.x, kk.y, kk.z, kk.w}, qw[4] = {q[c].x, q[c].y, q[c].z, q[c].w};
#pragma unroll
    for (int i = 0; i < 4; ++i) {
      s = __builtin_fmaf(__uint_as_float(qw[i] << 16), __uint_as_float(kw[i] << 16), s);
      s = __builtin_fmaf(__uint_as_float(qw[i] & 0xffff0000u), __uint_as_float(kw[i] & 0xffff0000u), s);
    }
  }
  return s;
}
__device__ __forceinline__ void slow_rows(const char* __restrict__ qrow, const char* __restrict__ kh, const char* __restrict__ vh, char* __restrict__ orow) {
  constexpr float CS = 0.088388347648318440f * 1.4426950408889634f;
  uint4 q[16];
#pragma unroll
  for (int c = 0; c < 16; ++c) q[c] = ((const uint4*)qrow)[c];
  float m = -__builtin_inff();
#pragma unroll 1
  for (int j = 0; j < 512; ++j) { const float s = dot128(q, (const uint4*)(kh + j * 256)); m = (s > m || s != s) ? s : m; }
#pragma unroll 1
  for (int half = 0; half < 2; ++half) {
    float o[64], l = 0.f;
#pragma unroll
    for (int d = 0; d < 64; ++d) o[d] = 0.f;
#pragma unroll 1
    for (int j = 0; j < 512; ++j) {
      const float p = __builtin_amdgcn_exp2f((dot128(q, (const uint4*)(kh + j * 256)) - m) * CS);
      l += p;
      const uint4* vr = (const uint4*)(vh + j * 256 + half * 128);
#pragma unroll
      for (int c = 0; c < 8; ++c) {
        const uint4 vv = vr[c]; const unsigned vw[4] = {vv.x, vv.y, vv.z, vv.w};
#pragma unroll
        for (int i = 0; i < 4; ++i) {
          o[8 * c + 2 * i] = __builtin_fmaf(p, __uint_as_float(vw[i] << 16), o[8 * c + 2 * i]);
          o[8 * c + 2 * i + 1] = __builtin_fmaf(p, __uint_as_float(vw[i] & 0xffff0000u), o[8 * c + 2 * i + 1]);
        }
      }
    }
    const float il = 1.0f / l;
    typedef float f32x2 __attribute__((ext_vector_type(2))); typedef __bf16 bf16x2 __attribute__((ext_vector_type(2)));
#pragma unroll
    for (int c = 0; c < 8; ++c) {
      unsigned w[4];
#pragma unroll
      for (int i = 0; i < 4; ++i) { f32x2 x = {o[8 * c + 2 * i] * il, o[8 * c + 2 * i + 1] * il}; w[i] = __builtin_bit_cast(unsigned, __builtin_convertvector(x, bf16x2)); }
      ((uint4*)(orow + half * 128))[c] = make_uint4(w[0], w[1], w[2], w[3]);
    }
  }
}

__global__ __launch_bounds__(256, 1) void attn_fwd(const char* __restrict__ Q, const char* __restrict__ K, const char* __restrict__ V, char* __restrict__ O, char* __restrict__ W) {
  __shared__ __attribute__((aligned(1024))) char lds[163840];
  const unsigned ldsb = (unsigned)(size_t)(__attribute__((address_space(3))) char*)lds;
  const unsigned bx = blockIdx.x, hb = bx;
  unsigned flag, tid = threadIdx.x;
  asm volatile(
    "s_memrealtime s[82:83]\n"
    "s_load_dwordx2 s[48:49], %2, 0\n"
    "s_load_dwordx2 s[40:41], %2, 8\n"
    "s_load_dwordx2 s[44:45], %2, 16\n"
    "s_load_dwordx2 s[52:53], %2, 24\n"
    "v_readfirstlane_b32 s39, v0\n"
    "s_lshr_b32 s39, s39, 6\n"
    "v_mbcnt_lo_u32_b32 v253, -1, 0\n"
    "v_mbcnt_hi_u32_b32 v253, -1, v253\n"
    "s_getpc_b64 s[74:75]\n"
    "s_mul_i32 s37, s39, 9216\n"
    "v_lshl_add_u32 v64, v253, 4, s37\n"
    "global_load_dwordx4 v[68:71], v64, s[74:75] offset:0\n"
    "global_load_dwordx4 v[72:75], v64, s[74:75] offset:1024\n"
    "global_load_dwordx4 v[76:79], v64, s[74:75] offset:2048\n"
    "global_load_dwordx4 v[80:83], v64, s[74:75] offset:3072\n"
    "v_add_u32 v64, 0x1000, v64\n"
    "global_load_dwordx4 v[84:87], v64, s[74:75] offset:0\n"
    "global_load_dwordx4 v[88:91], v64, s[74:75] offset:1024\n"
    "global_load_dwordx4 v[92:95], v64, s[74:75] offset:2048\n"
    "global_load_dwordx4 v[96:99], v64, s[74:75] offset:3072\n"
    "v_add_u32 v64, 0x1000, v64\n"
    "global_load_dwordx4 v[100:103], v64, s[74:75] offset:0\n"
    "s_mov_b32 s42, 134217728\n"
    "s_mov_b32 s43, 0x20000\n"
    "s_mov_b32 s46, 134217728\n"
    "s_mov_b32 s47, 0x20000\n"
    "s_mov_b32 s50, 67108864\n"
    "s_mov_b32 s51, 0x20000\n"
    "s_mov_b32 s54, 0\n"
    "s_mov_b32 s55, 0x20000\n"
    "v_bfe_u32 v224, v253, 2, 2\n"
    "v_lshlrev_b32 v224, 8, v224\n"
    "v_lshrrev_b32 v225, 4, v253\n"
    "v_lshl_add_u32 v224, v225, 6, v224\n"
    "v_and_b32 v225, 3, v253\n"
    "v_lshl_add_u32 v237, v225, 4, v224\n"
    "v_xor_b32 v238, 32, v237\n"
    "v_xor_b32 v239, 48, v237\n"
    "v_xor_b32 v240, 16, v237\n"
    "s_lshl_b32 s37, s39, 12\n"
    "s_add_u32 s70, s37, %4\n"
    "s_add_u32 s71, s70, 49152\n"
    "s_lshl_b32 s56, %3, 17\n"
    "s_add_u32 s56, s56, s37\n"
    "s_mov_b32 s57, s56\n"
    "s_mov_b32 s58, 0\n"
    "s_mov_b32 s59, 0\n"
    "s_mov_b32 s36, 0\n"
    "s_lshl_b32 s37, s39, 14\n"
    "s_add_u32 s62, s37, 98304\n"
    "s_add_u32 s62, s62, %4\n"
    "s_lshl_b32 s63, %3, 16\n"
    "s_add_u32 s63, s63, s37\n"
    "s_mov_b32 s64, s63\n"
    "s_waitcnt lgkmcnt(0)\n"
    "s_add_u32 s73, s82, 677\n"
    "s_mov_b32 s88, 185\n"
    "s_and_b32 s49, s49, 0xffff\n"
    "s_and_b32 s41, s41, 0xffff\n"
    "s_and_b32 s45, s45, 0xffff\n"
    "s_and_b32 s53, s53, 0xffff\n"
    "s_add_u32 m0, s62, 0\n"
    "s_nop 0\n"
    "s_add_u32 s37, s63, 0\n"
    "buffer_load_dwordx4 v237, s[48:51], s37 offen offset:0 nt lds\n"
    "buffer_load_dwordx4 v238, s[48:51], s37 offen offset:1024 nt lds\n"
    "buffer_load_dwordx4 v239, s[48:51], s37 offen offset:2048 nt lds\n"
    "buffer_load_dwordx4 v240, s[48:51], s37 offen offset:3072 nt lds\n"
    "s_add_u32 m0, s62, 4096\n"
    "s_nop 0\n"
    "s_add_u32 s37, s63, 4096\n"
    "buffer_load_dwordx4 v237, s[48:51], s37 offen offset:0 nt lds\n"
    "buffer_load_dwordx4 v238, s[48:51], s37 offen offset:1024 nt lds\n"
    "buffer_load_dwordx4 v239, s[48:51], s37 offen offset:2048 nt lds\n"
    "buffer_load_dwordx4 v240, s[48:51], s37 offen offset:3072 nt lds\n"
    "s_add_u32 m0, s62, 8192\n"
    "s_nop 0\n"
    "s_add_u32 s37, s63, 8192\n"
    "buffer_load_dwordx4 v237, s[48:51], s37 offen offset:0 nt lds\n"
    "buffer_load_dwordx4 v238, s[48:51], s37 offen offset:1024 nt lds\n"
    "buffer_load_dwordx4 v239, s[48:51], s37 offen offset:2048 nt lds\n"
    "buffer_load_dwordx4 v240, s[48:51], s37 offen offset:3072 nt lds\n"
    "s_add_u32 m0, s62, 12288\n"
    "s_nop 0\n"
    "s_add_u32 s37, s63, 12288\n"
    "buffer_load_dwordx4 v237, s[48:51], s37 offen offset:0 nt lds\n"
    "buffer_load_dwordx4 v238, s[48:51], s37 offen offset:1024 nt lds\n"
    "buffer_load_dwordx4 v239, s[48:51], s37 offen offset:2048 nt lds\n"
    "buffer_load_dwordx4 v240, s[48:51], s37 offen offset:3072 nt lds\n"
    "s_add_u32 m0, s70, s58\n"
    "s_nop 0\n"
    "buffer_load_dwordx4 v237, s[40:43], s56 offen offset:0 nt lds\n"
    "buffer_load_dwordx4 v238, s[40:43], s56 offen offset:1024 nt lds\n"
    "buffer_load_dwordx4 v239, s[40:43], s56 offen offset:2048 nt lds\n"
    "buffer_load_dwordx4 v240, s[40:43], s56 offen offset:3072 nt lds\n"
    "s_add_u32 s56, s56, 16384\n"
    "s_add_u32 s58, s58, 16384\n"
    "s_cmp_eq_u32 s58, 49152\n"
    "s_cselect_b32 s58, 0, s58\n"
    "s_add_u32 s63, s63, 16777216\n"
    "s_mov_b32 s67, 0x53800000\n"
    "s_mov_b32 s68, 0\n"
    "s_mov_b32 s69, 1\n"
    "s_mov_b32 s60, 0\n"
    "s_mov_b32 s72, 0\n"
    "v_bfe_u32 v0, v253, 2, 2\n"
    "v_lshrrev_b32 v2, 1, v0\n"
    "v_xor_b32 v1, v0, v2\n"
    "v_and_b32 v1, 1, v1\n"
    "v_lshl_add_u32 v1, v1, 1, v2\n"
    "v_lshrrev_b32 v2, 4, v253\n"
    "v_xor_b32 v1, v1, v2\n"
    "v_lshlrev_b32 v1, 4, v1\n"
    "v_lshl_add_u32 v1, v0, 10, v1\n"
    "v_and_b32 v3, 3, v253\n"
    "v_lshl_add_u32 v1, v3, 6, v1\n"
    "v_add_u32 v241, s62, v1\n"
    "v_add_u32 v235, %4, v1\n"
    "v_mov_b32 v232, v235\n"
    "v_lshrrev_b32 v4, 1, v2\n"
    "v_xor_b32 v1, v2, v4\n"
    "v_and_b32 v1, 1, v1\n"
    "v_lshl_add_u32 v1, v1, 1, v4\n"
    "v_lshrrev_b32 v4, 1, v3\n"
    "v_xor_b32 v1, v1, v4\n"
    "v_lshlrev_b32 v1, 4, v1\n"
    "v_lshl_add_u32 v1, v2, 10, v1\n"
    "v_lshl_add_u32 v1, v0, 6, v1\n"
    "v_and_b32 v4, 1, v3\n"
    "v_lshl_add_u32 v1, v4, 3, v1\n"
    "s_add_u32 s37, %4, 49152\n"
    "v_add_u32 v236, s37, v1\n"
    "v_mov_b32 v233, v236\n"
    "v_xor_b32 v234, 32, v236\n"
    "v_and_b32 v0, 15, v253\n"
    "v_and_b32 v1, 7, v253\n"
    "v_lshrrev_b32 v4, 1, v2\n"
    "v_xor_b32 v1, v1, v4\n"
    "v_lshlrev_b32 v1, 4, v1\n"
    "v_lshl_add_u32 v1, v0, 8, v1\n"
    "v_and_b32 v4, 1, v2\n"
    "v_lshl_add_u32 v1, v4, 3, v1\n"
    "v_add_u32 v242, s62, v1\n"
    "v_xor_b32 v250, 32, v242\n"
    "v_xor_b32 v251, 64, v242\n"
    "v_xor_b32 v252, 96, v242\n"
    "v_xor_b32 v1, v0, v2\n"
    "v_lshlrev_b32 v1, 4, v1\n"
    "v_lshl_add_u32 v1, v2, 8, v1\n"
    "v_add_u32 v243, s62, v1\n"
    "v_xor_b32 v244, 64, v243\n"
    "v_lshlrev_b32 v245, 4, v253\n"
    "v_mov_b32 v1, 0x3f803f80\n"
    "v_cmp_eq_u32 vcc, 0, v3\n"
    "s_nop 1\n"
    "v_cndmask_b32 v1, 0, v1, vcc\n"
    "v_accvgpr_write_b32 a144, v1\n"
    "v_accvgpr_write_b32 a145, v1\n"
    "v_accvgpr_write_b32 a146, v1\n"
    "v_accvgpr_write_b32 a147, v1\n"
    "v_lshrrev_b32 v1, 2, v0\n"
    "v_cmp_eq_u32 vcc, v1, v2\n"
    "v_and_b32 v4, 1, v3\n"
    "v_lshlrev_b32 v4, 4, v4\n"
    "v_mov_b32 v5, 0x3e03\n"
    "v_mov_b32 v6, 0xb9d8\n"
    "v_lshlrev_b32 v5, v4, v5\n"
    "v_lshlrev_b32 v6, v4, v6\n"
    "v_cndmask_b32 v5, 0, v5, vcc\n"
    "v_cndmask_b32 v6, 0, v6, vcc\n"
    "v_cmp_gt_u32 vcc, 2, v3\n"
    "s_nop 1\n"
    "v_cndmask_b32 v7, 0, v5, vcc\n"
    "v_cndmask_b32 v8, v5, 0, vcc\n"
    "v_cndmask_b32 v9, 0, v6, vcc\n"
    "v_cndmask_b32 v10, v6, 0, vcc\n"
    "v_mov_b32 v192, v7\n"
    "v_mov_b32 v193, v8\n"
    "v_mov_b32 v194, v9\n"
    "v_mov_b32 v195, v10\n"
    "v_lshrrev_b32 v1, 3, v0\n"
    "v_and_b32 v5, 1, v2\n"
    "v_cmp_eq_u32 vcc, v1, v5\n"
    "v_mov_b32 v5, 0x3e03\n"
    "v_mov_b32 v6, 0xb9d8\n"
    "v_cmp_gt_u32 s[74:75], 2, v2\n"
    "s_nop 1\n"
    "v_cndmask_b32 v5, v6, v5, s[74:75]\n"
    "v_lshlrev_b32 v5, v4, v5\n"
    "v_cndmask_b32 v5, 0, v5, vcc\n"
    "v_bfe_u32 v1, v0, 1, 2\n"
    "v_cmp_eq_u32 vcc, 0, v1\n"
    "s_nop 1\n"
    "v_cndmask_b32 v6, 0, v5, vcc\n"
    "v_accvgpr_write_b32 a148, v6\n"
    "v_cmp_eq_u32 vcc, 1, v1\n"
    "s_nop 1\n"
    "v_cndmask_b32 v6, 0, v5, vcc\n"
    "v_accvgpr_write_b32 a149, v6\n"
    "v_cmp_eq_u32 vcc, 2, v1\n"
    "s_nop 1\n"
    "v_cndmask_b32 v6, 0, v5, vcc\n"
    "v_accvgpr_write_b32 a150, v6\n"
    "v_cmp_eq_u32 vcc, 3, v1\n"
    "s_nop 1\n"
    "v_cndmask_b32 v6, 0, v5, vcc\n"
    "v_accvgpr_write_b32 a151, v6\n"
    "v_and_b32 v253, 32, v253\n"
    "v_xor_b32 v253, v253, v241\n"
    "s_waitcnt vmcnt(16)\n"
    "ds_read_b64 v[0:1], v241 offset:0\n"
    "ds_read_b64 v[2:3], v241 offset:0\n"
    "ds_read_b64 v[4:5], v241 offset:8\n"
    "ds_read_b64 v[6:7], v241 offset:8\n"
    "ds_read_b64 v[8:9], v241 offset:256\n"
    "ds_read_b64 v[10:11], v241 offset:256\n"
    "ds_read_b64 v[12:13], v241 offset:264\n"
    "ds_read_b64 v[14:15], v241 offset:264\n"
    "ds_read_b64 v[16:17], v241 offset:512\n"
    "ds_read_b64 v[18:19], v241 offset:512\n"
    "ds_read_b64 v[20:21], v241 offset:520\n"
    "ds_read_b64 v[22:23], v241 offset:520\n"
    "ds_read_b64 v[24:25], v241 offset:768\n"
    "ds_read_b64 v[26:27], v241 offset:768\n"
    "ds_read_b64 v[28:29], v241 offset:776\n"
    "ds_read_b64 v[30:31], v241 offset:776\n"
    "s_waitcnt lgkmcnt(14)\n"
    "v_mfma_f32_16x16x32_bf16 v[64:67], v[192:195], v[0:3], 0\n"
    "s_add_u32 m0, s70, s58\n"
    "s_nop 0\n"
    "buffer_load_dwordx4 v237, s[40:43], s56 offen offset:0 nt lds\n"
    "buffer_load_dwordx4 v238, s[40:43], s56 offen offset:1024 nt lds\n"
    "buffer_load_dwordx4 v239, s[40:43], s56 offen offset:2048 nt lds\n"
    "buffer_load_dwordx4 v240, s[40:43], s56 offen offset:3072 nt lds\n"
    "s_add_u32 s56, s56, 16384\n"
    "s_add_u32 s58, s58, 16384\n"
    "s_cmp_eq_u32 s58, 49152\n"
    "s_cselect_b32 s58, 0, s58\n"
    "s_waitcnt vmcnt(16)\n"
    "ds_read_b64 v[32:33], v241 offset:4096\n"
    "ds_read_b64 v[34:35], v241 offset:4096\n"
    "s_waitcnt lgkmcnt(14)\n"
    "v_mfma_f32_16x16x32_bf16 v[68:71], v[192:195], v[4:7], 0\n"
    "ds_read_b64 v[36:37], v241 offset:4104\n"
    "ds_read_b64 v[38:39], v241 offset:4104\n"
    "s_waitcnt lgkmcnt(14)\n"
    "v_mfma_f32_16x16x32_bf16 v[72:75], v[192:195], v[8:11], 0\n"
    "ds_read_b64 v[40:41], v241 offset:4352\n"
    "ds_read_b64 v[42:43], v241 offset:4352\n"
    "s_waitcnt lgkmcnt(14)\n"
    "v_mfma_f32_16x16x32_bf16 v[76:79], v[192:195], v[12:15], 0\n"
    "ds_read_b64 v[44:45], v241 offset:4360\n"
    "ds_read_b64 v[46:47], v241 offset:4360\n"
    "s_waitcnt lgkmcnt(14)\n"
    "v_mfma_f32_16x16x32_bf16 v[80:83], v[192:195], v[16:19], 0\n"
    "v_cvt_pk_bf16_f32 v64, v64, v65\n"
    "v_cvt_pk_bf16_f32 v65, v66, v67\n"
    "v_accvgpr_write_b32 a0, v64\n"
    "v_accvgpr_write_b32 a1, v65\n"
    "ds_read_b64 v[48:49], v241 offset:4608\n"
    "ds_read_b64 v[50:51], v241 offset:4608\n"
    "s_waitcnt lgkmcnt(14)\n"
    "v_mfma_f32_16x16x32_bf16 v[84:87], v[192:195], v[20:23], 0\n"
    "v_cvt_pk_bf16_f32 v68, v68, v69\n"
    "v_cvt_pk_bf16_f32 v69, v70, v71\n"
    "v_accvgpr_write_b32 a2, v68\n"
    "v_accvgpr_write_b32 a3, v69\n"
    "ds_read_b64 v[52:53], v241 offset:4616\n"
    "ds_read_b64 v[54:55], v241 offset:4616\n"
    "s_waitcnt lgkmcnt(14)\n"
    "v_mfma_f32_16x16x32_bf16 v[88:91], v[192:195], v[24:27], 0\n"
    "v_cvt_pk_bf16_f32 v72, v72, v73\n"
    "v_cvt_pk_bf16_f32 v73, v74, v75\n"
    "v_accvgpr_write_b32 a4, v72\n"
    "v_accvgpr_write_b32 a5, v73\n"
    "ds_read_b64 v[56:57], v241 offset:4864\n"
    "ds_read_b64 v[58:59], v241 offset:4864\n"
    "s_waitcnt lgkmcnt(14)\n"
    "v_mfma_f32_16x16x32_bf16 v[92:95], v[192:195], v[28:31], 0\n"
    "v_cvt_pk_bf16_f32 v76, v76, v77\n"
    "v_cvt_pk_bf16_f32 v77, v78, v79\n"
    "v_accvgpr_write_b32 a6, v76\n"
    "v_accvgpr_write_b32 a7, v77\n"
    "ds_read_b64 v[60:61], v241 offset:4872\n"
    "ds_read_b64 v[62:63], v241 offset:4872\n"
    "s_waitcnt lgkmcnt(14)\n"
    "v_mfma_f32_16x16x32_bf16 v[96:99], v[192:195], v[32:35], 0\n"
    "v_cvt_pk_bf16_f32 v80, v80, v81\n"
    "v_cvt_pk_bf16_f32 v81, v82, v83\n"
    "v_accvgpr_write_b32 a8, v80\n"
    "v_accvgpr_write_b32 a9, v81\n"
    "s_add_u32 m0, s71, s59\n"
    "s_nop 0\n"
    "buffer_load_dwordx4 v237, s[44:47], s57 offen offset:0 lds\n"
    "buffer_load_dwordx4 v238, s[44:47], s57 offen offset:1024 lds\n"
    "buffer_load_dwordx4 v239, s[44:47], s57 offen offset:2048 lds\n"
    "buffer_load_dwordx4 v240, s[44:47], s57 offen offset:3072 lds\n"
    "s_add_u32 s57, s57, 16384\n"
    "s_add_u32 s59, s59, 16384\n"
    "s_cmp_eq_u32 s59, 49152\n"
    "s_cselect_b32 s59, 0, s59\n"
    "s_waitcnt vmcnt(16)\n"
    "ds_read_b64 v[128:129], v241 offset:8192\n"
    "ds_read_b64 v[130:131], v241 offset:8192\n"
    "s_waitcnt lgkmcnt(14)\n"
    "v_mfma_f32_16x16x32_bf16 v[100:103], v[192:195], v[36:39], 0\n"
    "v_cvt_pk_bf16_f32 v84, v84, v85\n"
    "v_cvt_pk_bf16_f32 v85, v86, v87\n"
    "v_accvgpr_write_b32 a10, v84\n"
    "v_accvgpr_write_b32 a11, v85\n"
    "ds_read_b64 v[132:133], v241 offset:8200\n"
    "ds_read_b64 v[134:135], v241 offset:8200\n"
    "s_waitcnt lgkmcnt(14)\n"
    "v_mfma_f32_16x16x32_bf16 v[104:107], v[192:195], v[40:43], 0\n"
    "v_cvt_pk_bf16_f32 v88, v88, v89\n"
    "v_cvt_pk_bf16_f32 v89, v90, v91\n"
    "v_accvgpr_write_b32 a12, v88\n"
    "v_accvgpr_write_b32 a13, v89\n"
    "ds_read_b64 v[136:137], v241 offset:8448\n"
    "ds_read_b64 v[138:139], v241 offset:8448\n"
    "s_waitcnt lgkmcnt(14)\n"
    "v_mfma_f32_16x16x32_bf16 v[108:111], v[192:195], v[44:47], 0\n"
    "v_cvt_pk_bf16_f32 v92, v92, v93\n"
    "v_cvt_pk_bf16_f32 v93, v94, v95\n"
    "v_accvgpr_write_b32 a14, v92\n"
    "v_accvgpr_write_b32 a15, v93\n"
    "ds_read_b64 v[140:141], v241 offset:8456\n"
    "ds_read_b64 v[142:143], v241 offset:8456\n"
    "s_waitcnt lgkmcnt(14)\n"
    "v_mfma_f32_16x16x32_bf16 v[112:115], v[192:195], v[48:51], 0\n"
    "v_cvt_pk_bf16_f32 v96, v96, v97\n"
    "v_cvt_pk_bf16_f32 v97, v98, v99\n"
    "v_accvgpr_write_b32 a16, v96\n"
    "v_accvgpr_write_b32 a17, v97\n"
    "ds_read_b64 v[144:145], v241 offset:8704\n"
    "ds_read_b64 v[146:147], v241 offset:8704\n"
    "s_waitcnt lgkmcnt(14)\n"
    "v_mfma_f32_16x16x32_bf16 v[116:119], v[192:195], v[52:55], 0\n"
    "v_cvt_pk_bf16_f32 v100, v100, v101\n"
    "v_cvt_pk_bf16_f32 v101, v102, v103\n"
    "v_accvgpr_write_b32 a18, v100\n"
    "v_accvgpr_write_b32 a19, v101\n"
    "ds_read_b64 v[148:149], v241 offset:8712\n"
    "ds_read_b64 v[150:151], v241 offset:8712\n"
    "s_waitcnt lgkmcnt(14)\n"
    "v_mfma_f32_16x16x32_bf16 v[120:123], v[192:195], v[56:59], 0\n"
    "v_cvt_pk_bf16_f32 v104, v104, v105\n"
    "v_cvt_pk_bf16_f32 v105, v106, v107\n"
    "v_accvgpr_write_b32 a20, v104\n"
    "v_accvgpr_write_b32 a21, v105\n"
    "ds_read_b64 v[152:153], v241 offset:8960\n"
    "ds_read_b64 v[154:155], v241 offset:8960\n"
    "s_waitcnt lgkmcnt(14)\n"
    "v_mfma_f32_16x16x32_bf16 v[124:127], v[192:195], v[60:63], 0\n"
    "v_cvt_pk_bf16_f32 v108, v108, v109\n"
    "v_cvt_pk_bf16_f32 v109, v110, v111\n"
    "v_accvgpr_write_b32 a22, v108\n"
    "v_accvgpr_write_b32 a23, v109\n"
    "ds_read_b64 v[156:157], v241 offset:8968\n"
    "ds_read_b64 v[158:159], v241 offset:8968\n"
    "s_waitcnt lgkmcnt(14)\n"
    "v_mfma_f32_16x16x32_bf16 v[0:3], v[192:195], v[128:131], 0\n"
    "v_cvt_pk_bf16_f32 v112, v112, v113\n"
    "v_cvt_pk_bf16_f32 v113, v114, v115\n"
    "v_accvgpr_write_b32 a24, v112\n"
    "v_accvgpr_write_b32 a25, v113\n"
    "s_waitcnt vmcnt(12)\n"
    "ds_read_b64 v[160:161], v241 offset:12288\n"
    "ds_read_b64 v[162:163], v241 offset:12288\n"
    "s_waitcnt lgkmcnt(14)\n"
    "v_mfma_f32_16x16x32_bf16 v[4:7], v[192:195], v[132:135], 0\n"
    "v_cvt_pk_bf16_f32 v116, v116, v117\n"
    "v_cvt_pk_bf16_f32 v117, v118, v119\n"
    "v_accvgpr_write_b32 a26, v116\n"
    "v_accvgpr_write_b32 a27, v117\n"
    "ds_read_b64 v[164:165], v241 offset:12296\n"
    "ds_read_b64 v[166:167], v241 offset:12296\n"
    "s_waitcnt lgkmcnt(14)\n"
    "v_mfma_f32_16x16x32_bf16 v[8:11], v[192:195], v[136:139], 0\n"
    "v_cvt_pk_bf16_f32 v120, v120, v121\n"
    "v_cvt_pk_bf16_f32 v121, v122, v123\n"
    "v_accvgpr_write_b32 a28, v120\n"
    "v_accvgpr_write_b32 a29, v121\n"
    "ds_read_b64 v[168:169], v241 offset:12544\n"
    "ds_read_b64 v[170:171], v241 offset:12544\n"
    "s_waitcnt lgkmcnt(14)\n"
    "v_mfma_f32_16x16x32_bf16 v[12:15], v[192:195], v[140:143], 0\n"
    "v_cvt_pk_bf16_f32 v124, v124, v125\n"
    "v_cvt_pk_bf16_f32 v125, v126, v127\n"
    "v_accvgpr_write_b32 a30, v124\n"
    "v_accvgpr_write_b32 a31, v125\n"
    "ds_read_b64 v[172:173], v241 offset:12552\n"
    "ds_read_b64 v[174:175], v241 offset:12552\n"
    "s_waitcnt lgkmcnt(14)\n"
    "v_mfma_f32_16x16x32_bf16 v[16:19], v[192:195], v[144:147], 0\n"
    "v_cvt_pk_bf16_f32 v0, v0, v1\n"
    "v_cvt_pk_bf16_f32 v1, v2, v3\n"
    "v_accvgpr_write_b32 a32, v0\n"
    "v_accvgpr_write_b32 a33, v1\n"
    "ds_read_b64 v[176:177], v241 offset:12800\n"
    "ds_read_b64 v[178:179], v241 offset:12800\n"
    "s_waitcnt lgkmcnt(14)\n"
    "v_mfma_f32_16x16x32_bf16 v[20:23], v[192:195], v[148:151], 0\n"
    "v_cvt_pk_bf16_f32 v4, v4, v5\n"
    "v_cvt_pk_bf16_f32 v5, v6, v7\n"
    "v_accvgpr_write_b32 a34, v4\n"
    "v_accvgpr_write_b32 a35, v5\n"
    "ds_read_b64 v[180:181], v241 offset:12808\n"
    "ds_read_b64 v[182:183], v241 offset:12808\n"
    "s_waitcnt lgkmcnt(14)\n"
    "v_mfma_f32_16x16x32_bf16 v[24:27], v[192:195], v[152:155], 0\n"
    "v_cvt_pk_bf16_f32 v8, v8, v9\n"
    "v_cvt_pk_bf16_f32 v9, v10, v11\n"
    "v_accvgpr_write_b32 a36, v8\n"
    "v_accvgpr_write_b32 a37, v9\n"
    "ds_read_b64 v[184:185], v241 offset:13056\n"
    "ds_read_b64 v[186:187], v241 offset:13056\n"
    "s_waitcnt lgkmcnt(14)\n"
    "v_mfma_f32_16x16x32_bf16 v[28:31], v[192:195], v[156:159], 0\n"
    "v_cvt_pk_bf16_f32 v12, v12, v13\n"
    "v_cvt_pk_bf16_f32 v13, v14, v15\n"
    "v_accvgpr_write_b32 a38, v12\n"
    "v_accvgpr_write_b32 a39, v13\n"
    "ds_read_b64 v[188:189], v241 offset:13064\n"
    "ds_read_b64 v[190:191], v241 offset:13064\n"
    "s_waitcnt lgkmcnt(14)\n"
    "v_mfma_f32_16x16x32_bf16 v[32:35], v[192:195], v[160:163], 0\n"
    "v_cvt_pk_bf16_f32 v16, v16, v17\n"
    "v_cvt_pk_bf16_f32 v17, v18, v19\n"
    "v_accvgpr_write_b32 a40, v16\n"
    "v_accvgpr_write_b32 a41, v17\n"
    "s_waitcnt vmcnt(8)\n"
    "s_barrier\n"
    "ds_read_b128 a[64:67], v232 offset:0\n"
    "ds_read_b128 a[68:71], v232 offset:256\n"
    "ds_read_b128 a[72:75], v232 offset:512\n"
    "ds_read_b128 a[76:79], v232 offset:768\n"
    "s_waitcnt lgkmcnt(15)\n"
    "v_mfma_f32_16x16x32_bf16 v[36:39], v[192:195], v[164:167], 0\n"
    "v_cvt_pk_bf16_f32 v20, v20, v21\n"
    "v_cvt_pk_bf16_f32 v21, v22, v23\n"
    "v_accvgpr_write_b32 a42, v20\n"
    "v_accvgpr_write_b32 a43, v21\n"
    "s_waitcnt lgkmcnt(14)\n"
    "v_mfma_f32_16x16x32_bf16 v[40:43], v[192:195], v[168:171], 0\n"
    "v_cvt_pk_bf16_f32 v24, v24, v25\n"
    "v_cvt_pk_bf16_f32 v25, v26, v27\n"
    "v_accvgpr_write_b32 a44, v24\n"
    "v_accvgpr_write_b32 a45, v25\n"
    "s_waitcnt lgkmcnt(12)\n"
    "v_mfma_f32_16x16x32_bf16 v[44:47], v[192:195], v[172:175], 0\n"
    "v_cvt_pk_bf16_f32 v28, v28, v29\n"
    "v_cvt_pk_bf16_f32 v29, v30, v31\n"
    "v_accvgpr_write_b32 a46, v28\n"
    "v_accvgpr_write_b32 a47, v29\n"
    "s_waitcnt lgkmcnt(10)\n"
    "v_mfma_f32_16x16x32_bf16 v[48:51], v[192:195], v[176:179], 0\n"
    "v_cvt_pk_bf16_f32 v32, v32, v33\n"
    "v_cvt_pk_bf16_f32 v33, v34, v35\n"
    "v_accvgpr_write_b32 a48, v32\n"
    "v_accvgpr_write_b32 a49, v33\n"
    "s_waitcnt lgkmcnt(8)\n"
    "v_mfma_f32_16x16x32_bf16 v[52:55], v[192:195], v[180:183], 0\n"
    "v_cvt_pk_bf16_f32 v36, v36, v37\n"
    "v_cvt_pk_bf16_f32 v37, v38, v39\n"
    "v_accvgpr_write_b32 a50, v36\n"
    "v_accvgpr_write_b32 a51, v37\n"
    "s_waitcnt lgkmcnt(6)\n"
    "v_mfma_f32_16x16x32_bf16 v[56:59], v[192:195], v[184:187], 0\n"
    "v_cvt_pk_bf16_f32 v40, v40, v41\n"
    "v_cvt_pk_bf16_f32 v41, v42, v43\n"
    "v_accvgpr_write_b32 a52, v40\n"
    "v_accvgpr_write_b32 a53, v41\n"
    "s_waitcnt lgkmcnt(4)\n"
    "v_mfma_f32_16x16x32_bf16 v[60:63], v[192:195], v[188:191], 0\n"
    "v_cvt_pk_bf16_f32 v44, v44, v45\n"
    "v_cvt_pk_bf16_f32 v45, v46, v47\n"
    "v_accvgpr_write_b32 a54, v44\n"
    "v_accvgpr_write_b32 a55, v45\n"
    "v_cvt_pk_bf16_f32 v48, v48, v49\n"
    "v_cvt_pk_bf16_f32 v49, v50, v51\n"
    "v_accvgpr_write_b32 a56, v48\n"
    "v_accvgpr_write_b32 a57, v49\n"
    "v_cvt_pk_bf16_f32 v52, v52, v53\n"
    "v_cvt_pk_bf16_f32 v53, v54, v55\n"
    "v_accvgpr_write_b32 a58, v52\n"
    "v_accvgpr_write_b32 a59, v53\n"
    "v_cvt_pk_bf16_f32 v56, v56, v57\n"
    "v_cvt_pk_bf16_f32 v57, v58, v59\n"
    "v_accvgpr_write_b32 a60, v56\n"
    "v_accvgpr_write_b32 a61, v57\n"
    "v_cvt_pk_bf16_f32 v60, v60, v61\n"
    "v_cvt_pk_bf16_f32 v61, v62, v63\n"
    "v_accvgpr_write_b32 a62, v60\n"
    "v_accvgpr_write_b32 a63, v61\n"
    "s_nop 1\n"
    "s_sleep 4\n"
    "s_waitcnt lgkmcnt(0)\n"
    "v_mfma_f32_16x16x32_bf16 v[128:131], a[64:67], a[0:3], 0\n"
    "ds_read_b128 a[80:83], v232 offset:4096\n"
    "v_mfma_f32_16x16x32_bf16 v[128:131], a[68:71], a[4:7], v[128:131]\n"
    "ds_read_b128 a[84:87], v232 offset:4352\n"
    "s_add_u32 m0, s70, s58\n"
    "s_nop 0\n"
    "buffer_load_dwordx4 v237, s[40:43], s56 offen offset:0 nt lds\n"
    "v_mfma_f32_16x16x32_bf16 v[128:131], a[72:75], a[8:11], v[128:131]\n"
    "ds_read_b128 a[88:91], v232 offset:4608\n"
    "v_mfma_f32_16x16x32_bf16 v[128:131], a[76:79], a[12:15], v[128:131]\n"
    "ds_read_b128 a[92:95], v232 offset:4864\n"
    "v_mfma_f32_16x16x32_bf16 v[132:135], a[76:79], a[28:31], 0\n"
    "v_mfma_f32_16x16x32_bf16 v[132:135], a[72:75], a[24:27], v[132:135]\n"
    "buffer_load_dwordx4 v238, s[40:43], s56 offen offset:1024 nt lds\n"
    "v_mfma_f32_16x16x32_bf16 v[132:135], a[68:71], a[20:23], v[132:135]\n"
    "v_mfma_f32_16x16x32_bf16 v[132:135], a[64:67], a[16:19], v[132:135]\n"
    "s_sleep 4\n"
    "v_mfma_f32_16x16x32_bf16 v[136:139], a[64:67], a[32:35], 0\n"
    "v_mfma_f32_16x16x32_bf16 v[136:139], a[68:71], a[36:39], v[136:139]\n"
    "buffer_load_dwordx4 v239, s[40:43], s56 offen offset:2048 nt lds\n"
    "v_mfma_f32_16x16x32_bf16 v[136:139], a[72:75], a[40:43], v[136:139]\n"
    "v_mfma_f32_16x16x32_bf16 v[136:139], a[76:79], a[44:47], v[136:139]\n"
    "v_mfma_f32_16x16x32_bf16 v[140:143], a[76:79], a[60:63], 0\n"
    "v_mfma_f32_16x16x32_bf16 v[140:143], a[72:75], a[56:59], v[140:143]\n"
    "buffer_load_dwordx4 v240, s[40:43], s56 offen offset:3072 nt lds\n"
    "v_mfma_f32_16x16x32_bf16 v[140:143], a[68:71], a[52:55], v[140:143]\n"
    "v_mfma_f32_16x16x32_bf16 v[140:143], a[64:67], a[48:51], v[140:143]\n"
    "v_max3_f32 v112, v128, v129, v130\n"
    "v_max3_f32 v116, v132, v133, v134\n"
    "s_nop 0\n"
    "v_max3_f32 v120, v136, v137, v138\n"
    "s_nop 3\n"
    "v_max3_f32 v124, v140, v141, v142\n"
    "v_max_f32 v112, v112, v131\n"
    "v_max_f32 v116, v116, v135\n"
    "v_max_f32 v120, v120, v139\n"
    "v_max_f32 v124, v124, v143\n"
    "v_permlane32_swap_b32 v112, v116\n"
    "s_nop 0\n"
    "v_permlane32_swap_b32 v120, v124\n"
    "v_max_f32 v112, v112, v116\n"
    "v_max_f32 v120, v120, v124\n"
    "s_nop 1\n"
    "v_permlane16_swap_b32 v112, v120\n"
    "v_min_f32 v112, -v112, -v120\n"
    "v_mov_b32 v120, v112\n"
    "s_nop 1\n"
    "v_permlane16_swap_b32 v112, v120\n"
    "v_mov_b32 v116, v112\n"
    "v_mov_b32 v124, v120\n"
    "s_nop 0\n"
    "v_permlane32_swap_b32 v112, v116\n"
    "v_permlane32_swap_b32 v120, v124\n"
    "v_mov_b32 v125, v124\n"
    "v_mov_b32 v126, v124\n"
    "v_mov_b32 v127, v124\n"
    "v_mov_b32 v121, v120\n"
    "v_mov_b32 v122, v120\n"
    "v_mov_b32 v123, v120\n"
    "v_mov_b32 v117, v116\n"
    "v_mov_b32 v118, v116\n"
    "v_mov_b32 v119, v116\n"
    "v_mov_b32 v113, v112\n"
    "v_mov_b32 v114, v112\n"
    "v_mov_b32 v115, v112\n"
    "s_sleep 4\n"
    "s_waitcnt lgkmcnt(0)\n"
    "v_mfma_f32_16x16x32_bf16 v[156:159], a[80:83], a[48:51], v[124:127]\n"
    "ds_read_b128 a[64:67], v232 offset:8192\n"
    "v_add_f32 v128, v128, v112\n"
    "v_add_f32 v129, v129, v112\n"
    "v_add_f32 v130, v130, v112\n"
    "v_add_f32 v131, v131, v112\n"
    "v_mfma_f32_16x16x32_bf16 v[156:159], a[84:87], a[52:55], v[156:159]\n"
    "s_add_u32 s56, s56, 16384\n"
    "s_add_u32 s58, s58, 16384\n"
    "s_cmp_eq_u32 s58, 49152\n"
    "s_cselect_b32 s58, 0, s58\n"
    "s_add_u32 m0, s71, s59\n"
    "v_exp_f32 v128, v128\n"
    "v_exp_f32 v129, v129\n"
    "v_mfma_f32_16x16x32_bf16 v[156:159], a[88:91], a[56:59], v[156:159]\n"
    "v_exp_f32 v130, v130\n"
    "v_exp_f32 v131, v131\n"
    "v_mfma_f32_16x16x32_bf16 v[156:159], a[92:95], a[60:63], v[156:159]\n"
    "ds_read_b128 a[68:71], v232 offset:8448\n"
    "v_cvt_pk_bf16_f32 v160, v128, v129\n"
    "v_cvt_pk_bf16_f32 v161, v130, v131\n"
    "v_add_f32 v132, v132, v116\n"
    "v_mfma_f32_16x16x32_bf16 v[152:155], a[92:95], a[44:47], v[120:123]\n"
    "ds_read_b128 a[72:75], v232 offset:8704\n"
    "v_add_f32 v133, v133, v116\n"
    "v_add_f32 v134, v134, v116\n"
    "v_add_f32 v135, v135, v116\n"
    "v_mfma_f32_16x16x32_bf16 v[152:155], a[88:91], a[40:43], v[152:155]\n"
    "ds_read_b128 a[76:79], v232 offset:8960\n"
    "v_exp_f32 v132, v132\n"
    "v_exp_f32 v133, v133\n"
    "v_mfma_f32_16x16x32_bf16 v[152:155], a[84:87], a[36:39], v[152:155]\n"
    "v_exp_f32 v134, v134\n"
    "v_exp_f32 v135, v135\n"
    "v_mfma_f32_16x16x32_bf16 v[152:155], a[80:83], a[32:35], v[152:155]\n"
    "v_cvt_pk_bf16_f32 v164, v132, v133\n"
    "v_cvt_pk_bf16_f32 v165, v134, v135\n"
    "v_add_f32 v136, v136, v120\n"
    "v_add_f32 v137, v137, v120\n"
    "s_sleep 4\n"
    "v_mfma_f32_16x16x32_bf16 v[148:151], a[80:83], a[16:19], v[116:119]\n"
    "v_add_f32 v138, v138, v120\n"
    "v_add_f32 v139, v139, v120\n"
    "v_exp_f32 v136, v136\n"
    "v_mfma_f32_16x16x32_bf16 v[148:151], a[84:87], a[20:23], v[148:151]\n"
    "v_exp_f32 v137, v137\n"
    "v_exp_f32 v138, v138\n"
    "v_mfma_f32_16x16x32_bf16 v[148:151], a[88:91], a[24:27], v[148:151]\n"
    "v_exp_f32 v139, v139\n"
    "v_cvt_pk_bf16_f32 v168, v136, v137\n"
    "v_cvt_pk_bf16_f32 v169, v138, v139\n"
    "v_add_f32 v140, v140, v124\n"
    "v_mfma_f32_16x16x32_bf16 v[148:151], a[92:95], a[28:31], v[148:151]\n"
    "v_add_f32 v141, v141, v124\n"
    "v_add_f32 v142, v142, v124\n"
    "v_add_f32 v143, v143, v124\n"
    "v_mfma_f32_16x16x32_bf16 v[144:147], a[92:95], a[12:15], v[112:115]\n"
    "v_exp_f32 v140, v140\n"
    "v_exp_f32 v141, v141\n"
    "v_mfma_f32_16x16x32_bf16 v[144:147], a[88:91], a[8:11], v[144:147]\n"
    "v_exp_f32 v142, v142\n"
    "v_exp_f32 v143, v143\n"
    "v_cvt_pk_bf16_f32 v172, v140, v141\n"
    "v_mfma_f32_16x16x32_bf16 v[144:147], a[84:87], a[4:7], v[144:147]\n"
    "v_cvt_pk_bf16_f32 v173, v142, v143\n"
    "v_exp_f32 v156, v156\n"
    "v_mfma_f32_16x16x32_bf16 v[144:147], a[80:83], a[0:3], v[144:147]\n"
    "v_exp_f32 v157, v157\n"
    "v_exp_f32 v158, v158\n"
    "s_sleep 4\n"
    "s_waitcnt lgkmcnt(0)\n"
    "v_mfma_f32_16x16x32_bf16 v[128:131], a[64:67], a[0:3], v[112:115]\n"
    "ds_read_b128 a[80:83], v232 offset:12288\n"
    "v_exp_f32 v159, v159\n"
    "v_cvt_pk_bf16_f32 v174, v156, v157\n"
    "v_cvt_pk_bf16_f32 v175, v158, v159\n"
    "v_mfma_f32_16x16x32_bf16 v[128:131], a[68:71], a[4:7], v[128:131]\n"
    "v_exp_f32 v152, v152\n"
    "v_exp_f32 v153, v153\n"
    "v_mfma_f32_16x16x32_bf16 v[128:131], a[72:75], a[8:11], v[128:131]\n"
    "v_exp_f32 v154, v154\n"
    "v_exp_f32 v155, v155\n"
    "v_mfma_f32_16x16x32_bf16 v[128:131], a[76:79], a[12:15], v[128:131]\n"
    "ds_read_b128 a[84:87], v232 offset:12544\n"
    "v_cvt_pk_bf16_f32 v170, v152, v153\n"
    "v_cvt_pk_bf16_f32 v171, v154, v155\n"
    "v_mfma_f32_16x16x32_bf16 v[132:135], a[76:79], a[28:31], v[116:119]\n"
    "ds_read_b128 a[88:91], v232 offset:12800\n"
    "v_exp_f32 v148, v148\n"
    "v_exp_f32 v149, v149\n"
    "v_mfma_f32_16x16x32_bf16 v[132:135], a[72:75], a[24:27], v[132:135]\n"
    "ds_read_b128 a[92:95], v232 offset:13056\n"
    "v_exp_f32 v150, v150\n"
    "v_mfma_f32_16x16x32_bf16 v[132:135], a[68:71], a[20:23], v[132:135]\n"
    "v_exp_f32 v151, v151\n"
    "v_cvt_pk_bf16_f32 v166, v148, v149\n"
    "v_cvt_pk_bf16_f32 v167, v150, v151\n"
    "v_mfma_f32_16x16x32_bf16 v[132:135], a[64:67], a[16:19], v[132:135]\n"
    "v_exp_f32 v144, v144\n"
    "v_exp_f32 v145, v145\n"
    "s_sleep 4\n"
    "v_mfma_f32_16x16x32_bf16 v[136:139], a[64:67], a[32:35], v[120:123]\n"
    "v_exp_f32 v146, v146\n"
    "v_exp_f32 v147, v147\n"
    "v_cvt_pk_bf16_f32 v162, v144, v145\n"
    "v_mfma_f32_16x16x32_bf16 v[136:139], a[68:71], a[36:39], v[136:139]\n"
    "v_cvt_pk_bf16_f32 v163, v146, v147\n"
    "v_exp_f32 v128, v128\n"
    "v_mfma_f32_16x16x32_bf16 v[136:139], a[72:75], a[40:43], v[136:139]\n"
    "buffer_load_dwordx4 v237, s[44:47], s57 offen offset:0 lds\n"
    "v_exp_f32 v129, v129\n"
    "v_mfma_f32_16x16x32_bf16 v[136:139], a[76:79], a[44:47], v[136:139]\n"
    "buffer_load_dwordx4 v238, s[44:47], s57 offen offset:1024 lds\n"
    "v_exp_f32 v130, v130\n"
    "v_mfma_f32_16x16x32_bf16 v[140:143], a[76:79], a[60:63], v[124:127]\n"
    "buffer_load_dwordx4 v239, s[44:47], s57 offen offset:2048 lds\n"
    "v_exp_f32 v131, v131\n"
    "v_cvt_pk_bf16_f32 v176, v128, v129\n"
    "v_mfma_f32_16x16x32_bf16 v[140:143], a[72:75], a[56:59], v[140:143]\n"
    "buffer_load_dwordx4 v240, s[44:47], s57 offen offset:3072 lds\n"
    "s_add_u32 s57, s57, 16384\n"
    "s_add_u32 s59, s59, 16384\n"
    "s_cmp_eq_u32 s59, 49152\n"
    "s_cselect_b32 s59, 0, s59\n"
    "v_cvt_pk_bf16_f32 v177, v130, v131\n"
    "v_mfma_f32_16x16x32_bf16 v[140:143], a[68:71], a[52:55], v[140:143]\n"
    "v_exp_f32 v132, v132\n"
    "v_exp_f32 v133, v133\n"
    "v_mfma_f32_16x16x32_bf16 v[140:143], a[64:67], a[48:51], v[140:143]\n"
    "v_exp_f32 v134, v134\n"
    "v_exp_f32 v135, v135\n"
    "v_cvt_pk_bf16_f32 v180, v132, v133\n"
    "v_add_u32 v233, s60, v236\n"
    "s_add_u32 s60, s60, 16384\n"
    "s_cmp_eq_u32 s60, 49152\n"
    "s_cselect_b32 s60, 0, s60\n"
    "v_xor_b32 v234, 32, v233\n"
    "v_add_u32 v232, s60, v235\n"
    "s_waitcnt vmcnt(8) lgkmcnt(0)\n"
    "s_barrier\n"
    "s_sleep 4\n"
    "v_mfma_f32_16x16x32_bf16 v[156:159], a[80:83], a[48:51], v[124:127]\n"
    "ds_read_b128 a[64:67], v232 offset:0\n"
    "ds_read_b128 a[68:71], v232 offset:256\n"
    "ds_read_b128 a[72:75], v232 offset:512\n"
    "ds_read_b128 a[76:79], v232 offset:768\n"
    "v_mfma_f32_16x16x32_bf16 v[156:159], a[84:87], a[52:55], v[156:159]\n"
    "ds_read_b64_tr_b16 a[96:97], v233 offset:0\n"
    "ds_read_b64_tr_b16 a[98:99], v233 offset:4096\n"
    "s_add_u32 m0, s70, s58\n"
    "v_cvt_pk_bf16_f32 v181, v134, v135\n"
    "v_exp_f32 v136, v136\n"
    "v_mfma_f32_16x16x32_bf16 v[156:159], a[88:91], a[56:59], v[156:159]\n"
    "ds_read_b64_tr_b16 a[100:101], v233 offset:8192\n"
    "v_exp_f32 v137, v137\n"
    "v_mfma_f32_16x16x32_bf16 v[156:159], a[92:95], a[60:63], v[156:159]\n"
    "ds_read_b64_tr_b16 a[102:103], v233 offset:12288\n"
    "v_exp_f32 v138, v138\n"
    "v_exp_f32 v139, v139\n"
    "v_mfma_f32_16x16x32_bf16 v[152:155], a[92:95], a[44:47], v[120:123]\n"
    "ds_read_b64_tr_b16 a[104:105], v234 offset:0\n"
    "v_cvt_pk_bf16_f32 v184, v136, v137\n"
    "v_cvt_pk_bf16_f32 v185, v138, v139\n"
    "v_exp_f32 v140, v140\n"
    "v_mfma_f32_16x16x32_bf16 v[152:155], a[88:91], a[40:43], v[152:155]\n"
    "ds_read_b64_tr_b16 a[106:107], v234 offset:4096\n"
    "v_exp_f32 v141, v141\n"
    "v_mfma_f32_16x16x32_bf16 v[152:155], a[84:87], a[36:39], v[152:155]\n"
    "ds_read_b64_tr_b16 a[108:109], v234 offset:8192\n"
    "v_exp_f32 v142, v142\n"
    "v_exp_f32 v143, v143\n"
    "v_mfma_f32_16x16x32_bf16 v[152:155], a[80:83], a[32:35], v[152:155]\n"
    "ds_read_b64_tr_b16 a[110:111], v234 offset:12288\n"
    "v_cvt_pk_bf16_f32 v188, v140, v141\n"
    "v_cvt_pk_bf16_f32 v189, v142, v143\n"
    "v_exp_f32 v156, v156\n"
    "s_sleep 4\n"
    "v_mfma_f32_16x16x32_bf16 v[148:151], a[80:83], a[16:19], v[116:119]\n"
    "v_exp_f32 v157, v157\n"
    "v_exp_f32 v158, v158\n"
    "v_mfma_f32_16x16x32_bf16 v[148:151], a[84:87], a[20:23], v[148:151]\n"
    "v_exp_f32 v159, v159\n"
    "v_cvt_pk_bf16_f32 v190, v156, v157\n"
    "v_cvt_pk_bf16_f32 v191, v158, v159\n"
    "v_mfma_f32_16x16x32_bf16 v[148:151], a[88:91], a[24:27], v[148:151]\n"
    "buffer_load_dwordx4 v237, s[40:43], s56 offen offset:0 nt lds\n"
    "buffer_load_dwordx4 v238, s[40:43], s56 offen offset:1024 nt lds\n"
    "v_mfma_f32_16x16x32_bf16 v[148:151], a[92:95], a[28:31], v[148:151]\n"
    "v_exp_f32 v152, v152\n"
    "v_exp_f32 v153, v153\n"
    "v_mfma_f32_16x16x32_bf16 v[144:147], a[92:95], a[12:15], v[112:115]\n"
    "v_exp_f32 v154, v154\n"
    "v_exp_f32 v155, v155\n"
    "v_cvt_pk_bf16_f32 v186, v152, v153\n"
    "v_mfma_f32_16x16x32_bf16 v[144:147], a[88:91], a[8:11], v[144:147]\n"
    "v_cvt_pk_bf16_f32 v187, v154, v155\n"
    "buffer_load_dwordx4 v239, s[40:43], s56 offen offset:2048 nt lds\n"
    "v_mfma_f32_16x16x32_bf16 v[144:147], a[84:87], a[4:7], v[144:147]\n"
    "v_mfma_f32_16x16x32_bf16 v[144:147], a[80:83], a[0:3], v[144:147]\n"
    "v_exp_f32 v148, v148\n"
    "v_exp_f32 v149, v149\n"
    "v_exp_f32 v150, v150\n"
    "v_exp_f32 v151, v151\n"
    "v_cvt_pk_bf16_f32 v182, v148, v149\n"
    "v_cvt_pk_bf16_f32 v183, v150, v151\n"
    "buffer_load_dwordx4 v240, s[40:43], s56 offen offset:3072 nt lds\n"
    "s_add_u32 s56, s56, 16384\n"
    "s_add_u32 s58, s58, 16384\n"
    "s_cmp_eq_u32 s58, 49152\n"
    "s_cselect_b32 s58, 0, s58\n"
    ".p2align 6\n"
    ".Lloop:\n"
    "s_waitcnt lgkmcnt(0)\n"
    "v_mfma_f32_16x16x32_bf16 v[128:131], a[64:67], a[0:3], v[112:115]\n"
    "ds_read_b128 a[80:83], v232 offset:4096\n"
    "v_xor_b32 v242, 0x80, v242\n"
    "v_mfma_f32_16x16x32_bf16 v[128:131], a[68:71], a[4:7], v[128:131]\n"
    "ds_read_b128 a[84:87], v232 offset:4352\n"
    "s_add_u32 m0, s71, s59\n"
    "v_exp_f32 v144, v144\n"
    "v_mfma_f32_16x16x32_bf16 v[128:131], a[72:75], a[8:11], v[128:131]\n"
    "ds_read_b128 a[88:91], v232 offset:4608\n"
    "v_exp_f32 v145, v145\n"
    "v_mfma_f32_16x16x32_bf16 v[128:131], a[76:79], a[12:15], v[128:131]\n"
    "ds_read_b128 a[92:95], v232 offset:4864\n"
    "v_xor_b32 v250, 0x80, v250\n"
    "v_mfma_f32_16x16x32_bf16 v[132:135], a[76:79], a[28:31], v[116:119]\n"
    "ds_read_b64_tr_b16 a[112:113], v233 offset:256\n"
    "v_exp_f32 v146, v146\n"
    "v_mfma_f32_16x16x32_bf16 v[132:135], a[72:75], a[24:27], v[132:135]\n"
    "ds_read_b64_tr_b16 a[114:115], v233 offset:4352\n"
    "v_exp_f32 v147, v147\n"
    "v_mfma_f32_16x16x32_bf16 v[132:135], a[68:71], a[20:23], v[132:135]\n"
    "ds_read_b64_tr_b16 a[116:117], v233 offset:8448\n"
    "v_cvt_pk_bf16_f32 v178, v144, v145\n"
    "v_cvt_pk_bf16_f32 v179, v146, v147\n"
    "v_mfma_f32_16x16x32_bf16 v[132:135], a[64:67], a[16:19], v[132:135]\n"
    "ds_read_b64_tr_b16 a[118:119], v233 offset:12544\n"
    "v_xor_b32 v251, 0x80, v251\n"
    "v_mfma_f32_16x16x32_bf16 v[136:139], a[64:67], a[32:35], v[120:123]\n"
    "ds_read_b64_tr_b16 a[120:121], v234 offset:256\n"
    "v_exp_f32 v128, v128\n"
    "v_mfma_f32_16x16x32_bf16 v[136:139], a[68:71], a[36:39], v[136:139]\n"
    "ds_read_b64_tr_b16 a[122:123], v234 offset:4352\n"
    "v_exp_f32 v129, v129\n"
    "v_mfma_f32_16x16x32_bf16 v[136:139], a[72:75], a[40:43], v[136:139]\n"
    "ds_read_b64_tr_b16 a[124:125], v234 offset:8448\n"
    "v_exp_f32 v130, v130\n"
    "v_mfma_f32_16x16x32_bf16 v[136:139], a[76:79], a[44:47], v[136:139]\n"
    "ds_read_b64_tr_b16 a[126:127], v234 offset:12544\n"
    "v_xor_b32 v252, 0x80, v252\n"
    "v_mfma_f32_16x16x32_bf16 v[140:143], a[76:79], a[60:63], v[124:127]\n"
    "v_exp_f32 v131, v131\n"
    "v_cvt_pk_bf16_f32 v192, v128, v129\n"
    "v_mfma_f32_16x16x32_bf16 v[140:143], a[72:75], a[56:59], v[140:143]\n"
    "v_mul_f32 v64, v246, v64\n"
    "v_mul_f32 v65, v246, v65\n"
    "v_cvt_pk_bf16_f32 v193, v130, v131\n"
    "v_mfma_f32_16x16x32_bf16 v[140:143], a[68:71], a[52:55], v[140:143]\n"
    "v_mul_f32 v66, v246, v66\n"
    "v_mul_f32 v67, v246, v67\n"
    "v_cvt_pk_bf16_f32 v64, v64, v65\n"
    "v_mfma_f32_16x16x32_bf16 v[140:143], a[64:67], a[48:51], v[140:143]\n"
    "v_cvt_pk_bf16_f32 v65, v66, v67\n"
    "v_mul_f32 v68, v247, v68\n"
    "v_mfma_f32_16x16x32_bf16 a[128:131], a[144:147], v[160:163], 0\n"
    "v_mul_f32 v69, v247, v69\n"
    "buffer_load_dwordx4 v237, s[44:47], s57 offen offset:0 lds\n"
    "v_mfma_f32_16x16x32_bf16 a[128:131], a[144:147], v[176:179], a[128:131]\n"
    "v_mul_f32 v70, v247, v70\n"
    "v_mul_f32 v71, v247, v71\n"
    "v_cvt_pk_bf16_f32 v68, v68, v69\n"
    "v_mfma_f32_16x16x32_bf16 a[132:135], a[144:147], v[164:167], 0\n"
    "v_cvt_pk_bf16_f32 v69, v70, v71\n"
    "buffer_load_dwordx4 v238, s[44:47], s57 offen offset:1024 lds\n"
    "v_mfma_f32_16x16x32_bf16 a[132:135], a[144:147], v[180:183], a[132:135]\n"
    "ds_write2st64_b64 v242, v[64:65], v[68:69] offset0:0 offset1:8\n"
    "v_mfma_f32_16x16x32_bf16 a[136:139], a[144:147], v[168:171], 0\n"
    "v_mul_f32 v72, v248, v72\n"
    "v_mul_f32 v73, v248, v73\n"
    "v_mul_f32 v74, v248, v74\n"
    "v_mfma_f32_16x16x32_bf16 a[136:139], a[144:147], v[184:187], a[136:139]\n"
    "v_mul_f32 v75, v248, v75\n"
    "buffer_load_dwordx4 v239, s[44:47], s57 offen offset:2048 lds\n"
    "v_mfma_f32_16x16x32_bf16 a[140:143], a[144:147], v[172:175], 0\n"
    "v_cvt_pk_bf16_f32 v72, v72, v73\n"
    "v_cvt_pk_bf16_f32 v73, v74, v75\n"
    "v_mfma_f32_16x16x32_bf16 a[140:143], a[144:147], v[188:191], a[140:143]\n"
    "v_mul_f32 v76, v249, v76\n"
    "v_mul_f32 v77, v249, v77\n"
    "v_mul_f32 v78, v249, v78\n"
    "v_mfma_f32_16x16x32_bf16 v[0:3], a[96:99], v[160:163], 0\n"
    "v_mul_f32 v79, v249, v79\n"
    "buffer_load_dwordx4 v240, s[44:47], s57 offen offset:3072 lds\n"
    "v_mfma_f32_16x16x32_bf16 v[0:3], a[100:103], v[176:179], v[0:3]\n"
    "s_add_u32 s57, s57, 16384\n"
    "s_add_u32 s59, s59, 16384\n"
    "s_cmp_eq_u32 s59, 49152\n"
    "s_cselect_b32 s59, 0, s59\n"
    "v_cvt_pk_bf16_f32 v76, v76, v77\n"
    "v_cvt_pk_bf16_f32 v77, v78, v79\n"
    "v_mfma_f32_16x16x32_bf16 v[4:7], a[100:103], v[180:183], 0\n"
    "ds_write2st64_b64 v242, v[72:73], v[76:77] offset0:16 offset1:24\n"
    "v_mul_f32 v92, v249, v92\n"
    "v_mfma_f32_16x16x32_bf16 v[4:7], a[96:99], v[164:167], v[4:7]\n"
    "v_mul_f32 v93, v249, v93\n"
    "v_exp_f32 v132, v132\n"
    "v_mfma_f32_16x16x32_bf16 v[8:11], a[96:99], v[168:171], 0\n"
    "v_mul_f32 v94, v249, v94\n"
    "v_mul_f32 v95, v249, v95\n"
    "v_cvt_pk_bf16_f32 v92, v92, v93\n"
    "v_mfma_f32_16x16x32_bf16 v[8:11], a[100:103], v[184:187], v[8:11]\n"
    "v_cvt_pk_bf16_f32 v93, v94, v95\n"
    "v_mul_f32 v88, v248, v88\n"
    "v_mfma_f32_16x16x32_bf16 v[12:15], a[100:103], v[188:191], 0\n"
    "v_mul_f32 v89, v248, v89\n"
    "v_exp_f32 v133, v133\n"
    "v_mfma_f32_16x16x32_bf16 v[12:15], a[96:99], v[172:175], v[12:15]\n"
    "v_mul_f32 v90, v248, v90\n"
    "v_mul_f32 v91, v248, v91\n"
    "v_cvt_pk_bf16_f32 v88, v88, v89\n"
    "v_mfma_f32_16x16x32_bf16 v[28:31], a[104:107], v[172:175], 0\n"
    "v_cvt_pk_bf16_f32 v89, v90, v91\n"
    "v_exp_f32 v134, v134\n"
    "v_mfma_f32_16x16x32_bf16 v[28:31], a[108:111], v[188:191], v[28:31]\n"
    "ds_write2st64_b64 v250, v[92:93], v[88:89] offset0:24 offset1:16\n"
    "v_mfma_f32_16x16x32_bf16 v[24:27], a[108:111], v[184:187], 0\n"
    "v_mul_f32 v84, v247, v84\n"
    "v_mul_f32 v85, v247, v85\n"
    "v_mul_f32 v86, v247, v86\n"
    "v_mfma_f32_16x16x32_bf16 v[24:27], a[104:107], v[168:171], v[24:27]\n"
    "v_mul_f32 v87, v247, v87\n"
    "v_exp_f32 v135, v135\n"
    "v_mfma_f32_16x16x32_bf16 v[20:23], a[104:107], v[164:167], 0\n"
    "v_cvt_pk_bf16_f32 v84, v84, v85\n"
    "v_cvt_pk_bf16_f32 v85, v86, v87\n"
    "v_cvt_pk_bf16_f32 v196, v132, v133\n"
    "v_mfma_f32_16x16x32_bf16 v[20:23], a[108:111], v[180:183], v[20:23]\n"
    "v_mul_f32 v80, v246, v80\n"
    "v_mul_f32 v81, v246, v81\n"
    "v_mfma_f32_16x16x32_bf16 v[16:19], a[108:111], v[176:179], 0\n"
    "v_mul_f32 v82, v246, v82\n"
    "v_mul_f32 v83, v246, v83\n"
    "v_cvt_pk_bf16_f32 v197, v134, v135\n"
    "v_mfma_f32_16x16x32_bf16 v[16:19], a[104:107], v[160:163], v[16:19]\n"
    "v_cvt_pk_bf16_f32 v80, v80, v81\n"
    "v_cvt_pk_bf16_f32 v81, v82, v83\n"
    "s_waitcnt lgkmcnt(11)\n"
    "v_mfma_f32_16x16x32_bf16 v[156:159], a[80:83], a[48:51], v[124:127]\n"
    "ds_write2st64_b64 v250, v[84:85], v[80:81] offset0:8 offset1:0\n"
    "v_mul_f32 v96, v246, v96\n"
    "v_mfma_f32_16x16x32_bf16 v[156:159], a[84:87], a[52:55], v[156:159]\n"
    "ds_read_b128 a[64:67], v232 offset:8192\n"
    "v_mul_f32 v97, v246, v97\n"
    "v_mul_f32 v98, v246, v98\n"
    "v_mfma_f32_16x16x32_bf16 v[156:159], a[88:91], a[56:59], v[156:159]\n"
    "ds_read_b128 a[68:71], v232 offset:8448\n"
    "v_mul_f32 v99, v246, v99\n"
    "v_cvt_pk_bf16_f32 v96, v96, v97\n"
    "v_mfma_f32_16x16x32_bf16 v[156:159], a[92:95], a[60:63], v[156:159]\n"
    "ds_read_b128 a[72:75], v232 offset:8704\n"
    "v_cvt_pk_bf16_f32 v97, v98, v99\n"
    "v_mfma_f32_16x16x32_bf16 v[152:155], a[92:95], a[44:47], v[120:123]\n"
    "ds_read_b128 a[76:79], v232 offset:8960\n"
    "v_mul_f32 v100, v247, v100\n"
    "v_mul_f32 v101, v247, v101\n"
    "v_mfma_f32_16x16x32_bf16 v[152:155], a[88:91], a[40:43], v[152:155]\n"
    "ds_read_b64_tr_b16 a[96:97], v233 offset:512\n"
    "v_mul_f32 v102, v247, v102\n"
    "v_mul_f32 v103, v247, v103\n"
    "v_mfma_f32_16x16x32_bf16 v[152:155], a[84:87], a[36:39], v[152:155]\n"
    "ds_read_b64_tr_b16 a[98:99], v233 offset:4608\n"
    "v_cvt_pk_bf16_f32 v100, v100, v101\n"
    "v_cvt_pk_bf16_f32 v101, v102, v103\n"
    "v_mfma_f32_16x16x32_bf16 v[152:155], a[80:83], a[32:35], v[152:155]\n"
    "ds_write2st64_b64 v251, v[96:97], v[100:101] offset0:0 offset1:8\n"
    "v_mfma_f32_16x16x32_bf16 v[148:151], a[80:83], a[16:19], v[116:119]\n"
    "ds_read_b64_tr_b16 a[100:101], v233 offset:8704\n"
    "v_mul_f32 v104, v248, v104\n"
    "v_mul_f32 v105, v248, v105\n"
    "v_mfma_f32_16x16x32_bf16 v[148:151], a[84:87], a[20:23], v[148:151]\n"
    "ds_read_b64_tr_b16 a[102:103], v233 offset:12800\n"
    "v_mul_f32 v106, v248, v106\n"
    "v_mul_f32 v107, v248, v107\n"
    "v_mfma_f32_16x16x32_bf16 v[148:151], a[88:91], a[24:27], v[148:151]\n"
    "ds_read_b64_tr_b16 a[104:105], v234 offset:512\n"
    "v_cvt_pk_bf16_f32 v104, v104, v105\n"
    "v_cvt_pk_bf16_f32 v105, v106, v107\n"
    "v_mfma_f32_16x16x32_bf16 v[148:151], a[92:95], a[28:31], v[148:151]\n"
    "v_mul_f32 v108, v249, v108\n"
    "v_mul_f32 v109, v249, v109\n"
    "v_mfma_f32_16x16x32_bf16 v[144:147], a[92:95], a[12:15], v[112:115]\n"
    "ds_read_b64_tr_b16 a[106:107], v234 offset:4608\n"
    "v_mul_f32 v110, v249, v110\n"
    "v_mul_f32 v111, v249, v111\n"
    "v_mfma_f32_16x16x32_bf16 v[144:147], a[88:91], a[8:11], v[144:147]\n"
    "ds_read_b64_tr_b16 a[108:109], v234 offset:8704\n"
    "v_cvt_pk_bf16_f32 v108, v108, v109\n"
    "v_cvt_pk_bf16_f32 v109, v110, v111\n"
    "v_mfma_f32_16x16x32_bf16 v[144:147], a[84:87], a[4:7], v[144:147]\n"
    "ds_write2st64_b64 v251, v[104:105], v[108:109] offset0:16 offset1:24\n"
    "v_accvgpr_read_b32 v228, a180\n"
    "v_mfma_f32_16x16x32_bf16 v[144:147], a[80:83], a[0:3], v[144:147]\n"
    "ds_read_b64_tr_b16 a[110:111], v234 offset:12800\n"
    "v_accvgpr_read_b32 v229, a181\n"
    "s_waitcnt lgkmcnt(15)\n"
    "v_mfma_f32_16x16x32_bf16 v[32:35], a[112:115], v[160:163], 0\n"
    "v_accvgpr_read_b32 v230, a182\n"
    "v_accvgpr_read_b32 v231, a183\n"
    "v_mul_f32 v228, v249, v228\n"
    "v_mfma_f32_16x16x32_bf16 v[32:35], a[116:119], v[176:179], v[32:35]\n"
    "v_mul_f32 v229, v249, v229\n"
    "v_exp_f32 v136, v136\n"
    "v_mfma_f32_16x16x32_bf16 v[36:39], a[116:119], v[180:183], 0\n"
    "v_mul_f32 v230, v249, v230\n"
    "v_mul_f32 v231, v249, v231\n"
    "v_cvt_pk_bf16_f32 v228, v228, v229\n"
    "v_mfma_f32_16x16x32_bf16 v[36:39], a[112:115], v[164:167], v[36:39]\n"
    "v_cvt_pk_bf16_f32 v229, v230, v231\n"
    "v_accvgpr_read_b32 v224, a176\n"
    "v_mfma_f32_16x16x32_bf16 v[40:43], a[112:115], v[168:171], 0\n"
    "v_accvgpr_read_b32 v225, a177\n"
    "v_exp_f32 v137, v137\n"
    "v_mfma_f32_16x16x32_bf16 v[40:43], a[116:119], v[184:187], v[40:43]\n"
    "v_accvgpr_read_b32 v226, a178\n"
    "v_accvgpr_read_b32 v227, a179\n"
    "v_mul_f32 v224, v248, v224\n"
    "v_mfma_f32_16x16x32_bf16 v[44:47], a[116:119], v[188:191], 0\n"
    "v_mul_f32 v225, v248, v225\n"
    "v_exp_f32 v138, v138\n"
    "v_mfma_f32_16x16x32_bf16 v[44:47], a[112:115], v[172:175], v[44:47]\n"
    "v_mul_f32 v226, v248, v226\n"
    "v_mul_f32 v227, v248, v227\n"
    "v_mfma_f32_16x16x32_bf16 v[60:63], a[120:123], v[172:175], 0\n"
    "v_cvt_pk_bf16_f32 v224, v224, v225\n"
    "v_cvt_pk_bf16_f32 v225, v226, v227\n"
    "v_mfma_f32_16x16x32_bf16 v[60:63], a[124:127], v[188:191], v[60:63]\n"
    "ds_write2st64_b64 v252, v[228:229], v[224:225] offset0:24 offset1:16\n"
    "v_accvgpr_read_b32 v228, a172\n"
    "v_mfma_f32_16x16x32_bf16 v[56:59], a[124:127], v[184:187], 0\n"
    "v_accvgpr_read_b32 v229, a173\n"
    "v_exp_f32 v139, v139\n"
    "v_mfma_f32_16x16x32_bf16 v[56:59], a[120:123], v[168:171], v[56:59]\n"
    "v_accvgpr_read_b32 v230, a174\n"
    "v_accvgpr_read_b32 v231, a175\n"
    "v_cvt_pk_bf16_f32 v200, v136, v137\n"
    "v_mfma_f32_16x16x32_bf16 v[52:55], a[120:123], v[164:167], 0\n"
    "v_mul_f32 v228, v247, v228\n"
    "v_mul_f32 v229, v247, v229\n"
    "v_cvt_pk_bf16_f32 v201, v138, v139\n"
    "v_mfma_f32_16x16x32_bf16 v[52:55], a[124:127], v[180:183], v[52:55]\n"
    "v_mul_f32 v230, v247, v230\n"
    "v_mul_f32 v231, v247, v231\n"
    "v_mfma_f32_16x16x32_bf16 v[48:51], a[124:127], v[176:179], 0\n"
    "v_cvt_pk_bf16_f32 v228, v228, v229\n"
    "v_cvt_pk_bf16_f32 v229, v230, v231\n"
    "v_accvgpr_read_b32 v224, a168\n"
    "v_mfma_f32_16x16x32_bf16 v[48:51], a[120:123], v[160:163], v[48:51]\n"
    "v_accvgpr_read_b32 v225, a169\n"
    "v_accvgpr_read_b32 v226, a170\n"
    "v_accvgpr_read_b32 v227, a171\n"
    "s_waitcnt lgkmcnt(11)\n"
    "v_mfma_f32_16x16x32_bf16 v[128:131], a[64:67], a[0:3], v[112:115]\n"
    "ds_read_b128 a[80:83], v232 offset:12288\n"
    "v_mul_f32 v224, v246, v224\n"
    "v_mul_f32 v225, v246, v225\n"
    "v_mfma_f32_16x16x32_bf16 v[128:131], a[68:71], a[4:7], v[128:131]\n"
    "ds_read_b128 a[84:87], v232 offset:12544\n"
    "v_mul_f32 v226, v246, v226\n"
    "v_mfma_f32_16x16x32_bf16 v[128:131], a[72:75], a[8:11], v[128:131]\n"
    "v_mul_f32 v227, v246, v227\n"
    "v_exp_f32 v140, v140\n"
    "v_mfma_f32_16x16x32_bf16 v[128:131], a[76:79], a[12:15], v[128:131]\n"
    "ds_read_b128 a[88:91], v232 offset:12800\n"
    "v_cvt_pk_bf16_f32 v224, v224, v225\n"
    "v_cvt_pk_bf16_f32 v225, v226, v227\n"
    "v_mfma_f32_16x16x32_bf16 v[132:135], a[76:79], a[28:31], v[116:119]\n"
    "ds_write2st64_b64 v252, v[228:229], v[224:225] offset0:8 offset1:0\n"
    "v_xor_b32 v242, 0x80, v242\n"
    "v_mfma_f32_16x16x32_bf16 v[132:135], a[72:75], a[24:27], v[132:135]\n"
    "ds_read_b128 a[92:95], v232 offset:13056\n"
    "v_xor_b32 v250, 0x80, v250\n"
    "v_mfma_f32_16x16x32_bf16 v[132:135], a[68:71], a[20:23], v[132:135]\n"
    "ds_read_b64_tr_b16 a[112:113], v233 offset:768\n"
    "v_xor_b32 v251, 0x80, v251\n"
    "v_xor_b32 v252, 0x80, v252\n"
    "v_mfma_f32_16x16x32_bf16 v[132:135], a[64:67], a[16:19], v[132:135]\n"
    "ds_read_b64_tr_b16 a[114:115], v233 offset:4864\n"
    "v_exp_f32 v141, v141\n"
    "v_mfma_f32_16x16x32_bf16 v[136:139], a[64:67], a[32:35], v[120:123]\n"
    "ds_read_b64_tr_b16 a[116:117], v233 offset:8960\n"
    "v_exp_f32 v142, v142\n"
    "v_mfma_f32_16x16x32_bf16 v[136:139], a[68:71], a[36:39], v[136:139]\n"
    "v_exp_f32 v143, v143\n"
    "v_mfma_f32_16x16x32_bf16 v[136:139], a[72:75], a[40:43], v[136:139]\n"
    "ds_read_b128 a[184:187], v243 offset:0\n"
    "v_cvt_pk_bf16_f32 v204, v140, v141\n"
    "v_cvt_pk_bf16_f32 v205, v142, v143\n"
    "v_mfma_f32_16x16x32_bf16 v[136:139], a[76:79], a[44:47], v[136:139]\n"
    "ds_read_b128 a[188:191], v244 offset:1024\n"
    "v_exp_f32 v156, v156\n"
    "v_mfma_f32_16x16x32_bf16 v[140:143], a[76:79], a[60:63], v[124:127]\n"
    "ds_read_b128 a[192:195], v243 offset:2048\n"
    "v_exp_f32 v157, v157\n"
    "v_mfma_f32_16x16x32_bf16 v[140:143], a[72:75], a[56:59], v[140:143]\n"
    "v_exp_f32 v158, v158\n"
    "v_mfma_f32_16x16x32_bf16 v[140:143], a[68:71], a[52:55], v[140:143]\n"
    "v_exp_f32 v159, v159\n"
    "v_cvt_pk_bf16_f32 v206, v156, v157\n"
    "v_mfma_f32_16x16x32_bf16 v[140:143], a[64:67], a[48:51], v[140:143]\n"
    "v_cvt_pk_bf16_f32 v207, v158, v159\n"
    "v_exp_f32 v152, v152\n"
    "s_waitcnt lgkmcnt(12)\n"
    "v_mfma_f32_16x16x32_bf16 v[64:67], a[96:99], v[160:163], 0\n"
    "ds_read_b128 a[196:199], v244 offset:3072\n"
    "v_exp_f32 v153, v153\n"
    "v_mfma_f32_16x16x32_bf16 v[64:67], a[100:103], v[176:179], v[64:67]\n"
    "ds_read_b64_tr_b16 a[118:119], v233 offset:13056\n"
    "v_mfma_f32_16x16x32_bf16 v[68:71], a[100:103], v[180:183], 0\n"
    "ds_read_b64_tr_b16 a[120:121], v234 offset:768\n"
    "v_exp_f32 v154, v154\n"
    "v_mfma_f32_16x16x32_bf16 v[68:71], a[96:99], v[164:167], v[68:71]\n"
    "ds_read_b64_tr_b16 a[122:123], v234 offset:4864\n"
    "v_exp_f32 v155, v155\n"
    "v_mfma_f32_16x16x32_bf16 v[72:75], a[96:99], v[168:171], 0\n"
    "ds_read_b64_tr_b16 a[124:125], v234 offset:8960\n"
    "v_cvt_pk_bf16_f32 v202, v152, v153\n"
    "v_cvt_pk_bf16_f32 v203, v154, v155\n"
    "v_mfma_f32_16x16x32_bf16 v[72:75], a[100:103], v[184:187], v[72:75]\n"
    "ds_read_b64_tr_b16 a[126:127], v234 offset:13056\n"
    "s_memrealtime s[90:91]\n"
    "v_mfma_f32_16x16x32_bf16 v[76:79], a[100:103], v[188:191], 0\n"
    "v_exp_f32 v148, v148\n"
    "v_mfma_f32_16x16x32_bf16 v[76:79], a[96:99], v[172:175], v[76:79]\n"
    "v_exp_f32 v149, v149\n"
    "v_mfma_f32_16x16x32_bf16 v[92:95], a[104:107], v[172:175], 0\n"
    "v_exp_f32 v150, v150\n"
    "v_mfma_f32_16x16x32_bf16 v[92:95], a[108:111], v[188:191], v[92:95]\n"
    "v_exp_f32 v151, v151\n"
    "v_cvt_pk_bf16_f32 v198, v148, v149\n"
    "v_mfma_f32_16x16x32_bf16 v[88:91], a[108:111], v[184:187], 0\n"
    "v_cvt_pk_bf16_f32 v199, v150, v151\n"
    "v_exp_f32 v144, v144\n"
    "v_mfma_f32_16x16x32_bf16 v[88:91], a[104:107], v[168:171], v[88:91]\n"
    "v_exp_f32 v145, v145\n"
    "v_mfma_f32_16x16x32_bf16 v[84:87], a[104:107], v[164:167], 0\n"
    "v_exp_f32 v146, v146\n"
    "v_mfma_f32_16x16x32_bf16 v[84:87], a[108:111], v[180:183], v[84:87]\n"
    "v_exp_f32 v147, v147\n"
    "v_cvt_pk_bf16_f32 v194, v144, v145\n"
    "v_mfma_f32_16x16x32_bf16 v[80:83], a[108:111], v[176:179], 0\n"
    "v_cvt_pk_bf16_f32 v195, v146, v147\n"
    "v_exp_f32 v128, v128\n"
    "v_mfma_f32_16x16x32_bf16 v[80:83], a[104:107], v[160:163], v[80:83]\n"
    "v_exp_f32 v129, v129\n"
    "v_add_u32 v233, s60, v236\n"
    "s_add_u32 s60, s60, 16384\n"
    "s_cmp_eq_u32 s60, 49152\n"
    "s_cselect_b32 s60, 0, s60\n"
    "v_xor_b32 v234, 32, v233\n"
    "v_add_u32 v232, s60, v235\n"
    "s_waitcnt vmcnt(8) lgkmcnt(0)\n"
    "s_sub_u32 s90, s90, s73\n"
    "s_cmp_ge_i32 s90, 0\n"
    "s_cbranch_scc1 .Lpace1x\n"
    ".Lpace1:\n"
    "s_sleep 1\n"
    "s_memrealtime s[90:91]\n"
    "s_waitcnt lgkmcnt(0)\n"
    "s_sub_u32 s90, s90, s73\n"
    "s_cmp_lt_i32 s90, 8\n"
    "s_cbranch_scc1 .Lpace1\n"
    ".Lpace1x:\n"
    "s_add_u32 s73, s73, s88\n"
    "s_barrier\n"
    "v_mfma_f32_16x16x32_bf16 v[156:159], a[80:83], a[48:51], v[124:127]\n"
    "ds_read_b128 a[64:67], v232 offset:0\n"
    "v_exp_f32 v130, v130\n"
    "v_mfma_f32_16x16x32_bf16 v[156:159], a[84:87], a[52:55], v[156:159]\n"
    "s_add_u32 m0, s70, s58\n"
    "v_exp_f32 v131, v131\n"
    "v_cvt_pk_bf16_f32 v208, v128, v129\n"
    "v_mfma_f32_16x16x32_bf16 v[156:159], a[88:91], a[56:59], v[156:159]\n"
    "v_cvt_pk_bf16_f32 v209, v130, v131\n"
    "v_exp_f32 v132, v132\n"
    "v_mfma_f32_16x16x32_bf16 v[156:159], a[92:95], a[60:63], v[156:159]\n"
    "ds_read_b128 a[68:71], v232 offset:256\n"
    "v_exp_f32 v133, v133\n"
    "v_mfma_f32_16x16x32_bf16 v[152:155], a[92:95], a[44:47], v[120:123]\n"
    "ds_read_b128 a[72:75], v232 offset:512\n"
    "v_exp_f32 v134, v134\n"
    "v_mfma_f32_16x16x32_bf16 v[152:155], a[88:91], a[40:43], v[152:155]\n"
    "v_exp_f32 v135, v135\n"
    "v_cvt_pk_bf16_f32 v212, v132, v133\n"
    "v_mfma_f32_16x16x32_bf16 v[152:155], a[84:87], a[36:39], v[152:155]\n"
    "v_cvt_pk_bf16_f32 v213, v134, v135\n"
    "v_exp_f32 v136, v136\n"
    "v_mfma_f32_16x16x32_bf16 v[152:155], a[80:83], a[32:35], v[152:155]\n"
    "ds_read_b128 a[76:79], v232 offset:768\n"
    "v_exp_f32 v137, v137\n"
    "v_mfma_f32_16x16x32_bf16 v[148:151], a[80:83], a[16:19], v[116:119]\n"
    "ds_read_b64_tr_b16 a[96:97], v233 offset:0\n"
    "v_exp_f32 v138, v138\n"
    "v_mfma_f32_16x16x32_bf16 v[148:151], a[84:87], a[20:23], v[148:151]\n"
    "v_exp_f32 v139, v139\n"
    "v_cvt_pk_bf16_f32 v216, v136, v137\n"
    "v_mfma_f32_16x16x32_bf16 v[148:151], a[88:91], a[24:27], v[148:151]\n"
    "v_cvt_pk_bf16_f32 v217, v138, v139\n"
    "v_exp_f32 v140, v140\n"
    "v_mfma_f32_16x16x32_bf16 v[148:151], a[92:95], a[28:31], v[148:151]\n"
    "ds_read_b64_tr_b16 a[98:99], v233 offset:4096\n"
    "v_exp_f32 v141, v141\n"
    "v_mfma_f32_16x16x32_bf16 v[144:147], a[92:95], a[12:15], v[112:115]\n"
    "v_exp_f32 v142, v142\n"
    "v_mfma_f32_16x16x32_bf16 v[144:147], a[88:91], a[8:11], v[144:147]\n"
    "ds_read_b64_tr_b16 a[100:101], v233 offset:8192\n"
    "v_exp_f32 v143, v143\n"
    "v_mfma_f32_16x16x32_bf16 v[144:147], a[84:87], a[4:7], v[144:147]\n"
    "ds_read_b64_tr_b16 a[102:103], v233 offset:12288\n"
    "v_cvt_pk_bf16_f32 v220, v140, v141\n"
    "v_cvt_pk_bf16_f32 v221, v142, v143\n"
    "v_mfma_f32_16x16x32_bf16 v[144:147], a[80:83], a[0:3], v[144:147]\n"
    "ds_read_b64_tr_b16 a[104:105], v234 offset:0\n"
    "v_exp_f32 v156, v156\n"
    "v_mfma_f32_16x16x32_bf16 v[96:99], a[112:115], v[160:163], 0\n"
    "ds_read_b64_tr_b16 a[106:107], v234 offset:4096\n"
    "v_exp_f32 v157, v157\n"
    "v_mfma_f32_16x16x32_bf16 v[96:99], a[116:119], v[176:179], v[96:99]\n"
    "ds_read_b64_tr_b16 a[108:109], v234 offset:8192\n"
    "v_exp_f32 v158, v158\n"
    "v_mfma_f32_16x16x32_bf16 v[100:103], a[116:119], v[180:183], 0\n"
    "ds_read_b64_tr_b16 a[110:111], v234 offset:12288\n"
    "v_exp_f32 v159, v159\n"
    "v_mfma_f32_16x16x32_bf16 v[100:103], a[112:115], v[164:167], v[100:103]\n"
    "v_cvt_pk_bf16_f32 v222, v156, v157\n"
    "v_cvt_pk_bf16_f32 v223, v158, v159\n"
    "v_mfma_f32_16x16x32_bf16 v[104:107], a[112:115], v[168:171], 0\n"
    "v_exp_f32 v152, v152\n"
    "v_mfma_f32_16x16x32_bf16 v[104:107], a[116:119], v[184:187], v[104:107]\n"
    "v_exp_f32 v153, v153\n"
    "v_exp_f32 v154, v154\n"
    "v_mfma_f32_16x16x32_bf16 v[108:111], a[116:119], v[188:191], 0\n"
    "buffer_load_dwordx4 v237, s[40:43], s56 offen offset:0 nt lds\n"
    "v_mfma_f32_16x16x32_bf16 v[108:111], a[112:115], v[172:175], v[108:111]\n"
    "v_exp_f32 v155, v155\n"
    "buffer_load_dwordx4 v238, s[40:43], s56 offen offset:1024 nt lds\n"
    "v_mfma_f32_16x16x32_bf16 a[180:183], a[120:123], v[172:175], 0\n"
    "v_cvt_pk_bf16_f32 v218, v152, v153\n"
    "v_cvt_pk_bf16_f32 v219, v154, v155\n"
    "v_mfma_f32_16x16x32_bf16 a[180:183], a[124:127], v[188:191], a[180:183]\n"
    "v_exp_f32 v148, v148\n"
    "buffer_load_dwordx4 v239, s[40:43], s56 offen offset:2048 nt lds\n"
    "v_mfma_f32_16x16x32_bf16 a[176:179], a[124:127], v[184:187], 0\n"
    "v_exp_f32 v149, v149\n"
    "v_mfma_f32_16x16x32_bf16 a[176:179], a[120:123], v[168:171], a[176:179]\n"
    "v_exp_f32 v150, v150\n"
    "buffer_load_dwordx4 v240, s[40:43], s56 offen offset:3072 nt lds\n"
    "s_add_u32 s56, s56, 16384\n"
    "s_add_u32 s58, s58, 16384\n"
    "s_cmp_eq_u32 s58, 49152\n"
    "s_cselect_b32 s58, 0, s58\n"
    "s_add_u32 s37, s64, 0\n"
    "v_mfma_f32_16x16x32_bf16 a[172:175], a[120:123], v[164:167], 0\n"
    "v_exp_f32 v151, v151\n"
    "v_cvt_pk_bf16_f32 v214, v148, v149\n"
    "v_mfma_f32_16x16x32_bf16 a[172:175], a[124:127], v[180:183], a[172:175]\n"
    "v_cvt_pk_bf16_f32 v215, v150, v151\n"
    "buffer_store_dwordx4 a[184:187], v245, s[52:55], s37 offen offset:0 sc1\n"
    "v_mfma_f32_16x16x32_bf16 a[168:171], a[124:127], v[176:179], 0\n"
    "buffer_store_dwordx4 a[188:191], v245, s[52:55], s37 offen offset:1024 sc1\n"
    "v_mfma_f32_16x16x32_bf16 a[168:171], a[120:123], v[160:163], a[168:171]\n"
    "buffer_store_dwordx4 a[192:195], v245, s[52:55], s37 offen offset:2048 sc1\n"
    "buffer_store_dwordx4 a[196:199], v245, s[52:55], s37 offen offset:3072 sc1\n"
    "s_waitcnt lgkmcnt(0)\n"
    "v_mfma_f32_16x16x32_bf16 v[128:131], a[64:67], a[0:3], v[112:115]\n"
    "v_exp_f32 v144, v144\n"
    "v_mfma_f32_16x16x32_bf16 v[128:131], a[68:71], a[4:7], v[128:131]\n"
    "s_add_u32 m0, s62, 0\n"
    "s_add_u32 s37, s63, 0\n"
    "v_exp_f32 v145, v145\n"
    "v_mfma_f32_16x16x32_bf16 v[128:131], a[72:75], a[8:11], v[128:131]\n"
    "v_exp_f32 v146, v146\n"
    "v_mfma_f32_16x16x32_bf16 v[128:131], a[76:79], a[12:15], v[128:131]\n"
    "v_exp_f32 v147, v147\n"
    "v_mfma_f32_16x16x32_bf16 v[132:135], a[76:79], a[28:31], v[116:119]\n"
    "ds_read_b128 a[80:83], v232 offset:4096\n"
    "v_cvt_pk_bf16_f32 v210, v144, v145\n"
    "v_mfma_f32_16x16x32_bf16 v[132:135], a[72:75], a[24:27], v[132:135]\n"
    "ds_read_b128 a[84:87], v232 offset:4352\n"
    "v_cvt_pk_bf16_f32 v211, v146, v147\n"
    "v_mfma_f32_16x16x32_bf16 v[132:135], a[68:71], a[20:23], v[132:135]\n"
    "buffer_load_dwordx4 v237, s[48:51], s37 offen offset:0 nt lds\n"
    "v_mfma_f32_16x16x32_bf16 v[132:135], a[64:67], a[16:19], v[132:135]\n"
    "buffer_load_dwordx4 v238, s[48:51], s37 offen offset:1024 nt lds\n"
    "v_mfma_f32_16x16x32_bf16 v[136:139], a[64:67], a[32:35], v[120:123]\n"
    "v_exp_f32 v128, v128\n"
    "v_mfma_f32_16x16x32_bf16 v[136:139], a[68:71], a[36:39], v[136:139]\n"
    "v_exp_f32 v129, v129\n"
    "v_mfma_f32_16x16x32_bf16 v[136:139], a[72:75], a[40:43], v[136:139]\n"
    "v_exp_f32 v130, v130\n"
    "v_mfma_f32_16x16x32_bf16 v[136:139], a[76:79], a[44:47], v[136:139]\n"
    "v_exp_f32 v131, v131\n"
    "v_mfma_f32_16x16x32_bf16 v[140:143], a[76:79], a[60:63], v[124:127]\n"
    "ds_read_b128 a[88:91], v232 offset:4608\n"
    "v_cvt_pk_bf16_f32 v160, v128, v129\n"
    "v_mfma_f32_16x16x32_bf16 v[140:143], a[72:75], a[56:59], v[140:143]\n"
    "ds_read_b128 a[92:95], v232 offset:4864\n"
    "v_cvt_pk_bf16_f32 v161, v130, v131\n"
    "v_mfma_f32_16x16x32_bf16 v[140:143], a[68:71], a[52:55], v[140:143]\n"
    "buffer_load_dwordx4 v239, s[48:51], s37 offen offset:2048 nt lds\n"
    "v_mfma_f32_16x16x32_bf16 v[140:143], a[64:67], a[48:51], v[140:143]\n"
    "v_exp_f32 v132, v132\n"
    "v_mfma_f32_16x16x32_bf16 a[128:131], a[144:147], v[192:195], a[128:131]\n"
    "v_exp_f32 v133, v133\n"
    "v_mfma_f32_16x16x32_bf16 a[128:131], a[144:147], v[208:211], a[128:131]\n"
    "v_exp_f32 v134, v134\n"
    "v_mfma_f32_16x16x32_bf16 a[132:135], a[144:147], v[196:199], a[132:135]\n"
    "v_exp_f32 v135, v135\n"
    "v_mfma_f32_16x16x32_bf16 a[132:135], a[144:147], v[212:215], a[132:135]\n"
    "ds_read_b64_tr_b16 a[112:113], v233 offset:256\n"
    "v_cvt_pk_bf16_f32 v164, v132, v133\n"
    "v_mfma_f32_16x16x32_bf16 a[136:139], a[144:147], v[200:203], a[136:139]\n"
    "ds_read_b64_tr_b16 a[114:115], v233 offset:4352\n"
    "v_cvt_pk_bf16_f32 v165, v134, v135\n"
    "v_mfma_f32_16x16x32_bf16 a[136:139], a[144:147], v[216:219], a[136:139]\n"
    "buffer_load_dwordx4 v240, s[48:51], s37 offen offset:3072 nt lds\n"
    "v_mfma_f32_16x16x32_bf16 a[140:143], a[144:147], v[204:207], a[140:143]\n"
    "v_exp_f32 v136, v136\n"
    "v_mfma_f32_16x16x32_bf16 a[140:143], a[144:147], v[220:223], a[140:143]\n"
    "v_exp_f32 v137, v137\n"
    "v_mfma_f32_16x16x32_bf16 v[0:3], a[96:99], v[192:195], v[0:3]\n"
    "v_exp_f32 v138, v138\n"
    "v_mfma_f32_16x16x32_bf16 v[0:3], a[100:103], v[208:211], v[0:3]\n"
    "s_add_u32 m0, s71, s59\n"
    "s_nop 0\n"
    "buffer_load_dwordx4 v237, s[44:47], s57 offen offset:0 lds\n"
    "v_mfma_f32_16x16x32_bf16 v[4:7], a[100:103], v[212:215], v[4:7]\n"
    "v_exp_f32 v139, v139\n"
    "v_mfma_f32_16x16x32_bf16 v[4:7], a[96:99], v[196:199], v[4:7]\n"
    "ds_read_b64_tr_b16 a[116:117], v233 offset:8448\n"
    "v_cvt_pk_bf16_f32 v168, v136, v137\n"
    "v_mfma_f32_16x16x32_bf16 v[8:11], a[96:99], v[200:203], v[8:11]\n"
    "ds_read_b64_tr_b16 a[118:119], v233 offset:12544\n"
    "v_cvt_pk_bf16_f32 v169, v138, v139\n"
    "v_mfma_f32_16x16x32_bf16 v[8:11], a[100:103], v[216:219], v[8:11]\n"
    "v_exp_f32 v140, v140\n"
    "v_mfma_f32_16x16x32_bf16 v[12:15], a[100:103], v[220:223], v[12:15]\n"
    "v_exp_f32 v141, v141\n"
    "v_mfma_f32_16x16x32_bf16 v[12:15], a[96:99], v[204:207], v[12:15]\n"
    "buffer_load_dwordx4 v238, s[44:47], s57 offen offset:1024 lds\n"
    "v_mfma_f32_16x16x32_bf16 v[28:31], a[104:107], v[204:207], v[28:31]\n"
    "v_exp_f32 v142, v142\n"
    "v_mfma_f32_16x16x32_bf16 v[28:31], a[108:111], v[220:223], v[28:31]\n"
    "v_exp_f32 v143, v143\n"
    "v_mfma_f32_16x16x32_bf16 v[24:27], a[108:111], v[216:219], v[24:27]\n"
    "ds_read_b64_tr_b16 a[120:121], v234 offset:256\n"
    "v_cvt_pk_bf16_f32 v172, v140, v141\n"
    "v_mfma_f32_16x16x32_bf16 v[24:27], a[104:107], v[200:203], v[24:27]\n"
    "ds_read_b64_tr_b16 a[122:123], v234 offset:4352\n"
    "v_cvt_pk_bf16_f32 v173, v142, v143\n"
    "v_mfma_f32_16x16x32_bf16 v[20:23], a[104:107], v[196:199], v[20:23]\n"
    "ds_read_b64_tr_b16 a[124:125], v234 offset:8448\n"
    "v_mfma_f32_16x16x32_bf16 v[20:23], a[108:111], v[212:215], v[20:23]\n"
    "ds_read_b64_tr_b16 a[126:127], v234 offset:12544\n"
    "v_mfma_f32_16x16x32_bf16 v[16:19], a[108:111], v[208:211], v[16:19]\n"
    "buffer_load_dwordx4 v239, s[44:47], s57 offen offset:2048 lds\n"
    "v_mfma_f32_16x16x32_bf16 v[16:19], a[104:107], v[192:195], v[16:19]\n"
    "s_waitcnt lgkmcnt(8)\n"
    "v_mfma_f32_16x16x32_bf16 v[156:159], a[80:83], a[48:51], v[124:127]\n"
    "ds_read_b128 a[64:67], v232 offset:8192\n"
    "v_mfma_f32_16x16x32_bf16 v[156:159], a[84:87], a[52:55], v[156:159]\n"
    "ds_read_b128 a[68:71], v232 offset:8448\n"
    "v_mfma_f32_16x16x32_bf16 v[156:159], a[88:91], a[56:59], v[156:159]\n"
    "ds_read_b128 a[72:75], v232 offset:8704\n"
    "v_mfma_f32_16x16x32_bf16 v[156:159], a[92:95], a[60:63], v[156:159]\n"
    "buffer_load_dwordx4 v240, s[44:47], s57 offen offset:3072 lds\n"
    "v_mfma_f32_16x16x32_bf16 v[152:155], a[92:95], a[44:47], v[120:123]\n"
    "ds_read_b128 a[76:79], v232 offset:8960\n"
    "v_mfma_f32_16x16x32_bf16 v[152:155], a[88:91], a[40:43], v[152:155]\n"
    "ds_read_b64_tr_b16 a[96:97], v233 offset:512\n"
    "v_mfma_f32_16x16x32_bf16 v[152:155], a[84:87], a[36:39], v[152:155]\n"
    "ds_read_b64_tr_b16 a[98:99], v233 offset:4608\n"
    "v_mfma_f32_16x16x32_bf16 v[152:155], a[80:83], a[32:35], v[152:155]\n"
    "v_exp_f32 v156, v156\n"
    "v_mfma_f32_16x16x32_bf16 v[148:151], a[80:83], a[16:19], v[116:119]\n"
    "v_exp_f32 v157, v157\n"
    "v_mfma_f32_16x16x32_bf16 v[148:151], a[84:87], a[20:23], v[148:151]\n"
    "s_add_u32 s57, s57, 16384\n"
    "s_add_u32 s59, s59, 16384\n"
    "s_cmp_eq_u32 s59, 49152\n"
    "s_cselect_b32 s59, 0, s59\n"
    "v_exp_f32 v158, v158\n"
    "v_mfma_f32_16x16x32_bf16 v[148:151], a[88:91], a[24:27], v[148:151]\n"
    "v_exp_f32 v159, v159\n"
    "v_mfma_f32_16x16x32_bf16 v[148:151], a[92:95], a[28:31], v[148:151]\n"
    "ds_read_b64_tr_b16 a[100:101], v233 offset:8704\n"
    "v_cvt_pk_bf16_f32 v174, v156, v157\n"
    "v_mfma_f32_16x16x32_bf16 v[144:147], a[92:95], a[12:15], v[112:115]\n"
    "ds_read_b64_tr_b16 a[102:103], v233 offset:12800\n"
    "v_cvt_pk_bf16_f32 v175, v158, v159\n"
    "v_mfma_f32_16x16x32_bf16 v[144:147], a[88:91], a[8:11], v[144:147]\n"
    "v_exp_f32 v152, v152\n"
    "v_mfma_f32_16x16x32_bf16 v[144:147], a[84:87], a[4:7], v[144:147]\n"
    "v_exp_f32 v153, v153\n"
    "v_mfma_f32_16x16x32_bf16 v[144:147], a[80:83], a[0:3], v[144:147]\n"
    "v_exp_f32 v154, v154\n"
    "s_waitcnt lgkmcnt(8)\n"
    "v_mfma_f32_16x16x32_bf16 v[32:35], a[112:115], v[192:195], v[32:35]\n"
    "v_exp_f32 v155, v155\n"
    "v_mfma_f32_16x16x32_bf16 v[32:35], a[116:119], v[208:211], v[32:35]\n"
    "ds_read_b64_tr_b16 a[104:105], v234 offset:512\n"
    "v_cvt_pk_bf16_f32 v170, v152, v153\n"
    "v_mfma_f32_16x16x32_bf16 v[36:39], a[116:119], v[212:215], v[36:39]\n"
    "ds_read_b64_tr_b16 a[106:107], v234 offset:4608\n"
    "v_cvt_pk_bf16_f32 v171, v154, v155\n"
    "v_mfma_f32_16x16x32_bf16 v[36:39], a[112:115], v[196:199], v[36:39]\n"
    "v_exp_f32 v148, v148\n"
    "v_mfma_f32_16x16x32_bf16 v[40:43], a[112:115], v[200:203], v[40:43]\n"
    "v_exp_f32 v149, v149\n"
    "v_mfma_f32_16x16x32_bf16 v[40:43], a[116:119], v[216:219], v[40:43]\n"
    "v_exp_f32 v150, v150\n"
    "v_mfma_f32_16x16x32_bf16 v[44:47], a[116:119], v[220:223], v[44:47]\n"
    "v_exp_f32 v151, v151\n"
    "v_mfma_f32_16x16x32_bf16 v[44:47], a[112:115], v[204:207], v[44:47]\n"
    "ds_read_b64_tr_b16 a[108:109], v234 offset:8704\n"
    "v_cvt_pk_bf16_f32 v166, v148, v149\n"
    "v_mfma_f32_16x16x32_bf16 v[60:63], a[120:123], v[204:207], v[60:63]\n"
    "ds_read_b64_tr_b16 a[110:111], v234 offset:12800\n"
    "v_cvt_pk_bf16_f32 v167, v150, v151\n"
    "v_mfma_f32_16x16x32_bf16 v[60:63], a[124:127], v[220:223], v[60:63]\n"
    "v_exp_f32 v144, v144\n"
    "v_mfma_f32_16x16x32_bf16 v[56:59], a[124:127], v[216:219], v[56:59]\n"
    "v_exp_f32 v145, v145\n"
    "v_mfma_f32_16x16x32_bf16 v[56:59], a[120:123], v[200:203], v[56:59]\n"
    "v_exp_f32 v146, v146\n"
    "v_mfma_f32_16x16x32_bf16 v[52:55], a[120:123], v[196:199], v[52:55]\n"
    "v_exp_f32 v147, v147\n"
    "v_mfma_f32_16x16x32_bf16 v[52:55], a[124:127], v[212:215], v[52:55]\n"
    "v_cvt_pk_bf16_f32 v162, v144, v145\n"
    "v_cvt_pk_bf16_f32 v163, v146, v147\n"
    "v_mfma_f32_16x16x32_bf16 v[48:51], a[124:127], v[208:211], v[48:51]\n"
    "v_mfma_f32_16x16x32_bf16 v[48:51], a[120:123], v[192:195], v[48:51]\n"
    "s_waitcnt lgkmcnt(8)\n"
    "v_mfma_f32_16x16x32_bf16 v[128:131], a[64:67], a[0:3], v[112:115]\n"
    "ds_read_b128 a[200:203], v243 offset:4096\n"
    "v_mfma_f32_16x16x32_bf16 v[128:131], a[68:71], a[4:7], v[128:131]\n"
    "ds_read_b128 a[204:207], v244 offset:5120\n"
    "v_mfma_f32_16x16x32_bf16 v[128:131], a[72:75], a[8:11], v[128:131]\n"
    "ds_read_b128 a[208:211], v243 offset:6144\n"
    "v_mfma_f32_16x16x32_bf16 v[128:131], a[76:79], a[12:15], v[128:131]\n"
    "ds_read_b128 a[212:215], v244 offset:7168\n"
    "v_mfma_f32_16x16x32_bf16 v[132:135], a[76:79], a[28:31], v[116:119]\n"
    "ds_read_b128 a[80:83], v232 offset:12288\n"
    "v_mfma_f32_16x16x32_bf16 v[132:135], a[72:75], a[24:27], v[132:135]\n"
    "ds_read_b128 a[84:87], v232 offset:12544\n"
    "v_mfma_f32_16x16x32_bf16 v[132:135], a[68:71], a[20:23], v[132:135]\n"
    "ds_read_b128 a[88:91], v232 offset:12800\n"
    "v_mfma_f32_16x16x32_bf16 v[132:135], a[64:67], a[16:19], v[132:135]\n"
    "v_exp_f32 v128, v128\n"
    "v_mfma_f32_16x16x32_bf16 v[136:139], a[64:67], a[32:35], v[120:123]\n"
    "v_exp_f32 v129, v129\n"
    "v_mfma_f32_16x16x32_bf16 v[136:139], a[68:71], a[36:39], v[136:139]\n"
    "v_exp_f32 v130, v130\n"
    "v_mfma_f32_16x16x32_bf16 v[136:139], a[72:75], a[40:43], v[136:139]\n"
    "v_exp_f32 v131, v131\n"
    "v_mfma_f32_16x16x32_bf16 v[136:139], a[76:79], a[44:47], v[136:139]\n"
    "ds_read_b128 a[92:95], v232 offset:13056\n"
    "v_cvt_pk_bf16_f32 v176, v128, v129\n"
    "v_mfma_f32_16x16x32_bf16 v[140:143], a[76:79], a[60:63], v[124:127]\n"
    "ds_read_b64_tr_b16 a[112:113], v233 offset:768\n"
    "v_cvt_pk_bf16_f32 v177, v130, v131\n"
    "v_mfma_f32_16x16x32_bf16 v[140:143], a[72:75], a[56:59], v[140:143]\n"
    "v_exp_f32 v132, v132\n"
    "v_mfma_f32_16x16x32_bf16 v[140:143], a[68:71], a[52:55], v[140:143]\n"
    "v_exp_f32 v133, v133\n"
    "v_mfma_f32_16x16x32_bf16 v[140:143], a[64:67], a[48:51], v[140:143]\n"
    "ds_read_b64_tr_b16 a[114:115], v233 offset:4864\n"
    "s_waitcnt lgkmcnt(10)\n"
    "v_mfma_f32_16x16x32_bf16 v[64:67], a[96:99], v[192:195], v[64:67]\n"
    "ds_read_b64_tr_b16 a[116:117], v233 offset:8960\n"
    "v_mfma_f32_16x16x32_bf16 v[64:67], a[100:103], v[208:211], v[64:67]\n"
    "ds_read_b64_tr_b16 a[118:119], v233 offset:13056\n"
    "v_mfma_f32_16x16x32_bf16 v[68:71], a[100:103], v[212:215], v[68:71]\n"
    "ds_read_b64_tr_b16 a[120:121], v234 offset:768\n"
    "v_mfma_f32_16x16x32_bf16 v[68:71], a[96:99], v[196:199], v[68:71]\n"
    "ds_read_b64_tr_b16 a[122:123], v234 offset:4864\n"
    "v_mfma_f32_16x16x32_bf16 v[72:75], a[96:99], v[200:203], v[72:75]\n"
    "ds_read_b64_tr_b16 a[124:125], v234 offset:8960\n"
    "v_mfma_f32_16x16x32_bf16 v[72:75], a[100:103], v[216:219], v[72:75]\n"
    "ds_read_b64_tr_b16 a[126:127], v234 offset:13056\n"
    "s_memrealtime s[90:91]\n"
    "v_mfma_f32_16x16x32_bf16 v[76:79], a[100:103], v[220:223], v[76:79]\n"
    "v_exp_f32 v134, v134\n"
    "v_mfma_f32_16x16x32_bf16 v[76:79], a[96:99], v[204:207], v[76:79]\n"
    "v_exp_f32 v135, v135\n"
    "v_mfma_f32_16x16x32_bf16 v[92:95], a[104:107], v[204:207], v[92:95]\n"
    "v_cvt_pk_bf16_f32 v180, v132, v133\n"
    "v_cvt_pk_bf16_f32 v181, v134, v135\n"
    "v_mfma_f32_16x16x32_bf16 v[92:95], a[108:111], v[220:223], v[92:95]\n"
    "v_exp_f32 v136, v136\n"
    "v_mfma_f32_16x16x32_bf16 v[88:91], a[108:111], v[216:219], v[88:91]\n"
    "v_exp_f32 v137, v137\n"
    "v_mfma_f32_16x16x32_bf16 v[88:91], a[104:107], v[200:203], v[88:91]\n"
    "v_exp_f32 v138, v138\n"
    "v_mfma_f32_16x16x32_bf16 v[84:87], a[104:107], v[196:199], v[84:87]\n"
    "v_exp_f32 v139, v139\n"
    "v_mfma_f32_16x16x32_bf16 v[84:87], a[108:111], v[212:215], v[84:87]\n"
    "v_cvt_pk_bf16_f32 v184, v136, v137\n"
    "v_cvt_pk_bf16_f32 v185, v138, v139\n"
    "v_mfma_f32_16x16x32_bf16 v[80:83], a[108:111], v[208:211], v[80:83]\n"
    "v_exp_f32 v140, v140\n"
    "v_mfma_f32_16x16x32_bf16 v[80:83], a[104:107], v[192:195], v[80:83]\n"
    "v_exp_f32 v141, v141\n"
    "v_add_u32 v233, s60, v236\n"
    "s_add_u32 s60, s60, 16384\n"
    "s_cmp_eq_u32 s60, 49152\n"
    "s_cselect_b32 s60, 0, s60\n"
    "v_xor_b32 v234, 32, v233\n"
    "v_add_u32 v232, s60, v235\n"
    "s_waitcnt vmcnt(16) lgkmcnt(0)\n"
    "s_sub_u32 s90, s90, s73\n"
    "s_cmp_ge_i32 s90, 0\n"
    "s_cbranch_scc1 .Lpace2x\n"
    ".Lpace2:\n"
    "s_sleep 1\n"
    "s_memrealtime s[90:91]\n"
    "s_waitcnt lgkmcnt(0)\n"
    "s_sub_u32 s90, s90, s73\n"
    "s_cmp_lt_i32 s90, 8\n"
    "s_cbranch_scc1 .Lpace2\n"
    ".Lpace2x:\n"
    "s_add_u32 s73, s73, s88\n"
    "s_barrier\n"
    "v_mfma_f32_16x16x32_bf16 v[156:159], a[80:83], a[48:51], v[124:127]\n"
    "v_exp_f32 v142, v142\n"
    "v_mfma_f32_16x16x32_bf16 v[156:159], a[84:87], a[52:55], v[156:159]\n"
    "s_add_u32 m0, s70, s58\n"
    "v_exp_f32 v143, v143\n"
    "v_mfma_f32_16x16x32_bf16 v[156:159], a[88:91], a[56:59], v[156:159]\n"
    "ds_read_b128 a[64:67], v232 offset:0\n"
    "v_cvt_pk_bf16_f32 v188, v140, v141\n"
    "v_mfma_f32_16x16x32_bf16 v[156:159], a[92:95], a[60:63], v[156:159]\n"
    "ds_read_b128 a[68:71], v232 offset:256\n"
    "v_cvt_pk_bf16_f32 v189, v142, v143\n"
    "v_mfma_f32_16x16x32_bf16 v[152:155], a[92:95], a[44:47], v[120:123]\n"
    "buffer_load_dwordx4 v237, s[40:43], s56 offen offset:0 nt lds\n"
    "v_mfma_f32_16x16x32_bf16 v[152:155], a[88:91], a[40:43], v[152:155]\n"
    "ds_read_b128 a[72:75], v232 offset:512\n"
    "v_mfma_f32_16x16x32_bf16 v[152:155], a[84:87], a[36:39], v[152:155]\n"
    "ds_read_b128 a[76:79], v232 offset:768\n"
    "v_mfma_f32_16x16x32_bf16 v[152:155], a[80:83], a[32:35], v[152:155]\n"
    "v_exp_f32 v156, v156\n"
    "v_mfma_f32_16x16x32_bf16 v[148:151], a[80:83], a[16:19], v[116:119]\n"
    "v_exp_f32 v157, v157\n"
    "v_mfma_f32_16x16x32_bf16 v[148:151], a[84:87], a[20:23], v[148:151]\n"
    "v_exp_f32 v158, v158\n"
    "v_mfma_f32_16x16x32_bf16 v[148:151], a[88:91], a[24:27], v[148:151]\n"
    "v_exp_f32 v159, v159\n"
    "v_mfma_f32_16x16x32_bf16 v[148:151], a[92:95], a[28:31], v[148:151]\n"
    "ds_read_b64_tr_b16 a[96:97], v233 offset:0\n"
    "v_cvt_pk_bf16_f32 v190, v156, v157\n"
    "v_mfma_f32_16x16x32_bf16 v[144:147], a[92:95], a[12:15], v[112:115]\n"
    "ds_read_b64_tr_b16 a[98:99], v233 offset:4096\n"
    "v_cvt_pk_bf16_f32 v191, v158, v159\n"
    "v_mfma_f32_16x16x32_bf16 v[144:147], a[88:91], a[8:11], v[144:147]\n"
    "v_exp_f32 v152, v152\n"
    "v_mfma_f32_16x16x32_bf16 v[144:147], a[84:87], a[4:7], v[144:147]\n"
    "v_exp_f32 v153, v153\n"
    "v_mfma_f32_16x16x32_bf16 v[144:147], a[80:83], a[0:3], v[144:147]\n"
    "v_exp_f32 v154, v154\n"
    "v_mfma_f32_16x16x32_bf16 v[96:99], a[112:115], v[192:195], v[96:99]\n"
    "ds_read_b64_tr_b16 a[100:101], v233 offset:8192\n"
    "v_mfma_f32_16x16x32_bf16 v[96:99], a[116:119], v[208:211], v[96:99]\n"
    "ds_read_b64_tr_b16 a[102:103], v233 offset:12288\n"
    "v_mfma_f32_16x16x32_bf16 v[100:103], a[116:119], v[212:215], v[100:103]\n"
    "ds_read_b64_tr_b16 a[104:105], v234 offset:0\n"
    "v_mfma_f32_16x16x32_bf16 v[100:103], a[112:115], v[196:199], v[100:103]\n"
    "ds_read_b64_tr_b16 a[106:107], v234 offset:4096\n"
    "v_mfma_f32_16x16x32_bf16 v[104:107], a[112:115], v[200:203], v[104:107]\n"
    "ds_read_b64_tr_b16 a[108:109], v234 offset:8192\n"
    "v_mfma_f32_16x16x32_bf16 v[104:107], a[116:119], v[216:219], v[104:107]\n"
    "ds_read_b64_tr_b16 a[110:111], v234 offset:12288\n"
    "v_mfma_f32_16x16x32_bf16 v[108:111], a[116:119], v[220:223], v[108:111]\n"
    "v_exp_f32 v155, v155\n"
    "v_mfma_f32_16x16x32_bf16 v[108:111], a[112:115], v[204:207], v[108:111]\n"
    "v_cvt_pk_bf16_f32 v186, v152, v153\n"
    "v_cvt_pk_bf16_f32 v187, v154, v155\n"
    "v_mfma_f32_16x16x32_bf16 a[180:183], a[120:123], v[204:207], a[180:183]\n"
    "v_exp_f32 v148, v148\n"
    "v_mfma_f32_16x16x32_bf16 a[180:183], a[124:127], v[220:223], a[180:183]\n"
    "v_exp_f32 v149, v149\n"
    "v_mfma_f32_16x16x32_bf16 a[176:179], a[124:127], v[216:219], a[176:179]\n"
    "v_exp_f32 v150, v150\n"
    "v_mfma_f32_16x16x32_bf16 a[176:179], a[120:123], v[200:203], a[176:179]\n"
    "buffer_load_dwordx4 v238, s[40:43], s56 offen offset:1024 nt lds\n"
    "v_mfma_f32_16x16x32_bf16 a[172:175], a[120:123], v[196:199], a[172:175]\n"
    "v_exp_f32 v151, v151\n"
    "v_mfma_f32_16x16x32_bf16 a[172:175], a[124:127], v[212:215], a[172:175]\n"
    "v_cvt_pk_bf16_f32 v182, v148, v149\n"
    "v_cvt_pk_bf16_f32 v183, v150, v151\n"
    "v_mfma_f32_16x16x32_bf16 a[168:171], a[124:127], v[208:211], a[168:171]\n"
    "buffer_load_dwordx4 v239, s[40:43], s56 offen offset:2048 nt lds\n"
    "v_mfma_f32_16x16x32_bf16 a[168:171], a[120:123], v[192:195], a[168:171]\n"
    "buffer_load_dwordx4 v240, s[40:43], s56 offen offset:3072 nt lds\n"
    "s_add_u32 s56, s56, 16384\n"
    "s_add_u32 s58, s58, 16384\n"
    "s_cmp_eq_u32 s58, 49152\n"
    "s_cselect_b32 s58, 0, s58\n"
    "s_waitcnt lgkmcnt(0)\n"
    "v_mfma_f32_16x16x32_bf16 v[128:131], a[64:67], a[0:3], v[112:115]\n"
    "ds_read_b128 a[80:83], v232 offset:4096\n"
    "v_lshrrev_b32 v247, 8, v245\n"
    "v_mfma_f32_16x16x32_bf16 v[128:131], a[68:71], a[4:7], v[128:131]\n"
    "ds_read_b128 a[84:87], v232 offset:4352\n"
    "s_add_u32 m0, s62, 4096\n"
    "s_add_u32 s37, s63, 4096\n"
    "v_mul_u32_u24 v247, 24, v247\n"
    "v_mfma_f32_16x16x32_bf16 v[128:131], a[72:75], a[8:11], v[128:131]\n"
    "ds_read_b128 a[88:91], v232 offset:4608\n"
    "v_and_b32 v247, 24, v247\n"
    "v_mfma_f32_16x16x32_bf16 v[128:131], a[76:79], a[12:15], v[128:131]\n"
    "ds_read_b128 a[92:95], v232 offset:4864\n"
    "v_xor_b32 v247, v247, v253\n"
    "v_mfma_f32_16x16x32_bf16 v[132:135], a[76:79], a[28:31], v[116:119]\n"
    "ds_read_b64_tr_b16 a[112:113], v233 offset:256\n"
    "v_xor_b32 v248, 32, v247\n"
    "v_mfma_f32_16x16x32_bf16 v[132:135], a[72:75], a[24:27], v[132:135]\n"
    "ds_read_b64_tr_b16 a[114:115], v233 offset:4352\n"
    "v_xor_b32 v246, 32, v253\n"
    "v_mfma_f32_16x16x32_bf16 v[132:135], a[68:71], a[20:23], v[132:135]\n"
    "v_exp_f32 v144, v144\n"
    "v_mfma_f32_16x16x32_bf16 v[132:135], a[64:67], a[16:19], v[132:135]\n"
    "v_exp_f32 v145, v145\n"
    "v_mfma_f32_16x16x32_bf16 v[136:139], a[64:67], a[32:35], v[120:123]\n"
    "v_exp_f32 v146, v146\n"
    "v_mfma_f32_16x16x32_bf16 v[136:139], a[68:71], a[36:39], v[136:139]\n"
    "v_exp_f32 v147, v147\n"
    "v_mfma_f32_16x16x32_bf16 v[136:139], a[72:75], a[40:43], v[136:139]\n"
    "ds_read_b64_tr_b16 a[116:117], v233 offset:8448\n"
    "v_cvt_pk_bf16_f32 v178, v144, v145\n"
    "v_mfma_f32_16x16x32_bf16 v[136:139], a[76:79], a[44:47], v[136:139]\n"
    "ds_read_b64_tr_b16 a[118:119], v233 offset:12544\n"
    "v_cvt_pk_bf16_f32 v179, v146, v147\n"
    "v_mfma_f32_16x16x32_bf16 v[140:143], a[76:79], a[60:63], v[124:127]\n"
    "buffer_load_dwordx4 v237, s[48:51], s37 offen offset:0 nt lds\n"
    "v_mfma_f32_16x16x32_bf16 v[140:143], a[72:75], a[56:59], v[140:143]\n"
    "buffer_load_dwordx4 v238, s[48:51], s37 offen offset:1024 nt lds\n"
    "v_mfma_f32_16x16x32_bf16 v[140:143], a[68:71], a[52:55], v[140:143]\n"
    "buffer_load_dwordx4 v239, s[48:51], s37 offen offset:2048 nt lds\n"
    "v_mfma_f32_16x16x32_bf16 v[140:143], a[64:67], a[48:51], v[140:143]\n"
    "v_exp_f32 v128, v128\n"
    "v_mfma_f32_16x16x32_bf16 a[128:131], a[144:147], v[160:163], a[128:131]\n"
    "v_exp_f32 v129, v129\n"
    "v_mfma_f32_16x16x32_bf16 a[128:131], a[144:147], v[176:179], a[128:131]\n"
    "v_exp_f32 v130, v130\n"
    "v_mfma_f32_16x16x32_bf16 a[132:135], a[144:147], v[164:167], a[132:135]\n"
    "v_exp_f32 v131, v131\n"
    "v_mfma_f32_16x16x32_bf16 a[132:135], a[144:147], v[180:183], a[132:135]\n"
    "ds_read_b64_tr_b16 a[120:121], v234 offset:256\n"
    "v_cvt_pk_bf16_f32 v192, v128, v129\n"
    "v_mfma_f32_16x16x32_bf16 a[136:139], a[144:147], v[168:171], a[136:139]\n"
    "ds_read_b64_tr_b16 a[122:123], v234 offset:4352\n"
    "v_cvt_pk_bf16_f32 v193, v130, v131\n"
    "v_mfma_f32_16x16x32_bf16 a[136:139], a[144:147], v[184:187], a[136:139]\n"
    "buffer_load_dwordx4 v240, s[48:51], s37 offen offset:3072 nt lds\n"
    "v_mfma_f32_16x16x32_bf16 a[140:143], a[144:147], v[172:175], a[140:143]\n"
    "v_exp_f32 v132, v132\n"
    "v_mfma_f32_16x16x32_bf16 a[140:143], a[144:147], v[188:191], a[140:143]\n"
    "v_exp_f32 v133, v133\n"
    "v_mfma_f32_16x16x32_bf16 v[0:3], a[96:99], v[160:163], v[0:3]\n"
    "v_exp_f32 v134, v134\n"
    "v_mfma_f32_16x16x32_bf16 v[0:3], a[100:103], v[176:179], v[0:3]\n"
    "s_add_u32 m0, s71, s59\n"
    "s_nop 0\n"
    "buffer_load_dwordx4 v237, s[44:47], s57 offen offset:0 lds\n"
    "v_mfma_f32_16x16x32_bf16 v[4:7], a[100:103], v[180:183], v[4:7]\n"
    "v_exp_f32 v135, v135\n"
    "v_mfma_f32_16x16x32_bf16 v[4:7], a[96:99], v[164:167], v[4:7]\n"
    "ds_read_b64_tr_b16 a[124:125], v234 offset:8448\n"
    "v_cvt_pk_bf16_f32 v196, v132, v133\n"
    "v_mfma_f32_16x16x32_bf16 v[8:11], a[96:99], v[168:171], v[8:11]\n"
    "ds_read_b64_tr_b16 a[126:127], v234 offset:12544\n"
    "v_cvt_pk_bf16_f32 v197, v134, v135\n"
    "v_mfma_f32_16x16x32_bf16 v[8:11], a[100:103], v[184:187], v[8:11]\n"
    "v_exp_f32 v136, v136\n"
    "v_mfma_f32_16x16x32_bf16 v[12:15], a[100:103], v[188:191], v[12:15]\n"
    "v_exp_f32 v137, v137\n"
    "v_mfma_f32_16x16x32_bf16 v[12:15], a[96:99], v[172:175], v[12:15]\n"
    "buffer_load_dwordx4 v238, s[44:47], s57 offen offset:1024 lds\n"
    "v_mfma_f32_16x16x32_bf16 v[28:31], a[104:107], v[172:175], v[28:31]\n"
    "v_exp_f32 v138, v138\n"
    "v_mfma_f32_16x16x32_bf16 v[28:31], a[108:111], v[188:191], v[28:31]\n"
    "v_exp_f32 v139, v139\n"
    "v_mfma_f32_16x16x32_bf16 v[24:27], a[108:111], v[184:187], v[24:27]\n"
    "v_cvt_pk_bf16_f32 v200, v136, v137\n"
    "v_cvt_pk_bf16_f32 v201, v138, v139\n"
    "v_mfma_f32_16x16x32_bf16 v[24:27], a[104:107], v[168:171], v[24:27]\n"
    "v_exp_f32 v140, v140\n"
    "v_mfma_f32_16x16x32_bf16 v[20:23], a[104:107], v[164:167], v[20:23]\n"
    "v_exp_f32 v141, v141\n"
    "v_mfma_f32_16x16x32_bf16 v[20:23], a[108:111], v[180:183], v[20:23]\n"
    "buffer_load_dwordx4 v239, s[44:47], s57 offen offset:2048 lds\n"
    "v_mfma_f32_16x16x32_bf16 v[16:19], a[108:111], v[176:179], v[16:19]\n"
    "v_exp_f32 v142, v142\n"
    "v_mfma_f32_16x16x32_bf16 v[16:19], a[104:107], v[160:163], v[16:19]\n"
    "v_exp_f32 v143, v143\n"
    "s_waitcnt lgkmcnt(8)\n"
    "v_mfma_f32_16x16x32_bf16 v[156:159], a[80:83], a[48:51], v[124:127]\n"
    "ds_read_b128 a[64:67], v232 offset:8192\n"
    "v_cvt_pk_bf16_f32 v204, v140, v141\n"
    "v_mfma_f32_16x16x32_bf16 v[156:159], a[84:87], a[52:55], v[156:159]\n"
    "ds_read_b128 a[68:71], v232 offset:8448\n"
    "v_cvt_pk_bf16_f32 v205, v142, v143\n"
    "v_mfma_f32_16x16x32_bf16 v[156:159], a[88:91], a[56:59], v[156:159]\n"
    "ds_read_b128 a[72:75], v232 offset:8704\n"
    "v_mfma_f32_16x16x32_bf16 v[156:159], a[92:95], a[60:63], v[156:159]\n"
    "buffer_load_dwordx4 v240, s[44:47], s57 offen offset:3072 lds\n"
    "v_mfma_f32_16x16x32_bf16 v[152:155], a[92:95], a[44:47], v[120:123]\n"
    "ds_read_b128 a[76:79], v232 offset:8960\n"
    "v_mfma_f32_16x16x32_bf16 v[152:155], a[88:91], a[40:43], v[152:155]\n"
    "ds_read_b64_tr_b16 a[96:97], v233 offset:512\n"
    "v_mfma_f32_16x16x32_bf16 v[152:155], a[84:87], a[36:39], v[152:155]\n"
    "ds_read_b64_tr_b16 a[98:99], v233 offset:4608\n"
    "v_mfma_f32_16x16x32_bf16 v[152:155], a[80:83], a[32:35], v[152:155]\n"
    "v_exp_f32 v156, v156\n"
    "v_mfma_f32_16x16x32_bf16 v[148:151], a[80:83], a[16:19], v[116:119]\n"
    "v_exp_f32 v157, v157\n"
    "v_mfma_f32_16x16x32_bf16 v[148:151], a[84:87], a[20:23], v[148:151]\n"
    "s_add_u32 s57, s57, 16384\n"
    "s_add_u32 s59, s59, 16384\n"
    "s_cmp_eq_u32 s59, 49152\n"
    "s_cselect_b32 s59, 0, s59\n"
    "v_exp_f32 v158, v158\n"
    "v_mfma_f32_16x16x32_bf16 v[148:151], a[88:91], a[24:27], v[148:151]\n"
    "v_exp_f32 v159, v159\n"
    "v_mfma_f32_16x16x32_bf16 v[148:151], a[92:95], a[28:31], v[148:151]\n"
    "ds_read_b64_tr_b16 a[100:101], v233 offset:8704\n"
    "v_cvt_pk_bf16_f32 v206, v156, v157\n"
    "v_mfma_f32_16x16x32_bf16 v[144:147], a[92:95], a[12:15], v[112:115]\n"
    "ds_read_b64_tr_b16 a[102:103], v233 offset:12800\n"
    "v_cvt_pk_bf16_f32 v207, v158, v159\n"
    "v_mfma_f32_16x16x32_bf16 v[144:147], a[88:91], a[8:11], v[144:147]\n"
    "v_exp_f32 v152, v152\n"
    "v_mfma_f32_16x16x32_bf16 v[144:147], a[84:87], a[4:7], v[144:147]\n"
    "v_exp_f32 v153, v153\n"
    "v_mfma_f32_16x16x32_bf16 v[144:147], a[80:83], a[0:3], v[144:147]\n"
    "v_exp_f32 v154, v154\n"
    "s_waitcnt lgkmcnt(8)\n"
    "v_mfma_f32_16x16x32_bf16 v[32:35], a[112:115], v[160:163], v[32:35]\n"
    "v_exp_f32 v155, v155\n"
    "v_mfma_f32_16x16x32_bf16 v[32:35], a[116:119], v[176:179], v[32:35]\n"
    "ds_read_b64_tr_b16 a[104:105], v234 offset:512\n"
    "v_cvt_pk_bf16_f32 v202, v152, v153\n"
    "v_mfma_f32_16x16x32_bf16 v[36:39], a[116:119], v[180:183], v[36:39]\n"
    "ds_read_b64_tr_b16 a[106:107], v234 offset:4608\n"
    "v_cvt_pk_bf16_f32 v203, v154, v155\n"
    "v_mfma_f32_16x16x32_bf16 v[36:39], a[112:115], v[164:167], v[36:39]\n"
    "v_exp_f32 v148, v148\n"
    "v_mfma_f32_16x16x32_bf16 v[40:43], a[112:115], v[168:171], v[40:43]\n"
    "v_exp_f32 v149, v149\n"
    "v_mfma_f32_16x16x32_bf16 v[40:43], a[116:119], v[184:187], v[40:43]\n"
    "v_exp_f32 v150, v150\n"
    "v_mfma_f32_16x16x32_bf16 v[44:47], a[116:119], v[188:191], v[44:47]\n"
    "v_exp_f32 v151, v151\n"
    "v_mfma_f32_16x16x32_bf16 v[44:47], a[112:115], v[172:175], v[44:47]\n"
    "ds_read_b64_tr_b16 a[108:109], v234 offset:8704\n"
    "v_cvt_pk_bf16_f32 v198, v148, v149\n"
    "v_mfma_f32_16x16x32_bf16 v[60:63], a[120:123], v[172:175], v[60:63]\n"
    "ds_read_b64_tr_b16 a[110:111], v234 offset:12800\n"
    "v_cvt_pk_bf16_f32 v199, v150, v151\n"
    "v_mfma_f32_16x16x32_bf16 v[60:63], a[124:127], v[188:191], v[60:63]\n"
    "v_exp_f32 v144, v144\n"
    "v_mfma_f32_16x16x32_bf16 v[56:59], a[124:127], v[184:187], v[56:59]\n"
    "v_exp_f32 v145, v145\n"
    "v_mfma_f32_16x16x32_bf16 v[56:59], a[120:123], v[168:171], v[56:59]\n"
    "v_exp_f32 v146, v146\n"
    "v_mfma_f32_16x16x32_bf16 v[52:55], a[120:123], v[164:167], v[52:55]\n"
    "v_exp_f32 v147, v147\n"
    "v_mfma_f32_16x16x32_bf16 v[52:55], a[124:127], v[180:183], v[52:55]\n"
    "v_cvt_pk_bf16_f32 v194, v144, v145\n"
    "v_cvt_pk_bf16_f32 v195, v146, v147\n"
    "v_mfma_f32_16x16x32_bf16 v[48:51], a[124:127], v[176:179], v[48:51]\n"
    "v_mfma_f32_16x16x32_bf16 v[48:51], a[120:123], v[160:163], v[48:51]\n"
    "s_waitcnt lgkmcnt(8)\n"
    "v_mfma_f32_16x16x32_bf16 v[128:131], a[64:67], a[0:3], v[112:115]\n"
    "ds_read_b128 a[216:219], v243 offset:8192\n"
    "v_mfma_f32_16x16x32_bf16 v[128:131], a[68:71], a[4:7], v[128:131]\n"
    "ds_read_b128 a[220:223], v244 offset:9216\n"
    "v_mfma_f32_16x16x32_bf16 v[128:131], a[72:75], a[8:11], v[128:131]\n"
    "ds_read_b128 a[224:227], v243 offset:10240\n"
    "v_mfma_f32_16x16x32_bf16 v[128:131], a[76:79], a[12:15], v[128:131]\n"
    "ds_read_b128 a[228:231], v244 offset:11264\n"
    "v_mfma_f32_16x16x32_bf16 v[132:135], a[76:79], a[28:31], v[116:119]\n"
    "ds_read_b128 a[80:83], v232 offset:12288\n"
    "v_mfma_f32_16x16x32_bf16 v[132:135], a[72:75], a[24:27], v[132:135]\n"
    "ds_read_b128 a[84:87], v232 offset:12544\n"
    "v_mfma_f32_16x16x32_bf16 v[132:135], a[68:71], a[20:23], v[132:135]\n"
    "ds_read_b128 a[88:91], v232 offset:12800\n"
    "v_mfma_f32_16x16x32_bf16 v[132:135], a[64:67], a[16:19], v[132:135]\n"
    "v_exp_f32 v128, v128\n"
    "v_mfma_f32_16x16x32_bf16 v[136:139], a[64:67], a[32:35], v[120:123]\n"
    "v_exp_f32 v129, v129\n"
    "v_mfma_f32_16x16x32_bf16 v[136:139], a[68:71], a[36:39], v[136:139]\n"
    "v_exp_f32 v130, v130\n"
    "v_mfma_f32_16x16x32_bf16 v[136:139], a[72:75], a[40:43], v[136:139]\n"
    "v_exp_f32 v131, v131\n"
    "v_mfma_f32_16x16x32_bf16 v[136:139], a[76:79], a[44:47], v[136:139]\n"
    "ds_read_b128 a[92:95], v232 offset:13056\n"
    "v_cvt_pk_bf16_f32 v208, v128, v129\n"
    "v_mfma_f32_16x16x32_bf16 v[140:143], a[76:79], a[60:63], v[124:127]\n"
    "ds_read_b64_tr_b16 a[112:113], v233 offset:768\n"
    "v_cvt_pk_bf16_f32 v209, v130, v131\n"
    "v_mfma_f32_16x16x32_bf16 v[140:143], a[72:75], a[56:59], v[140:143]\n"
    "v_exp_f32 v132, v132\n"
    "v_mfma_f32_16x16x32_bf16 v[140:143], a[68:71], a[52:55], v[140:143]\n"
    "v_exp_f32 v133, v133\n"
    "v_mfma_f32_16x16x32_bf16 v[140:143], a[64:67], a[48:51], v[140:143]\n"
    "ds_read_b64_tr_b16 a[114:115], v233 offset:4864\n"
    "s_waitcnt lgkmcnt(10)\n"
    "v_mfma_f32_16x16x32_bf16 v[64:67], a[96:99], v[160:163], v[64:67]\n"
    "ds_read_b64_tr_b16 a[116:117], v233 offset:8960\n"
    "v_mfma_f32_16x16x32_bf16 v[64:67], a[100:103], v[176:179], v[64:67]\n"
    "ds_read_b64_tr_b16 a[118:119], v233 offset:13056\n"
    "v_mfma_f32_16x16x32_bf16 v[68:71], a[100:103], v[180:183], v[68:71]\n"
    "ds_read_b64_tr_b16 a[120:121], v234 offset:768\n"
    "v_mfma_f32_16x16x32_bf16 v[68:71], a[96:99], v[164:167], v[68:71]\n"
    "ds_read_b64_tr_b16 a[122:123], v234 offset:4864\n"
    "v_mfma_f32_16x16x32_bf16 v[72:75], a[96:99], v[168:171], v[72:75]\n"
    "ds_read_b64_tr_b16 a[124:125], v234 offset:8960\n"
    "v_mfma_f32_16x16x32_bf16 v[72:75], a[100:103], v[184:187], v[72:75]\n"
    "ds_read_b64_tr_b16 a[126:127], v234 offset:13056\n"
    "s_memrealtime s[90:91]\n"
    "v_mfma_f32_16x16x32_bf16 v[76:79], a[100:103], v[188:191], v[76:79]\n"
    "v_exp_f32 v134, v134\n"
    "v_mfma_f32_16x16x32_bf16 v[76:79], a[96:99], v[172:175], v[76:79]\n"
    "v_exp_f32 v135, v135\n"
    "v_mfma_f32_16x16x32_bf16 v[92:95], a[104:107], v[172:175], v[92:95]\n"
    "v_cvt_pk_bf16_f32 v212, v132, v133\n"
    "v_cvt_pk_bf16_f32 v213, v134, v135\n"
    "v_mfma_f32_16x16x32_bf16 v[92:95], a[108:111], v[188:191], v[92:95]\n"
    "v_exp_f32 v136, v136\n"
    "v_mfma_f32_16x16x32_bf16 v[88:91], a[108:111], v[184:187], v[88:91]\n"
    "v_exp_f32 v137, v137\n"
    "v_mfma_f32_16x16x32_bf16 v[88:91], a[104:107], v[168:171], v[88:91]\n"
    "v_exp_f32 v138, v138\n"
    "v_mfma_f32_16x16x32_bf16 v[84:87], a[104:107], v[164:167], v[84:87]\n"
    "v_exp_f32 v139, v139\n"
    "v_mfma_f32_16x16x32_bf16 v[84:87], a[108:111], v[180:183], v[84:87]\n"
    "v_cvt_pk_bf16_f32 v216, v136, v137\n"
    "v_cvt_pk_bf16_f32 v217, v138, v139\n"
    "v_mfma_f32_16x16x32_bf16 v[80:83], a[108:111], v[176:179], v[80:83]\n"
    "v_exp_f32 v140, v140\n"
    "v_mfma_f32_16x16x32_bf16 v[80:83], a[104:107], v[160:163], v[80:83]\n"
    "v_exp_f32 v141, v141\n"
    "v_add_u32 v233, s60, v236\n"
    "s_add_u32 s60, s60, 16384\n"
    "s_cmp_eq_u32 s60, 49152\n"
    "s_cselect_b32 s60, 0, s60\n"
    "v_xor_b32 v234, 32, v233\n"
    "v_add_u32 v232, s60, v235\n"
    "s_waitcnt vmcnt(12) lgkmcnt(0)\n"
    "s_sub_u32 s90, s90, s73\n"
    "s_cmp_ge_i32 s90, 0\n"
    "s_cbranch_scc1 .Lpace3x\n"
    ".Lpace3:\n"
    "s_sleep 1\n"
    "s_memrealtime s[90:91]\n"
    "s_waitcnt lgkmcnt(0)\n"
    "s_sub_u32 s90, s90, s73\n"
    "s_cmp_lt_i32 s90, 8\n"
    "s_cbranch_scc1 .Lpace3\n"
    ".Lpace3x:\n"
    "s_add_u32 s73, s73, s88\n"
    "s_barrier\n"
    "v_mfma_f32_16x16x32_bf16 v[156:159], a[80:83], a[48:51], v[124:127]\n"
    "v_exp_f32 v142, v142\n"
    "v_mfma_f32_16x16x32_bf16 v[156:159], a[84:87], a[52:55], v[156:159]\n"
    "s_add_u32 m0, s70, s58\n"
    "v_exp_f32 v143, v143\n"
    "v_mfma_f32_16x16x32_bf16 v[156:159], a[88:91], a[56:59], v[156:159]\n"
    "ds_read_b128 a[64:67], v232 offset:0\n"
    "v_cvt_pk_bf16_f32 v220, v140, v141\n"
    "v_mfma_f32_16x16x32_bf16 v[156:159], a[92:95], a[60:63], v[156:159]\n"
    "ds_read_b128 a[68:71], v232 offset:256\n"
    "v_cvt_pk_bf16_f32 v221, v142, v143\n"
    "v_mfma_f32_16x16x32_bf16 v[152:155], a[92:95], a[44:47], v[120:123]\n"
    "buffer_load_dwordx4 v237, s[40:43], s56 offen offset:0 nt lds\n"
    "v_mfma_f32_16x16x32_bf16 v[152:155], a[88:91], a[40:43], v[152:155]\n"
    "ds_read_b128 a[72:75], v232 offset:512\n"
    "v_mfma_f32_16x16x32_bf16 v[152:155], a[84:87], a[36:39], v[152:155]\n"
    "ds_read_b128 a[76:79], v232 offset:768\n"
    "v_mfma_f32_16x16x32_bf16 v[152:155], a[80:83], a[32:35], v[152:155]\n"
    "v_exp_f32 v156, v156\n"
    "v_mfma_f32_16x16x32_bf16 v[148:151], a[80:83], a[16:19], v[116:119]\n"
    "v_exp_f32 v157, v157\n"
    "v_mfma_f32_16x16x32_bf16 v[148:151], a[84:87], a[20:23], v[148:151]\n"
    "v_exp_f32 v158, v158\n"
    "v_mfma_f32_16x16x32_bf16 v[148:151], a[88:91], a[24:27], v[148:151]\n"
    "v_exp_f32 v159, v159\n"
    "v_mfma_f32_16x16x32_bf16 v[148:151], a[92:95], a[28:31], v[148:151]\n"
    "ds_read_b64_tr_b16 a[96:97], v233 offset:0\n"
    "v_cvt_pk_bf16_f32 v222, v156, v157\n"
    "v_mfma_f32_16x16x32_bf16 v[144:147], a[92:95], a[12:15], v[112:115]\n"
    "ds_read_b64_tr_b16 a[98:99], v233 offset:4096\n"
    "v_cvt_pk_bf16_f32 v223, v158, v159\n"
    "v_mfma_f32_16x16x32_bf16 v[144:147], a[88:91], a[8:11], v[144:147]\n"
    "v_exp_f32 v152, v152\n"
    "v_mfma_f32_16x16x32_bf16 v[144:147], a[84:87], a[4:7], v[144:147]\n"
    "v_exp_f32 v153, v153\n"
    "v_mfma_f32_16x16x32_bf16 v[144:147], a[80:83], a[0:3], v[144:147]\n"
    "v_exp_f32 v154, v154\n"
    "v_mfma_f32_16x16x32_bf16 v[96:99], a[112:115], v[160:163], v[96:99]\n"
    "ds_read_b64_tr_b16 a[100:101], v233 offset:8192\n"
    "v_mfma_f32_16x16x32_bf16 v[96:99], a[116:119], v[176:179], v[96:99]\n"
    "ds_read_b64_tr_b16 a[102:103], v233 offset:12288\n"
    "v_mfma_f32_16x16x32_bf16 v[100:103], a[116:119], v[180:183], v[100:103]\n"
    "ds_read_b64_tr_b16 a[104:105], v234 offset:0\n"
    "v_mfma_f32_16x16x32_bf16 v[100:103], a[112:115], v[164:167], v[100:103]\n"
    "ds_read_b64_tr_b16 a[106:107], v234 offset:4096\n"
    "v_mfma_f32_16x16x32_bf16 v[104:107], a[112:115], v[168:171], v[104:107]\n"
    "ds_read_b64_tr_b16 a[108:109], v234 offset:8192\n"
    "v_mfma_f32_16x16x32_bf16 v[104:107], a[116:119], v[184:187], v[104:107]\n"
    "ds_read_b64_tr_b16 a[110:111], v234 offset:12288\n"
    "v_mfma_f32_16x16x32_bf16 v[108:111], a[116:119], v[188:191], v[108:111]\n"
    "v_exp_f32 v155, v155\n"
    "v_mfma_f32_16x16x32_bf16 v[108:111], a[112:115], v[172:175], v[108:111]\n"
    "v_cvt_pk_bf16_f32 v218, v152, v153\n"
    "v_cvt_pk_bf16_f32 v219, v154, v155\n"
    "v_mfma_f32_16x16x32_bf16 a[180:183], a[120:123], v[172:175], a[180:183]\n"
    "v_exp_f32 v148, v148\n"
    "v_mfma_f32_16x16x32_bf16 a[180:183], a[124:127], v[188:191], a[180:183]\n"
    "v_exp_f32 v149, v149\n"
    "v_mfma_f32_16x16x32_bf16 a[176:179], a[124:127], v[184:187], a[176:179]\n"
    "v_exp_f32 v150, v150\n"
    "v_mfma_f32_16x16x32_bf16 a[176:179], a[120:123], v[168:171], a[176:179]\n"
    "buffer_load_dwordx4 v238, s[40:43], s56 offen offset:1024 nt lds\n"
    "v_mfma_f32_16x16x32_bf16 a[172:175], a[120:123], v[164:167], a[172:175]\n"
    "v_exp_f32 v151, v151\n"
    "v_mfma_f32_16x16x32_bf16 a[172:175], a[124:127], v[180:183], a[172:175]\n"
    "v_cvt_pk_bf16_f32 v214, v148, v149\n"
    "v_cvt_pk_bf16_f32 v215, v150, v151\n"
    "v_mfma_f32_16x16x32_bf16 a[168:171], a[124:127], v[176:179], a[168:171]\n"
    "buffer_load_dwordx4 v239, s[40:43], s56 offen offset:2048 nt lds\n"
    "v_mfma_f32_16x16x32_bf16 a[168:171], a[120:123], v[160:163], a[168:171]\n"
    "buffer_load_dwordx4 v240, s[40:43], s56 offen offset:3072 nt lds\n"
    "s_add_u32 s56, s56, 16384\n"
    "s_add_u32 s58, s58, 16384\n"
    "s_cmp_eq_u32 s58, 49152\n"
    "s_cselect_b32 s58, 0, s58\n"
    "s_cmp_eq_u32 s36, 3\n"
    "s_cbranch_scc1 .Ltail\n"
    "s_waitcnt lgkmcnt(0)\n"
    "v_mfma_f32_16x16x32_bf16 v[128:131], a[64:67], a[0:3], v[112:115]\n"
    "v_exp_f32 v144, v144\n"
    "v_mfma_f32_16x16x32_bf16 v[128:131], a[68:71], a[4:7], v[128:131]\n"
    "s_add_u32 m0, s62, 8192\n"
    "s_add_u32 s37, s63, 8192\n"
    "v_exp_f32 v145, v145\n"
    "v_mfma_f32_16x16x32_bf16 v[128:131], a[72:75], a[8:11], v[128:131]\n"
    "v_exp_f32 v146, v146\n"
    "v_mfma_f32_16x16x32_bf16 v[128:131], a[76:79], a[12:15], v[128:131]\n"
    "v_exp_f32 v147, v147\n"
    "v_mfma_f32_16x16x32_bf16 v[132:135], a[76:79], a[28:31], v[116:119]\n"
    "ds_read_b128 a[80:83], v232 offset:4096\n"
    "v_cvt_pk_bf16_f32 v210, v144, v145\n"
    "v_mfma_f32_16x16x32_bf16 v[132:135], a[72:75], a[24:27], v[132:135]\n"
    "ds_read_b128 a[84:87], v232 offset:4352\n"
    "v_cvt_pk_bf16_f32 v211, v146, v147\n"
    "v_mfma_f32_16x16x32_bf16 v[132:135], a[68:71], a[20:23], v[132:135]\n"
    "buffer_load_dwordx4 v237, s[48:51], s37 offen offset:0 nt lds\n"
    "v_mfma_f32_16x16x32_bf16 v[132:135], a[64:67], a[16:19], v[132:135]\n"
    "buffer_load_dwordx4 v238, s[48:51], s37 offen offset:1024 nt lds\n"
    "v_mfma_f32_16x16x32_bf16 v[136:139], a[64:67], a[32:35], v[120:123]\n"
    "v_exp_f32 v128, v128\n"
    "v_mfma_f32_16x16x32_bf16 v[136:139], a[68:71], a[36:39], v[136:139]\n"
    "v_exp_f32 v129, v129\n"
    "v_mfma_f32_16x16x32_bf16 v[136:139], a[72:75], a[40:43], v[136:139]\n"
    "v_exp_f32 v130, v130\n"
    "v_mfma_f32_16x16x32_bf16 v[136:139], a[76:79], a[44:47], v[136:139]\n"
    "v_exp_f32 v131, v131\n"
    "v_mfma_f32_16x16x32_bf16 v[140:143], a[76:79], a[60:63], v[124:127]\n"
    "ds_read_b128 a[88:91], v232 offset:4608\n"
    "v_cvt_pk_bf16_f32 v160, v128, v129\n"
    "v_mfma_f32_16x16x32_bf16 v[140:143], a[72:75], a[56:59], v[140:143]\n"
    "ds_read_b128 a[92:95], v232 offset:4864\n"
    "v_cvt_pk_bf16_f32 v161, v130, v131\n"
    "v_mfma_f32_16x16x32_bf16 v[140:143], a[68:71], a[52:55], v[140:143]\n"
    "buffer_load_dwordx4 v239, s[48:51], s37 offen offset:2048 nt lds\n"
    "v_mfma_f32_16x16x32_bf16 v[140:143], a[64:67], a[48:51], v[140:143]\n"
    "v_exp_f32 v132, v132\n"
    "v_mfma_f32_16x16x32_bf16 a[128:131], a[144:147], v[192:195], a[128:131]\n"
    "v_exp_f32 v133, v133\n"
    "v_mfma_f32_16x16x32_bf16 a[128:131], a[144:147], v[208:211], a[128:131]\n"
    "v_exp_f32 v134, v134\n"
    "v_mfma_f32_16x16x32_bf16 a[132:135], a[144:147], v[196:199], a[132:135]\n"
    "v_exp_f32 v135, v135\n"
    "v_mfma_f32_16x16x32_bf16 a[132:135], a[144:147], v[212:215], a[132:135]\n"
    "ds_read_b64_tr_b16 a[112:113], v233 offset:256\n"
    "v_cvt_pk_bf16_f32 v164, v132, v133\n"
    "v_mfma_f32_16x16x32_bf16 a[136:139], a[144:147], v[200:203], a[136:139]\n"
    "ds_read_b64_tr_b16 a[114:115], v233 offset:4352\n"
    "v_cvt_pk_bf16_f32 v165, v134, v135\n"
    "v_mfma_f32_16x16x32_bf16 a[136:139], a[144:147], v[216:219], a[136:139]\n"
    "buffer_load_dwordx4 v240, s[48:51], s37 offen offset:3072 nt lds\n"
    "v_mfma_f32_16x16x32_bf16 a[140:143], a[144:147], v[204:207], a[140:143]\n"
    "v_exp_f32 v136, v136\n"
    "v_mfma_f32_16x16x32_bf16 a[140:143], a[144:147], v[220:223], a[140:143]\n"
    "v_exp_f32 v137, v137\n"
    "v_mfma_f32_16x16x32_bf16 v[0:3], a[96:99], v[192:195], v[0:3]\n"
    "v_exp_f32 v138, v138\n"
    "v_mfma_f32_16x16x32_bf16 v[0:3], a[100:103], v[208:211], v[0:3]\n"
    "s_add_u32 m0, s71, s59\n"
    "s_nop 0\n"
    "buffer_load_dwordx4 v237, s[44:47], s57 offen offset:0 lds\n"
    "v_mfma_f32_16x16x32_bf16 v[4:7], a[100:103], v[212:215], v[4:7]\n"
    "v_exp_f32 v139, v139\n"
    "v_mfma_f32_16x16x32_bf16 v[4:7], a[96:99], v[196:199], v[4:7]\n"
    "ds_read_b64_tr_b16 a[116:117], v233 offset:8448\n"
    "v_cvt_pk_bf16_f32 v168, v136, v137\n"
    "v_mfma_f32_16x16x32_bf16 v[8:11], a[96:99], v[200:203], v[8:11]\n"
    "ds_read_b64_tr_b16 a[118:119], v233 offset:12544\n"
    "v_cvt_pk_bf16_f32 v169, v138, v139\n"
    "v_mfma_f32_16x16x32_bf16 v[8:11], a[100:103], v[216:219], v[8:11]\n"
    "v_exp_f32 v140, v140\n"
    "v_mfma_f32_16x16x32_bf16 v[12:15], a[100:103], v[220:223], v[12:15]\n"
    "v_exp_f32 v141, v141\n"
    "v_mfma_f32_16x16x32_bf16 v[12:15], a[96:99], v[204:207], v[12:15]\n"
    "buffer_load_dwordx4 v238, s[44:47], s57 offen offset:1024 lds\n"
    "v_mfma_f32_16x16x32_bf16 v[28:31], a[104:107], v[204:207], v[28:31]\n"
    "v_exp_f32 v142, v142\n"
    "v_mfma_f32_16x16x32_bf16 v[28:31], a[108:111], v[220:223], v[28:31]\n"
    "v_exp_f32 v143, v143\n"
    "v_mfma_f32_16x16x32_bf16 v[24:27], a[108:111], v[216:219], v[24:27]\n"
    "ds_read_b64_tr_b16 a[120:121], v234 offset:256\n"
    "v_cvt_pk_bf16_f32 v172, v140, v141\n"
    "v_mfma_f32_16x16x32_bf16 v[24:27], a[104:107], v[200:203], v[24:27]\n"
    "ds_read_b64_tr_b16 a[122:123], v234 offset:4352\n"
    "v_cvt_pk_bf16_f32 v173, v142, v143\n"
    "v_mfma_f32_16x16x32_bf16 v[20:23], a[104:107], v[196:199], v[20:23]\n"
    "ds_read_b64_tr_b16 a[124:125], v234 offset:8448\n"
    "v_mfma_f32_16x16x32_bf16 v[20:23], a[108:111], v[212:215], v[20:23]\n"
    "ds_read_b64_tr_b16 a[126:127], v234 offset:12544\n"
    "v_mfma_f32_16x16x32_bf16 v[16:19], a[108:111], v[208:211], v[16:19]\n"
    "buffer_load_dwordx4 v239, s[44:47], s57 offen offset:2048 lds\n"
    "v_mfma_f32_16x16x32_bf16 v[16:19], a[104:107], v[192:195], v[16:19]\n"
    "ds_read_b128 a[152:155], v253 offset:0\n"
    "s_waitcnt lgkmcnt(9)\n"
    "v_mfma_f32_16x16x32_bf16 v[156:159], a[80:83], a[48:51], v[124:127]\n"
    "ds_read_b128 a[156:159], v253 offset:256\n"
    "v_mfma_f32_16x16x32_bf16 v[156:159], a[84:87], a[52:55], v[156:159]\n"
    "ds_read_b128 a[160:163], v246 offset:0\n"
    "v_mfma_f32_16x16x32_bf16 v[156:159], a[88:91], a[56:59], v[156:159]\n"
    "ds_read_b128 a[164:167], v246 offset:256\n"
    "v_mfma_f32_16x16x32_bf16 v[156:159], a[92:95], a[60:63], v[156:159]\n"
    "buffer_load_dwordx4 v240, s[44:47], s57 offen offset:3072 lds\n"
    "v_mfma_f32_16x16x32_bf16 v[152:155], a[92:95], a[44:47], v[120:123]\n"
    "ds_read_b128 a[64:67], v232 offset:8192\n"
    "v_mfma_f32_16x16x32_bf16 v[152:155], a[88:91], a[40:43], v[152:155]\n"
    "ds_read_b128 a[68:71], v232 offset:8448\n"
    "v_mfma_f32_16x16x32_bf16 v[152:155], a[84:87], a[36:39], v[152:155]\n"
    "ds_read_b128 a[72:75], v232 offset:8704\n"
    "v_mfma_f32_16x16x32_bf16 v[152:155], a[80:83], a[32:35], v[152:155]\n"
    "v_exp_f32 v156, v156\n"
    "v_mfma_f32_16x16x32_bf16 v[148:151], a[80:83], a[16:19], v[116:119]\n"
    "v_exp_f32 v157, v157\n"
    "v_mfma_f32_16x16x32_bf16 v[148:151], a[84:87], a[20:23], v[148:151]\n"
    "s_add_u32 s57, s57, 16384\n"
    "s_add_u32 s59, s59, 16384\n"
    "s_cmp_eq_u32 s59, 49152\n"
    "s_cselect_b32 s59, 0, s59\n"
    "v_exp_f32 v158, v158\n"
    "v_mfma_f32_16x16x32_bf16 v[148:151], a[88:91], a[24:27], v[148:151]\n"
    "v_exp_f32 v159, v159\n"
    "v_mfma_f32_16x16x32_bf16 v[148:151], a[92:95], a[28:31], v[148:151]\n"
    "ds_read_b128 a[76:79], v232 offset:8960\n"
    "v_cvt_pk_bf16_f32 v174, v156, v157\n"
    "v_mfma_f32_16x16x32_bf16 v[144:147], a[92:95], a[12:15], v[112:115]\n"
    "ds_read_b64_tr_b16 a[96:97], v233 offset:512\n"
    "v_cvt_pk_bf16_f32 v175, v158, v159\n"
    "v_mfma_f32_16x16x32_bf16 v[144:147], a[88:91], a[8:11], v[144:147]\n"
    "v_exp_f32 v152, v152\n"
    "v_mfma_f32_16x16x32_bf16 v[144:147], a[84:87], a[4:7], v[144:147]\n"
    "v_exp_f32 v153, v153\n"
    "v_mfma_f32_16x16x32_bf16 v[144:147], a[80:83], a[0:3], v[144:147]\n"
    "v_exp_f32 v154, v154\n"
    "s_waitcnt lgkmcnt(9)\n"
    "v_mfma_f32_16x16x32_bf16 v[32:35], a[112:115], v[192:195], v[32:35]\n"
    "v_exp_f32 v155, v155\n"
    "v_mfma_f32_16x16x32_bf16 v[32:35], a[116:119], v[208:211], v[32:35]\n"
    "ds_read_b64_tr_b16 a[98:99], v233 offset:4608\n"
    "v_cvt_pk_bf16_f32 v170, v152, v153\n"
    "s_waitcnt lgkmcnt(9)\n"
    "v_mfma_f32_16x16x32_bf16 v[224:227], a[148:151], a[152:155], 0\n"
    "ds_read_b64_tr_b16 a[100:101], v233 offset:8704\n"
    "v_cvt_pk_bf16_f32 v171, v154, v155\n"
    "v_mfma_f32_16x16x32_bf16 v[36:39], a[116:119], v[212:215], v[36:39]\n"
    "v_exp_f32 v148, v148\n"
    "v_mfma_f32_16x16x32_bf16 v[36:39], a[112:115], v[196:199], v[36:39]\n"
    "v_exp_f32 v149, v149\n"
    "s_waitcnt lgkmcnt(9)\n"
    "v_mfma_f32_16x16x32_bf16 v[228:231], a[148:151], a[156:159], 0\n"
    "v_exp_f32 v150, v150\n"
    "v_mfma_f32_16x16x32_bf16 v[40:43], a[112:115], v[200:203], v[40:43]\n"
    "v_cvt_pk_bf16_f32 v224, v224, v225\n"
    "v_cvt_pk_bf16_f32 v225, v226, v227\n"
    "v_mfma_f32_16x16x32_bf16 v[40:43], a[116:119], v[216:219], v[40:43]\n"
    "v_exp_f32 v151, v151\n"
    "v_mfma_f32_16x16x32_bf16 v[44:47], a[116:119], v[220:223], v[44:47]\n"
    "ds_read_b64_tr_b16 a[102:103], v233 offset:12800\n"
    "v_cvt_pk_bf16_f32 v166, v148, v149\n"
    "v_mfma_f32_16x16x32_bf16 v[44:47], a[112:115], v[204:207], v[44:47]\n"
    "v_cvt_pk_bf16_f32 v228, v228, v229\n"
    "v_cvt_pk_bf16_f32 v229, v230, v231\n"
    "v_mfma_f32_16x16x32_bf16 v[60:63], a[120:123], v[204:207], v[60:63]\n"
    "ds_write2_b64 v247, v[224:225], v[228:229] offset0:0 offset1:32\n"
    "v_mfma_f32_16x16x32_bf16 v[60:63], a[124:127], v[220:223], v[60:63]\n"
    "ds_read_b64_tr_b16 a[104:105], v234 offset:512\n"
    "v_cvt_pk_bf16_f32 v167, v150, v151\n"
    "s_waitcnt lgkmcnt(11)\n"
    "v_mfma_f32_16x16x32_bf16 v[224:227], a[148:151], a[160:163], 0\n"
    "v_exp_f32 v144, v144\n"
    "v_mfma_f32_16x16x32_bf16 v[56:59], a[124:127], v[216:219], v[56:59]\n"
    "v_exp_f32 v145, v145\n"
    "v_mfma_f32_16x16x32_bf16 v[56:59], a[120:123], v[200:203], v[56:59]\n"
    "v_exp_f32 v146, v146\n"
    "s_waitcnt lgkmcnt(10)\n"
    "v_mfma_f32_16x16x32_bf16 v[228:231], a[148:151], a[164:167], 0\n"
    "ds_read_b64_tr_b16 a[106:107], v234 offset:4608\n"
    "v_mfma_f32_16x16x32_bf16 v[52:55], a[120:123], v[196:199], v[52:55]\n"
    "ds_read_b64_tr_b16 a[108:109], v234 offset:8704\n"
    "v_cvt_pk_bf16_f32 v224, v224, v225\n"
    "v_mfma_f32_16x16x32_bf16 v[52:55], a[124:127], v[212:215], v[52:55]\n"
    "ds_read_b64_tr_b16 a[110:111], v234 offset:12800\n"
    "v_cvt_pk_bf16_f32 v225, v226, v227\n"
    "v_mfma_f32_16x16x32_bf16 v[48:51], a[124:127], v[208:211], v[48:51]\n"
    "v_exp_f32 v147, v147\n"
    "v_mfma_f32_16x16x32_bf16 v[48:51], a[120:123], v[192:195], v[48:51]\n"
    "ds_read_b128 a[152:155], v253 offset:512\n"
    "v_cvt_pk_bf16_f32 v228, v228, v229\n"
    "s_waitcnt lgkmcnt(10)\n"
    "v_mfma_f32_16x16x32_bf16 v[128:131], a[64:67], a[0:3], v[112:115]\n"
    "ds_read_b128 a[156:159], v253 offset:768\n"
    "v_cvt_pk_bf16_f32 v229, v230, v231\n"
    "v_mfma_f32_16x16x32_bf16 v[128:131], a[68:71], a[4:7], v[128:131]\n"
    "ds_read_b128 a[160:163], v246 offset:512\n"
    "v_cvt_pk_bf16_f32 v162, v144, v145\n"
    "v_mfma_f32_16x16x32_bf16 v[128:131], a[72:75], a[8:11], v[128:131]\n"
    "ds_read_b128 a[164:167], v246 offset:768\n"
    "v_cvt_pk_bf16_f32 v163, v146, v147\n"
    "v_mfma_f32_16x16x32_bf16 v[128:131], a[76:79], a[12:15], v[128:131]\n"
    "ds_write2_b64 v248, v[224:225], v[228:229] offset0:0 offset1:32\n"
    "v_mfma_f32_16x16x32_bf16 v[132:135], a[76:79], a[28:31], v[116:119]\n"
    "ds_read_b128 a[184:187], v243 offset:12288\n"
    "v_mfma_f32_16x16x32_bf16 v[132:135], a[72:75], a[24:27], v[132:135]\n"
    "ds_read_b128 a[188:191], v244 offset:13312\n"
    "v_mfma_f32_16x16x32_bf16 v[132:135], a[68:71], a[20:23], v[132:135]\n"
    "ds_read_b128 a[192:195], v243 offset:14336\n"
    "v_mfma_f32_16x16x32_bf16 v[132:135], a[64:67], a[16:19], v[132:135]\n"
    "v_exp_f32 v128, v128\n"
    "v_mfma_f32_16x16x32_bf16 v[136:139], a[64:67], a[32:35], v[120:123]\n"
    "v_exp_f32 v129, v129\n"
    "v_mfma_f32_16x16x32_bf16 v[136:139], a[68:71], a[36:39], v[136:139]\n"
    "v_exp_f32 v130, v130\n"
    "v_mfma_f32_16x16x32_bf16 v[136:139], a[72:75], a[40:43], v[136:139]\n"
    "v_exp_f32 v131, v131\n"
    "v_mfma_f32_16x16x32_bf16 v[136:139], a[76:79], a[44:47], v[136:139]\n"
    "ds_read_b128 a[196:199], v244 offset:15360\n"
    "v_cvt_pk_bf16_f32 v176, v128, v129\n"
    "v_mfma_f32_16x16x32_bf16 v[140:143], a[76:79], a[60:63], v[124:127]\n"
    "ds_read_b128 a[80:83], v232 offset:12288\n"
    "v_cvt_pk_bf16_f32 v177, v130, v131\n"
    "v_mfma_f32_16x16x32_bf16 v[140:143], a[72:75], a[56:59], v[140:143]\n"
    "v_exp_f32 v132, v132\n"
    "v_mfma_f32_16x16x32_bf16 v[140:143], a[68:71], a[52:55], v[140:143]\n"
    "v_exp_f32 v133, v133\n"
    "v_mfma_f32_16x16x32_bf16 v[140:143], a[64:67], a[48:51], v[140:143]\n"
    "ds_read_b128 a[84:87], v232 offset:12544\n"
    "s_waitcnt lgkmcnt(11)\n"
    "v_mfma_f32_16x16x32_bf16 v[64:67], a[96:99], v[192:195], v[64:67]\n"
    "ds_read_b128 a[88:91], v232 offset:12800\n"
    "v_mfma_f32_16x16x32_bf16 v[64:67], a[100:103], v[208:211], v[64:67]\n"
    "ds_read_b128 a[92:95], v232 offset:13056\n"
    "s_waitcnt lgkmcnt(12)\n"
    "v_mfma_f32_16x16x32_bf16 v[224:227], a[148:151], a[152:155], 0\n"
    "ds_read_b64_tr_b16 a[112:113], v233 offset:768\n"
    "v_mfma_f32_16x16x32_bf16 v[68:71], a[100:103], v[212:215], v[68:71]\n"
    "ds_read_b64_tr_b16 a[114:115], v233 offset:4864\n"
    "v_mfma_f32_16x16x32_bf16 v[68:71], a[96:99], v[196:199], v[68:71]\n"
    "ds_read_b64_tr_b16 a[116:117], v233 offset:8960\n"
    "s_waitcnt lgkmcnt(14)\n"
    "v_mfma_f32_16x16x32_bf16 v[228:231], a[148:151], a[156:159], 0\n"
    "ds_read_b64_tr_b16 a[118:119], v233 offset:13056\n"
    "v_mfma_f32_16x16x32_bf16 v[72:75], a[96:99], v[200:203], v[72:75]\n"
    "v_cvt_pk_bf16_f32 v224, v224, v225\n"
    "v_cvt_pk_bf16_f32 v225, v226, v227\n"
    "v_mfma_f32_16x16x32_bf16 v[72:75], a[100:103], v[216:219], v[72:75]\n"
    "ds_read_b64_tr_b16 a[120:121], v234 offset:768\n"
    "v_mfma_f32_16x16x32_bf16 v[76:79], a[100:103], v[220:223], v[76:79]\n"
    "ds_read_b64_tr_b16 a[122:123], v234 offset:4864\n"
    "v_mfma_f32_16x16x32_bf16 v[76:79], a[96:99], v[204:207], v[76:79]\n"
    "v_cvt_pk_bf16_f32 v228, v228, v229\n"
    "v_cvt_pk_bf16_f32 v229, v230, v231\n"
    "s_memrealtime s[90:91]\n"
    "v_mfma_f32_16x16x32_bf16 v[92:95], a[104:107], v[204:207], v[92:95]\n"
    "ds_write2_b64 v247, v[224:225], v[228:229] offset0:64 offset1:96\n"
    "v_mfma_f32_16x16x32_bf16 v[92:95], a[108:111], v[220:223], v[92:95]\n"
    "ds_read_b64_tr_b16 a[124:125], v234 offset:8960\n"
    "ds_read_b64_tr_b16 a[126:127], v234 offset:13056\n"
    "s_waitcnt lgkmcnt(15)\n"
    "v_mfma_f32_16x16x32_bf16 v[224:227], a[148:151], a[160:163], 0\n"
    "v_exp_f32 v134, v134\n"
    "v_mfma_f32_16x16x32_bf16 v[88:91], a[108:111], v[216:219], v[88:91]\n"
    "v_exp_f32 v135, v135\n"
    "v_mfma_f32_16x16x32_bf16 v[88:91], a[104:107], v[200:203], v[88:91]\n"
    "v_cvt_pk_bf16_f32 v180, v132, v133\n"
    "v_cvt_pk_bf16_f32 v181, v134, v135\n"
    "v_mfma_f32_16x16x32_bf16 v[228:231], a[148:151], a[164:167], 0\n"
    "v_exp_f32 v136, v136\n"
    "v_mfma_f32_16x16x32_bf16 v[84:87], a[104:107], v[196:199], v[84:87]\n"
    "v_cvt_pk_bf16_f32 v224, v224, v225\n"
    "v_cvt_pk_bf16_f32 v225, v226, v227\n"
    "v_mfma_f32_16x16x32_bf16 v[84:87], a[108:111], v[212:215], v[84:87]\n"
    "v_exp_f32 v137, v137\n"
    "v_mfma_f32_16x16x32_bf16 v[80:83], a[108:111], v[208:211], v[80:83]\n"
    "v_exp_f32 v138, v138\n"
    "v_mfma_f32_16x16x32_bf16 v[80:83], a[104:107], v[192:195], v[80:83]\n"
    "v_cvt_pk_bf16_f32 v228, v228, v229\n"
    "v_cvt_pk_bf16_f32 v229, v230, v231\n"
    "v_add_u32 v233, s60, v236\n"
    "s_add_u32 s60, s60, 16384\n"
    "s_cmp_eq_u32 s60, 49152\n"
    "s_cselect_b32 s60, 0, s60\n"
    "v_xor_b32 v234, 32, v233\n"
    "v_add_u32 v232, s60, v235\n"
    "s_waitcnt vmcnt(12) lgkmcnt(0)\n"
    "s_sub_u32 s90, s90, s73\n"
    "s_cmp_ge_i32 s90, 0\n"
    "s_cbranch_scc1 .Lpace4x\n"
    ".Lpace4:\n"
    "s_sleep 1\n"
    "s_memrealtime s[90:91]\n"
    "s_waitcnt lgkmcnt(0)\n"
    "s_sub_u32 s90, s90, s73\n"
    "s_cmp_lt_i32 s90, 8\n"
    "s_cbranch_scc1 .Lpace4\n"
    ".Lpace4x:\n"
    "s_add_u32 s73, s73, s88\n"
    "s_barrier\n"
    "v_mfma_f32_16x16x32_bf16 v[156:159], a[80:83], a[48:51], v[124:127]\n"
    "ds_write2_b64 v248, v[224:225], v[228:229] offset0:64 offset1:96\n"
    "v_mfma_f32_16x16x32_bf16 v[156:159], a[84:87], a[52:55], v[156:159]\n"
    "ds_read_b128 a[64:67], v232 offset:0\n"
    "s_add_u32 m0, s70, s58\n"
    "v_exp_f32 v139, v139\n"
    "v_mfma_f32_16x16x32_bf16 v[156:159], a[88:91], a[56:59], v[156:159]\n"
    "ds_read_b128 a[68:71], v232 offset:256\n"
    "v_cvt_pk_bf16_f32 v184, v136, v137\n"
    "v_mfma_f32_16x16x32_bf16 v[156:159], a[92:95], a[60:63], v[156:159]\n"
    "ds_read_b128 a[72:75], v232 offset:512\n"
    "v_cvt_pk_bf16_f32 v185, v138, v139\n"
    "v_mfma_f32_16x16x32_bf16 v[152:155], a[92:95], a[44:47], v[120:123]\n"
    "ds_read_b128 a[76:79], v232 offset:768\n"
    "v_add_u32 v247, 0x1000, v247\n"
    "v_mfma_f32_16x16x32_bf16 v[152:155], a[88:91], a[40:43], v[152:155]\n"
    "ds_read_b64_tr_b16 a[96:97], v233 offset:0\n"
    "v_add_u32 v248, 0x1000, v248\n"
    "v_mfma_f32_16x16x32_bf16 v[152:155], a[84:87], a[36:39], v[152:155]\n"
    "v_exp_f32 v140, v140\n"
    "v_mfma_f32_16x16x32_bf16 v[152:155], a[80:83], a[32:35], v[152:155]\n"
    "v_exp_f32 v141, v141\n"
    "v_mfma_f32_16x16x32_bf16 v[148:151], a[80:83], a[16:19], v[116:119]\n"
    "v_exp_f32 v142, v142\n"
    "v_mfma_f32_16x16x32_bf16 v[148:151], a[84:87], a[20:23], v[148:151]\n"
    "ds_read_b64_tr_b16 a[98:99], v233 offset:4096\n"
    "v_exp_f32 v143, v143\n"
    "v_mfma_f32_16x16x32_bf16 v[148:151], a[88:91], a[24:27], v[148:151]\n"
    "ds_read_b64_tr_b16 a[100:101], v233 offset:8192\n"
    "v_cvt_pk_bf16_f32 v188, v140, v141\n"
    "v_mfma_f32_16x16x32_bf16 v[148:151], a[92:95], a[28:31], v[148:151]\n"
    "ds_read_b64_tr_b16 a[102:103], v233 offset:12288\n"
    "v_cvt_pk_bf16_f32 v189, v142, v143\n"
    "v_mfma_f32_16x16x32_bf16 v[144:147], a[92:95], a[12:15], v[112:115]\n"
    "v_exp_f32 v156, v156\n"
    "v_mfma_f32_16x16x32_bf16 v[144:147], a[88:91], a[8:11], v[144:147]\n"
    "v_exp_f32 v157, v157\n"
    "v_mfma_f32_16x16x32_bf16 v[144:147], a[84:87], a[4:7], v[144:147]\n"
    "v_exp_f32 v158, v158\n"
    "v_mfma_f32_16x16x32_bf16 v[144:147], a[80:83], a[0:3], v[144:147]\n"
    "v_exp_f32 v159, v159\n"
    "v_mfma_f32_16x16x32_bf16 v[96:99], a[112:115], v[192:195], v[96:99]\n"
    "ds_read_b64_tr_b16 a[104:105], v234 offset:0\n"
    "v_cvt_pk_bf16_f32 v190, v156, v157\n"
    "v_mfma_f32_16x16x32_bf16 v[96:99], a[116:119], v[208:211], v[96:99]\n"
    "ds_read_b64_tr_b16 a[106:107], v234 offset:4096\n"
    "v_cvt_pk_bf16_f32 v191, v158, v159\n"
    "v_mfma_f32_16x16x32_bf16 v[100:103], a[116:119], v[212:215], v[100:103]\n"
    "ds_read_b64_tr_b16 a[108:109], v234 offset:8192\n"
    "v_exp_f32 v152, v152\n"
    "v_mfma_f32_16x16x32_bf16 v[100:103], a[112:115], v[196:199], v[100:103]\n"
    "v_exp_f32 v153, v153\n"
    "v_mfma_f32_16x16x32_bf16 v[104:107], a[112:115], v[200:203], v[104:107]\n"
    "v_exp_f32 v154, v154\n"
    "v_mfma_f32_16x16x32_bf16 v[104:107], a[116:119], v[216:219], v[104:107]\n"
    "ds_read_b64_tr_b16 a[110:111], v234 offset:12288\n"
    "v_mfma_f32_16x16x32_bf16 v[108:111], a[116:119], v[220:223], v[108:111]\n"
    "v_exp_f32 v155, v155\n"
    "v_mfma_f32_16x16x32_bf16 v[108:111], a[112:115], v[204:207], v[108:111]\n"
    "v_cvt_pk_bf16_f32 v186, v152, v153\n"
    "v_cvt_pk_bf16_f32 v187, v154, v155\n"
    "v_mfma_f32_16x16x32_bf16 a[180:183], a[120:123], v[204:207], a[180:183]\n"
    "v_exp_f32 v148, v148\n"
    "v_mfma_f32_16x16x32_bf16 a[180:183], a[124:127], v[220:223], a[180:183]\n"
    "v_exp_f32 v149, v149\n"
    "v_mfma_f32_16x16x32_bf16 a[176:179], a[124:127], v[216:219], a[176:179]\n"
    "buffer_load_dwordx4 v237, s[40:43], s56 offen offset:0 nt lds\n"
    "v_mfma_f32_16x16x32_bf16 a[176:179], a[120:123], v[200:203], a[176:179]\n"
    "v_exp_f32 v150, v150\n"
    "v_mfma_f32_16x16x32_bf16 a[172:175], a[120:123], v[196:199], a[172:175]\n"
    "v_exp_f32 v151, v151\n"
    "v_cvt_pk_bf16_f32 v182, v148, v149\n"
    "v_mfma_f32_16x16x32_bf16 a[172:175], a[124:127], v[212:215], a[172:175]\n"
    "v_cvt_pk_bf16_f32 v183, v150, v151\n"
    "v_mfma_f32_16x16x32_bf16 a[168:171], a[124:127], v[208:211], a[168:171]\n"
    "buffer_load_dwordx4 v238, s[40:43], s56 offen offset:1024 nt lds\n"
    "v_mfma_f32_16x16x32_bf16 a[168:171], a[120:123], v[192:195], a[168:171]\n"
    "buffer_load_dwordx4 v239, s[40:43], s56 offen offset:2048 nt lds\n"
    "buffer_load_dwordx4 v240, s[40:43], s56 offen offset:3072 nt lds\n"
    "s_add_u32 s56, s56, 33439744\n"
    "s_add_u32 s58, s58, 16384\n"
    "s_cmp_eq_u32 s58, 49152\n"
    "s_cselect_b32 s58, 0, s58\n"
    "s_waitcnt lgkmcnt(0)\n"
    "v_mfma_f32_16x16x32_bf16 v[128:131], a[64:67], a[0:3], v[112:115]\n"
    "v_exp_f32 v144, v144\n"
    "v_mfma_f32_16x16x32_bf16 v[128:131], a[68:71], a[4:7], v[128:131]\n"
    "s_add_u32 m0, s62, 12288\n"
    "s_add_u32 s37, s63, 12288\n"
    "v_exp_f32 v145, v145\n"
    "v_mfma_f32_16x16x32_bf16 v[128:131], a[72:75], a[8:11], v[128:131]\n"
    "v_exp_f32 v146, v146\n"
    "v_mfma_f32_16x16x32_bf16 v[128:131], a[76:79], a[12:15], v[128:131]\n"
    "v_exp_f32 v147, v147\n"
    "v_mfma_f32_16x16x32_bf16 v[132:135], a[76:79], a[28:31], v[116:119]\n"
    "ds_read_b128 a[80:83], v232 offset:4096\n"
    "v_cvt_pk_bf16_f32 v178, v144, v145\n"
    "v_mfma_f32_16x16x32_bf16 v[132:135], a[72:75], a[24:27], v[132:135]\n"
    "ds_read_b128 a[84:87], v232 offset:4352\n"
    "v_cvt_pk_bf16_f32 v179, v146, v147\n"
    "v_mfma_f32_16x16x32_bf16 v[132:135], a[68:71], a[20:23], v[132:135]\n"
    "buffer_load_dwordx4 v237, s[48:51], s37 offen offset:0 nt lds\n"
    "v_mfma_f32_16x16x32_bf16 v[132:135], a[64:67], a[16:19], v[132:135]\n"
    "buffer_load_dwordx4 v238, s[48:51], s37 offen offset:1024 nt lds\n"
    "v_mfma_f32_16x16x32_bf16 v[136:139], a[64:67], a[32:35], v[120:123]\n"
    "v_exp_f32 v128, v128\n"
    "v_mfma_f32_16x16x32_bf16 v[136:139], a[68:71], a[36:39], v[136:139]\n"
    "v_exp_f32 v129, v129\n"
    "v_mfma_f32_16x16x32_bf16 v[136:139], a[72:75], a[40:43], v[136:139]\n"
    "v_exp_f32 v130, v130\n"
    "v_mfma_f32_16x16x32_bf16 v[136:139], a[76:79], a[44:47], v[136:139]\n"
    "v_exp_f32 v131, v131\n"
    "v_mfma_f32_16x16x32_bf16 v[140:143], a[76:79], a[60:63], v[124:127]\n"
    "ds_read_b128 a[88:91], v232 offset:4608\n"
    "v_cvt_pk_bf16_f32 v192, v128, v129\n"
    "v_mfma_f32_16x16x32_bf16 v[140:143], a[72:75], a[56:59], v[140:143]\n"
    "ds_read_b128 a[92:95], v232 offset:4864\n"
    "v_cvt_pk_bf16_f32 v193, v130, v131\n"
    "v_mfma_f32_16x16x32_bf16 v[140:143], a[68:71], a[52:55], v[140:143]\n"
    "buffer_load_dwordx4 v239, s[48:51], s37 offen offset:2048 nt lds\n"
    "v_mfma_f32_16x16x32_bf16 v[140:143], a[64:67], a[48:51], v[140:143]\n"
    "v_exp_f32 v132, v132\n"
    "v_mfma_f32_16x16x32_bf16 a[128:131], a[144:147], v[160:163], a[128:131]\n"
    "v_exp_f32 v133, v133\n"
    "v_mfma_f32_16x16x32_bf16 a[128:131], a[144:147], v[176:179], a[128:131]\n"
    "v_exp_f32 v134, v134\n"
    "v_mfma_f32_16x16x32_bf16 a[132:135], a[144:147], v[164:167], a[132:135]\n"
    "v_exp_f32 v135, v135\n"
    "v_mfma_f32_16x16x32_bf16 a[132:135], a[144:147], v[180:183], a[132:135]\n"
    "ds_read_b64_tr_b16 a[112:113], v233 offset:256\n"
    "v_cvt_pk_bf16_f32 v196, v132, v133\n"
    "v_mfma_f32_16x16x32_bf16 a[136:139], a[144:147], v[168:171], a[136:139]\n"
    "ds_read_b64_tr_b16 a[114:115], v233 offset:4352\n"
    "v_cvt_pk_bf16_f32 v197, v134, v135\n"
    "v_mfma_f32_16x16x32_bf16 a[136:139], a[144:147], v[184:187], a[136:139]\n"
    "buffer_load_dwordx4 v240, s[48:51], s37 offen offset:3072 nt lds\n"
    "v_mfma_f32_16x16x32_bf16 a[140:143], a[144:147], v[172:175], a[140:143]\n"
    "v_exp_f32 v136, v136\n"
    "v_mfma_f32_16x16x32_bf16 a[140:143], a[144:147], v[188:191], a[140:143]\n"
    "v_exp_f32 v137, v137\n"
    "v_mfma_f32_16x16x32_bf16 v[0:3], a[96:99], v[160:163], v[0:3]\n"
    "v_exp_f32 v138, v138\n"
    "v_mfma_f32_16x16x32_bf16 v[0:3], a[100:103], v[176:179], v[0:3]\n"
    "s_add_u32 m0, s71, s59\n"
    "s_nop 0\n"
    "buffer_load_dwordx4 v237, s[44:47], s57 offen offset:0 lds\n"
    "v_mfma_f32_16x16x32_bf16 v[4:7], a[100:103], v[180:183], v[4:7]\n"
    "v_exp_f32 v139, v139\n"
    "v_mfma_f32_16x16x32_bf16 v[4:7], a[96:99], v[164:167], v[4:7]\n"
    "ds_read_b64_tr_b16 a[116:117], v233 offset:8448\n"
    "v_cvt_pk_bf16_f32 v200, v136, v137\n"
    "v_mfma_f32_16x16x32_bf16 v[8:11], a[96:99], v[168:171], v[8:11]\n"
    "ds_read_b64_tr_b16 a[118:119], v233 offset:12544\n"
    "v_cvt_pk_bf16_f32 v201, v138, v139\n"
    "v_mfma_f32_16x16x32_bf16 v[8:11], a[100:103], v[184:187], v[8:11]\n"
    "v_exp_f32 v140, v140\n"
    "v_mfma_f32_16x16x32_bf16 v[12:15], a[100:103], v[188:191], v[12:15]\n"
    "v_exp_f32 v141, v141\n"
    "v_mfma_f32_16x16x32_bf16 v[12:15], a[96:99], v[172:175], v[12:15]\n"
    "buffer_load_dwordx4 v238, s[44:47], s57 offen offset:1024 lds\n"
    "v_mfma_f32_16x16x32_bf16 v[28:31], a[104:107], v[172:175], v[28:31]\n"
    "v_exp_f32 v142, v142\n"
    "v_mfma_f32_16x16x32_bf16 v[28:31], a[108:111], v[188:191], v[28:31]\n"
    "v_exp_f32 v143, v143\n"
    "v_mfma_f32_16x16x32_bf16 v[24:27], a[108:111], v[184:187], v[24:27]\n"
    "ds_read_b64_tr_b16 a[120:121], v234 offset:256\n"
    "v_cvt_pk_bf16_f32 v204, v140, v141\n"
    "v_mfma_f32_16x16x32_bf16 v[24:27], a[104:107], v[168:171], v[24:27]\n"
    "ds_read_b64_tr_b16 a[122:123], v234 offset:4352\n"
    "v_cvt_pk_bf16_f32 v205, v142, v143\n"
    "v_mfma_f32_16x16x32_bf16 v[20:23], a[104:107], v[164:167], v[20:23]\n"
    "ds_read_b64_tr_b16 a[124:125], v234 offset:8448\n"
    "v_mfma_f32_16x16x32_bf16 v[20:23], a[108:111], v[180:183], v[20:23]\n"
    "ds_read_b64_tr_b16 a[126:127], v234 offset:12544\n"
    "v_mfma_f32_16x16x32_bf16 v[16:19], a[108:111], v[176:179], v[16:19]\n"
    "buffer_load_dwordx4 v239, s[44:47], s57 offen offset:2048 lds\n"
    "v_mfma_f32_16x16x32_bf16 v[16:19], a[104:107], v[160:163], v[16:19]\n"
    "ds_read_b128 a[152:155], v253 offset:4096\n"
    "s_waitcnt lgkmcnt(9)\n"
    "v_mfma_f32_16x16x32_bf16 v[156:159], a[80:83], a[48:51], v[124:127]\n"
    "ds_read_b128 a[156:159], v253 offset:4352\n"
    "v_mfma_f32_16x16x32_bf16 v[156:159], a[84:87], a[52:55], v[156:159]\n"
    "ds_read_b128 a[160:163], v246 offset:4096\n"
    "v_mfma_f32_16x16x32_bf16 v[156:159], a[88:91], a[56:59], v[156:159]\n"
    "ds_read_b128 a[164:167], v246 offset:4352\n"
    "v_mfma_f32_16x16x32_bf16 v[156:159], a[92:95], a[60:63], v[156:159]\n"
    "buffer_load_dwordx4 v240, s[44:47], s57 offen offset:3072 lds\n"
    "v_mfma_f32_16x16x32_bf16 v[152:155], a[92:95], a[44:47], v[120:123]\n"
    "ds_read_b128 a[64:67], v232 offset:8192\n"
    "v_mfma_f32_16x16x32_bf16 v[152:155], a[88:91], a[40:43], v[152:155]\n"
    "ds_read_b128 a[68:71], v232 offset:8448\n"
    "v_mfma_f32_16x16x32_bf16 v[152:155], a[84:87], a[36:39], v[152:155]\n"
    "ds_read_b128 a[72:75], v232 offset:8704\n"
    "v_mfma_f32_16x16x32_bf16 v[152:155], a[80:83], a[32:35], v[152:155]\n"
    "v_exp_f32 v156, v156\n"
    "v_mfma_f32_16x16x32_bf16 v[148:151], a[80:83], a[16:19], v[116:119]\n"
    "v_exp_f32 v157, v157\n"
    "v_mfma_f32_16x16x32_bf16 v[148:151], a[84:87], a[20:23], v[148:151]\n"
    "s_add_u32 s57, s57, 16384\n"
    "s_add_u32 s59, s59, 16384\n"
    "s_cmp_eq_u32 s59, 49152\n"
    "s_cselect_b32 s59, 0, s59\n"
    "v_exp_f32 v158, v158\n"
    "v_mfma_f32_16x16x32_bf16 v[148:151], a[88:91], a[24:27], v[148:151]\n"
    "v_exp_f32 v159, v159\n"
    "v_mfma_f32_16x16x32_bf16 v[148:151], a[92:95], a[28:31], v[148:151]\n"
    "ds_read_b128 a[76:79], v232 offset:8960\n"
    "v_cvt_pk_bf16_f32 v206, v156, v157\n"
    "v_mfma_f32_16x16x32_bf16 v[144:147], a[92:95], a[12:15], v[112:115]\n"
    "ds_read_b64_tr_b16 a[96:97], v233 offset:512\n"
    "v_cvt_pk_bf16_f32 v207, v158, v159\n"
    "v_mfma_f32_16x16x32_bf16 v[144:147], a[88:91], a[8:11], v[144:147]\n"
    "v_exp_f32 v152, v152\n"
    "v_mfma_f32_16x16x32_bf16 v[144:147], a[84:87], a[4:7], v[144:147]\n"
    "v_exp_f32 v153, v153\n"
    "v_mfma_f32_16x16x32_bf16 v[144:147], a[80:83], a[0:3], v[144:147]\n"
    "v_exp_f32 v154, v154\n"
    "s_waitcnt lgkmcnt(9)\n"
    "v_mfma_f32_16x16x32_bf16 v[32:35], a[112:115], v[160:163], v[32:35]\n"
    "v_exp_f32 v155, v155\n"
    "v_mfma_f32_16x16x32_bf16 v[32:35], a[116:119], v[176:179], v[32:35]\n"
    "ds_read_b64_tr_b16 a[98:99], v233 offset:4608\n"
    "v_cvt_pk_bf16_f32 v202, v152, v153\n"
    "s_waitcnt lgkmcnt(9)\n"
    "v_mfma_f32_16x16x32_bf16 v[224:227], a[148:151], a[152:155], 0\n"
    "ds_read_b64_tr_b16 a[100:101], v233 offset:8704\n"
    "v_cvt_pk_bf16_f32 v203, v154, v155\n"
    "v_mfma_f32_16x16x32_bf16 v[36:39], a[116:119], v[180:183], v[36:39]\n"
    "v_exp_f32 v148, v148\n"
    "v_mfma_f32_16x16x32_bf16 v[36:39], a[112:115], v[164:167], v[36:39]\n"
    "v_exp_f32 v149, v149\n"
    "s_waitcnt lgkmcnt(9)\n"
    "v_mfma_f32_16x16x32_bf16 v[228:231], a[148:151], a[156:159], 0\n"
    "v_exp_f32 v150, v150\n"
    "v_mfma_f32_16x16x32_bf16 v[40:43], a[112:115], v[168:171], v[40:43]\n"
    "v_cvt_pk_bf16_f32 v224, v224, v225\n"
    "v_cvt_pk_bf16_f32 v225, v226, v227\n"
    "v_mfma_f32_16x16x32_bf16 v[40:43], a[116:119], v[184:187], v[40:43]\n"
    "v_exp_f32 v151, v151\n"
    "v_mfma_f32_16x16x32_bf16 v[44:47], a[116:119], v[188:191], v[44:47]\n"
    "ds_read_b64_tr_b16 a[102:103], v233 offset:12800\n"
    "v_cvt_pk_bf16_f32 v198, v148, v149\n"
    "v_mfma_f32_16x16x32_bf16 v[44:47], a[112:115], v[172:175], v[44:47]\n"
    "v_cvt_pk_bf16_f32 v228, v228, v229\n"
    "v_cvt_pk_bf16_f32 v229, v230, v231\n"
    "v_mfma_f32_16x16x32_bf16 v[60:63], a[120:123], v[172:175], v[60:63]\n"
    "ds_write2_b64 v247, v[224:225], v[228:229] offset0:0 offset1:32\n"
    "v_mfma_f32_16x16x32_bf16 v[60:63], a[124:127], v[188:191], v[60:63]\n"
    "ds_read_b64_tr_b16 a[104:105], v234 offset:512\n"
    "v_cvt_pk_bf16_f32 v199, v150, v151\n"
    "s_waitcnt lgkmcnt(11)\n"
    "v_mfma_f32_16x16x32_bf16 v[224:227], a[148:151], a[160:163], 0\n"
    "v_exp_f32 v144, v144\n"
    "v_mfma_f32_16x16x32_bf16 v[56:59], a[124:127], v[184:187], v[56:59]\n"
    "v_exp_f32 v145, v145\n"
    "v_mfma_f32_16x16x32_bf16 v[56:59], a[120:123], v[168:171], v[56:59]\n"
    "v_exp_f32 v146, v146\n"
    "s_waitcnt lgkmcnt(10)\n"
    "v_mfma_f32_16x16x32_bf16 v[228:231], a[148:151], a[164:167], 0\n"
    "ds_read_b64_tr_b16 a[106:107], v234 offset:4608\n"
    "v_mfma_f32_16x16x32_bf16 v[52:55], a[120:123], v[164:167], v[52:55]\n"
    "ds_read_b64_tr_b16 a[108:109], v234 offset:8704\n"
    "v_cvt_pk_bf16_f32 v224, v224, v225\n"
    "v_mfma_f32_16x16x32_bf16 v[52:55], a[124:127], v[180:183], v[52:55]\n"
    "ds_read_b64_tr_b16 a[110:111], v234 offset:12800\n"
    "v_cvt_pk_bf16_f32 v225, v226, v227\n"
    "v_mfma_f32_16x16x32_bf16 v[48:51], a[124:127], v[176:179], v[48:51]\n"
    "v_exp_f32 v147, v147\n"
    "v_mfma_f32_16x16x32_bf16 v[48:51], a[120:123], v[160:163], v[48:51]\n"
    "ds_read_b128 a[152:155], v253 offset:4608\n"
    "v_cvt_pk_bf16_f32 v228, v228, v229\n"
    "s_waitcnt lgkmcnt(10)\n"
    "v_mfma_f32_16x16x32_bf16 v[128:131], a[64:67], a[0:3], v[112:115]\n"
    "ds_read_b128 a[156:159], v253 offset:4864\n"
    "v_cvt_pk_bf16_f32 v229, v230, v231\n"
    "v_mfma_f32_16x16x32_bf16 v[128:131], a[68:71], a[4:7], v[128:131]\n"
    "ds_read_b128 a[160:163], v246 offset:4608\n"
    "v_cvt_pk_bf16_f32 v194, v144, v145\n"
    "v_mfma_f32_16x16x32_bf16 v[128:131], a[72:75], a[8:11], v[128:131]\n"
    "ds_read_b128 a[164:167], v246 offset:4864\n"
    "v_cvt_pk_bf16_f32 v195, v146, v147\n"
    "v_mfma_f32_16x16x32_bf16 v[128:131], a[76:79], a[12:15], v[128:131]\n"
    "ds_write2_b64 v248, v[224:225], v[228:229] offset0:0 offset1:32\n"
    "v_mfma_f32_16x16x32_bf16 v[132:135], a[76:79], a[28:31], v[116:119]\n"
    "ds_read_b128 a[80:83], v232 offset:12288\n"
    "v_mfma_f32_16x16x32_bf16 v[132:135], a[72:75], a[24:27], v[132:135]\n"
    "ds_read_b128 a[84:87], v232 offset:12544\n"
    "v_mfma_f32_16x16x32_bf16 v[132:135], a[68:71], a[20:23], v[132:135]\n"
    "ds_read_b128 a[88:91], v232 offset:12800\n"
    "v_mfma_f32_16x16x32_bf16 v[132:135], a[64:67], a[16:19], v[132:135]\n"
    "v_exp_f32 v128, v128\n"
    "v_mfma_f32_16x16x32_bf16 v[136:139], a[64:67], a[32:35], v[120:123]\n"
    "v_exp_f32 v129, v129\n"
    "v_mfma_f32_16x16x32_bf16 v[136:139], a[68:71], a[36:39], v[136:139]\n"
    "v_exp_f32 v130, v130\n"
    "v_mfma_f32_16x16x32_bf16 v[136:139], a[72:75], a[40:43], v[136:139]\n"
    "v_exp_f32 v131, v131\n"
    "v_mfma_f32_16x16x32_bf16 v[136:139], a[76:79], a[44:47], v[136:139]\n"
    "ds_read_b128 a[92:95], v232 offset:13056\n"
    "v_cvt_pk_bf16_f32 v208, v128, v129\n"
    "v_mfma_f32_16x16x32_bf16 v[140:143], a[76:79], a[60:63], v[124:127]\n"
    "ds_read_b64_tr_b16 a[112:113], v233 offset:768\n"
    "v_cvt_pk_bf16_f32 v209, v130, v131\n"
    "v_mfma_f32_16x16x32_bf16 v[140:143], a[72:75], a[56:59], v[140:143]\n"
    "v_exp_f32 v132, v132\n"
    "v_mfma_f32_16x16x32_bf16 v[140:143], a[68:71], a[52:55], v[140:143]\n"
    "v_exp_f32 v133, v133\n"
    "v_mfma_f32_16x16x32_bf16 v[140:143], a[64:67], a[48:51], v[140:143]\n"
    "v_exp_f32 v134, v134\n"
    "s_waitcnt lgkmcnt(10)\n"
    "v_mfma_f32_16x16x32_bf16 v[64:67], a[96:99], v[160:163], v[64:67]\n"
    "v_exp_f32 v135, v135\n"
    "v_mfma_f32_16x16x32_bf16 v[64:67], a[100:103], v[176:179], v[64:67]\n"
    "ds_read_b64_tr_b16 a[114:115], v233 offset:4864\n"
    "v_cvt_pk_bf16_f32 v212, v132, v133\n"
    "s_waitcnt lgkmcnt(10)\n"
    "v_mfma_f32_16x16x32_bf16 v[224:227], a[148:151], a[152:155], 0\n"
    "ds_read_b64_tr_b16 a[116:117], v233 offset:8960\n"
    "v_cvt_pk_bf16_f32 v213, v134, v135\n"
    "v_mfma_f32_16x16x32_bf16 v[68:71], a[100:103], v[180:183], v[68:71]\n"
    "v_exp_f32 v136, v136\n"
    "v_mfma_f32_16x16x32_bf16 v[68:71], a[96:99], v[164:167], v[68:71]\n"
    "v_exp_f32 v137, v137\n"
    "s_waitcnt lgkmcnt(10)\n"
    "v_mfma_f32_16x16x32_bf16 v[228:231], a[148:151], a[156:159], 0\n"
    "ds_read_b64_tr_b16 a[118:119], v233 offset:13056\n"
    "v_mfma_f32_16x16x32_bf16 v[72:75], a[96:99], v[168:171], v[72:75]\n"
    "v_cvt_pk_bf16_f32 v224, v224, v225\n"
    "v_cvt_pk_bf16_f32 v225, v226, v227\n"
    "v_mfma_f32_16x16x32_bf16 v[72:75], a[100:103], v[184:187], v[72:75]\n"
    "ds_read_b64_tr_b16 a[120:121], v234 offset:768\n"
    "v_mfma_f32_16x16x32_bf16 v[76:79], a[100:103], v[188:191], v[76:79]\n"
    "ds_read_b64_tr_b16 a[122:123], v234 offset:4864\n"
    "v_mfma_f32_16x16x32_bf16 v[76:79], a[96:99], v[172:175], v[76:79]\n"
    "v_cvt_pk_bf16_f32 v228, v228, v229\n"
    "v_cvt_pk_bf16_f32 v229, v230, v231\n"
    "s_memrealtime s[90:91]\n"
    "v_mfma_f32_16x16x32_bf16 v[92:95], a[104:107], v[172:175], v[92:95]\n"
    "ds_write2_b64 v247, v[224:225], v[228:229] offset0:64 offset1:96\n"
    "v_mfma_f32_16x16x32_bf16 v[92:95], a[108:111], v[188:191], v[92:95]\n"
    "ds_read_b64_tr_b16 a[124:125], v234 offset:8960\n"
    "ds_read_b64_tr_b16 a[126:127], v234 offset:13056\n"
    "s_waitcnt lgkmcnt(15)\n"
    "v_mfma_f32_16x16x32_bf16 v[224:227], a[148:151], a[160:163], 0\n"
    "v_exp_f32 v138, v138\n"
    "v_mfma_f32_16x16x32_bf16 v[88:91], a[108:111], v[184:187], v[88:91]\n"
    "v_exp_f32 v139, v139\n"
    "v_mfma_f32_16x16x32_bf16 v[88:91], a[104:107], v[168:171], v[88:91]\n"
    "v_cvt_pk_bf16_f32 v216, v136, v137\n"
    "v_cvt_pk_bf16_f32 v217, v138, v139\n"
    "s_waitcnt lgkmcnt(14)\n"
    "v_mfma_f32_16x16x32_bf16 v[228:231], a[148:151], a[164:167], 0\n"
    "v_exp_f32 v140, v140\n"
    "v_mfma_f32_16x16x32_bf16 v[84:87], a[104:107], v[164:167], v[84:87]\n"
    "v_cvt_pk_bf16_f32 v224, v224, v225\n"
    "v_cvt_pk_bf16_f32 v225, v226, v227\n"
    "v_mfma_f32_16x16x32_bf16 v[84:87], a[108:111], v[180:183], v[84:87]\n"
    "v_exp_f32 v141, v141\n"
    "v_mfma_f32_16x16x32_bf16 v[80:83], a[108:111], v[176:179], v[80:83]\n"
    "v_exp_f32 v142, v142\n"
    "v_mfma_f32_16x16x32_bf16 v[80:83], a[104:107], v[160:163], v[80:83]\n"
    "v_cvt_pk_bf16_f32 v228, v228, v229\n"
    "v_cvt_pk_bf16_f32 v229, v230, v231\n"
    "v_add_u32 v233, s60, v236\n"
    "s_add_u32 s60, s60, 16384\n"
    "s_cmp_eq_u32 s60, 49152\n"
    "s_cselect_b32 s60, 0, s60\n"
    "v_xor_b32 v234, 32, v233\n"
    "v_add_u32 v232, s60, v235\n"
    "s_waitcnt vmcnt(12) lgkmcnt(0)\n"
    "s_sub_u32 s90, s90, s73\n"
    "s_cmp_ge_i32 s90, 0\n"
    "s_cbranch_scc1 .Lpace5x\n"
    ".Lpace5:\n"
    "s_sleep 1\n"
    "s_memrealtime s[90:91]\n"
    "s_waitcnt lgkmcnt(0)\n"
    "s_sub_u32 s90, s90, s73\n"
    "s_cmp_lt_i32 s90, 8\n"
    "s_cbranch_scc1 .Lpace5\n"
    ".Lpace5x:\n"
    "s_add_u32 s73, s73, s88\n"
    "s_barrier\n"
    "v_mfma_f32_16x16x32_bf16 v[156:159], a[80:83], a[48:51], v[124:127]\n"
    "ds_write2_b64 v248, v[224:225], v[228:229] offset0:64 offset1:96\n"
    "v_mfma_f32_16x16x32_bf16 v[156:159], a[84:87], a[52:55], v[156:159]\n"
    "s_add_u32 m0, s70, s58\n"
    "v_exp_f32 v143, v143\n"
    "v_mfma_f32_16x16x32_bf16 v[156:159], a[88:91], a[56:59], v[156:159]\n"
    "ds_read_b128 a[64:67], v232 offset:0\n"
    "v_cvt_pk_bf16_f32 v220, v140, v141\n"
    "v_mfma_f32_16x16x32_bf16 v[156:159], a[92:95], a[60:63], v[156:159]\n"
    "ds_read_b128 a[68:71], v232 offset:256\n"
    "v_cvt_pk_bf16_f32 v221, v142, v143\n"
    "v_mfma_f32_16x16x32_bf16 v[152:155], a[92:95], a[44:47], v[120:123]\n"
    "ds_read_b128 a[72:75], v232 offset:512\n"
    "v_add_u32 v247, 0x1000, v247\n"
    "v_mfma_f32_16x16x32_bf16 v[152:155], a[88:91], a[40:43], v[152:155]\n"
    "ds_read_b128 a[76:79], v232 offset:768\n"
    "v_add_u32 v248, 0x1000, v248\n"
    "v_mfma_f32_16x16x32_bf16 v[152:155], a[84:87], a[36:39], v[152:155]\n"
    "buffer_load_dwordx4 v237, s[40:43], s56 offen offset:0 nt lds\n"
    "v_mfma_f32_16x16x32_bf16 v[152:155], a[80:83], a[32:35], v[152:155]\n"
    "v_exp_f32 v156, v156\n"
    "v_mfma_f32_16x16x32_bf16 v[148:151], a[80:83], a[16:19], v[116:119]\n"
    "v_exp_f32 v157, v157\n"
    "v_mfma_f32_16x16x32_bf16 v[148:151], a[84:87], a[20:23], v[148:151]\n"
    "v_exp_f32 v158, v158\n"
    "v_mfma_f32_16x16x32_bf16 v[148:151], a[88:91], a[24:27], v[148:151]\n"
    "v_exp_f32 v159, v159\n"
    "v_mfma_f32_16x16x32_bf16 v[148:151], a[92:95], a[28:31], v[148:151]\n"
    "ds_read_b64_tr_b16 a[96:97], v233 offset:0\n"
    "v_cvt_pk_bf16_f32 v222, v156, v157\n"
    "v_mfma_f32_16x16x32_bf16 v[144:147], a[92:95], a[12:15], v[112:115]\n"
    "ds_read_b64_tr_b16 a[98:99], v233 offset:4096\n"
    "v_cvt_pk_bf16_f32 v223, v158, v159\n"
    "v_mfma_f32_16x16x32_bf16 v[144:147], a[88:91], a[8:11], v[144:147]\n"
    "v_exp_f32 v152, v152\n"
    "v_mfma_f32_16x16x32_bf16 v[144:147], a[84:87], a[4:7], v[144:147]\n"
    "v_exp_f32 v153, v153\n"
    "v_mfma_f32_16x16x32_bf16 v[144:147], a[80:83], a[0:3], v[144:147]\n"
    "v_exp_f32 v154, v154\n"
    "v_mfma_f32_16x16x32_bf16 v[96:99], a[112:115], v[160:163], v[96:99]\n"
    "ds_read_b64_tr_b16 a[100:101], v233 offset:8192\n"
    "v_mfma_f32_16x16x32_bf16 v[96:99], a[116:119], v[176:179], v[96:99]\n"
    "ds_read_b64_tr_b16 a[102:103], v233 offset:12288\n"
    "v_mfma_f32_16x16x32_bf16 v[100:103], a[116:119], v[180:183], v[100:103]\n"
    "ds_read_b64_tr_b16 a[104:105], v234 offset:0\n"
    "v_mfma_f32_16x16x32_bf16 v[100:103], a[112:115], v[164:167], v[100:103]\n"
    "ds_read_b64_tr_b16 a[106:107], v234 offset:4096\n"
    "v_mfma_f32_16x16x32_bf16 v[104:107], a[112:115], v[168:171], v[104:107]\n"
    "ds_read_b64_tr_b16 a[108:109], v234 offset:8192\n"
    "v_mfma_f32_16x16x32_bf16 v[104:107], a[116:119], v[184:187], v[104:107]\n"
    "ds_read_b64_tr_b16 a[110:111], v234 offset:12288\n"
    "v_mfma_f32_16x16x32_bf16 v[108:111], a[116:119], v[188:191], v[108:111]\n"
    "v_exp_f32 v155, v155\n"
    "v_mfma_f32_16x16x32_bf16 v[108:111], a[112:115], v[172:175], v[108:111]\n"
    "v_cvt_pk_bf16_f32 v218, v152, v153\n"
    "v_cvt_pk_bf16_f32 v219, v154, v155\n"
    "v_mfma_f32_16x16x32_bf16 a[180:183], a[120:123], v[172:175], a[180:183]\n"
    "v_exp_f32 v148, v148\n"
    "v_mfma_f32_16x16x32_bf16 a[180:183], a[124:127], v[188:191], a[180:183]\n"
    "v_exp_f32 v149, v149\n"
    "v_mfma_f32_16x16x32_bf16 a[176:179], a[124:127], v[184:187], a[176:179]\n"
    "v_exp_f32 v150, v150\n"
    "v_mfma_f32_16x16x32_bf16 a[176:179], a[120:123], v[168:171], a[176:179]\n"
    "buffer_load_dwordx4 v238, s[40:43], s56 offen offset:1024 nt lds\n"
    "v_mfma_f32_16x16x32_bf16 a[172:175], a[120:123], v[164:167], a[172:175]\n"
    "v_exp_f32 v151, v151\n"
    "v_mfma_f32_16x16x32_bf16 a[172:175], a[124:127], v[180:183], a[172:175]\n"
    "v_cvt_pk_bf16_f32 v214, v148, v149\n"
    "v_cvt_pk_bf16_f32 v215, v150, v151\n"
    "v_mfma_f32_16x16x32_bf16 a[168:171], a[124:127], v[176:179], a[168:171]\n"
    "buffer_load_dwordx4 v239, s[40:43], s56 offen offset:2048 nt lds\n"
    "v_mfma_f32_16x16x32_bf16 a[168:171], a[120:123], v[160:163], a[168:171]\n"
    "buffer_load_dwordx4 v240, s[40:43], s56 offen offset:3072 nt lds\n"
    "s_add_u32 s56, s56, 16384\n"
    "s_add_u32 s58, s58, 16384\n"
    "s_cmp_eq_u32 s58, 49152\n"
    "s_cselect_b32 s58, 0, s58\n"
    "s_waitcnt lgkmcnt(0)\n"
    "v_mfma_f32_16x16x32_bf16 v[128:131], a[64:67], a[0:3], v[112:115]\n"
    "v_exp_f32 v144, v144\n"
    "v_mfma_f32_16x16x32_bf16 v[128:131], a[68:71], a[4:7], v[128:131]\n"
    "s_add_u32 s37, s64, 4096\n"
    "v_exp_f32 v145, v145\n"
    "v_mfma_f32_16x16x32_bf16 v[128:131], a[72:75], a[8:11], v[128:131]\n"
    "v_exp_f32 v146, v146\n"
    "v_mfma_f32_16x16x32_bf16 v[128:131], a[76:79], a[12:15], v[128:131]\n"
    "v_exp_f32 v147, v147\n"
    "v_mfma_f32_16x16x32_bf16 v[132:135], a[76:79], a[28:31], v[116:119]\n"
    "ds_read_b128 a[80:83], v232 offset:4096\n"
    "v_cvt_pk_bf16_f32 v210, v144, v145\n"
    "v_mfma_f32_16x16x32_bf16 v[132:135], a[72:75], a[24:27], v[132:135]\n"
    "ds_read_b128 a[84:87], v232 offset:4352\n"
    "v_cvt_pk_bf16_f32 v211, v146, v147\n"
    "v_mfma_f32_16x16x32_bf16 v[132:135], a[68:71], a[20:23], v[132:135]\n"
    "buffer_store_dwordx4 a[200:203], v245, s[52:55], s37 offen offset:0 sc1\n"
    "v_mfma_f32_16x16x32_bf16 v[132:135], a[64:67], a[16:19], v[132:135]\n"
    "buffer_store_dwordx4 a[204:207], v245, s[52:55], s37 offen offset:1024 sc1\n"
    "v_mfma_f32_16x16x32_bf16 v[136:139], a[64:67], a[32:35], v[120:123]\n"
    "v_exp_f32 v128, v128\n"
    "v_mfma_f32_16x16x32_bf16 v[136:139], a[68:71], a[36:39], v[136:139]\n"
    "v_exp_f32 v129, v129\n"
    "v_mfma_f32_16x16x32_bf16 v[136:139], a[72:75], a[40:43], v[136:139]\n"
    "v_exp_f32 v130, v130\n"
    "v_mfma_f32_16x16x32_bf16 v[136:139], a[76:79], a[44:47], v[136:139]\n"
    "v_exp_f32 v131, v131\n"
    "v_mfma_f32_16x16x32_bf16 v[140:143], a[76:79], a[60:63], v[124:127]\n"
    "ds_read_b128 a[88:91], v232 offset:4608\n"
    "v_cvt_pk_bf16_f32 v160, v128, v129\n"
    "v_mfma_f32_16x16x32_bf16 v[140:143], a[72:75], a[56:59], v[140:143]\n"
    "ds_read_b128 a[92:95], v232 offset:4864\n"
    "v_cvt_pk_bf16_f32 v161, v130, v131\n"
    "v_mfma_f32_16x16x32_bf16 v[140:143], a[68:71], a[52:55], v[140:143]\n"
    "buffer_store_dwordx4 a[208:211], v245, s[52:55], s37 offen offset:2048 sc1\n"
    "v_mfma_f32_16x16x32_bf16 v[140:143], a[64:67], a[48:51], v[140:143]\n"
    "v_exp_f32 v132, v132\n"
    "v_mfma_f32_16x16x32_bf16 a[128:131], a[144:147], v[192:195], a[128:131]\n"
    "v_exp_f32 v133, v133\n"
    "v_mfma_f32_16x16x32_bf16 a[128:131], a[144:147], v[208:211], a[128:131]\n"
    "v_exp_f32 v134, v134\n"
    "v_mfma_f32_16x16x32_bf16 a[132:135], a[144:147], v[196:199], a[132:135]\n"
    "v_exp_f32 v135, v135\n"
    "v_mfma_f32_16x16x32_bf16 a[132:135], a[144:147], v[212:215], a[132:135]\n"
    "ds_read_b64_tr_b16 a[112:113], v233 offset:256\n"
    "v_cvt_pk_bf16_f32 v164, v132, v133\n"
    "v_mfma_f32_16x16x32_bf16 a[136:139], a[144:147], v[200:203], a[136:139]\n"
    "ds_read_b64_tr_b16 a[114:115], v233 offset:4352\n"
    "v_cvt_pk_bf16_f32 v165, v134, v135\n"
    "v_mfma_f32_16x16x32_bf16 a[136:139], a[144:147], v[216:219], a[136:139]\n"
    "buffer_store_dwordx4 a[212:215], v245, s[52:55], s37 offen offset:3072 sc1\n"
    "v_mfma_f32_16x16x32_bf16 a[140:143], a[144:147], v[204:207], a[140:143]\n"
    "v_exp_f32 v136, v136\n"
    "v_mfma_f32_16x16x32_bf16 a[140:143], a[144:147], v[220:223], a[140:143]\n"
    "v_exp_f32 v137, v137\n"
    "v_mfma_f32_16x16x32_bf16 v[0:3], a[96:99], v[192:195], v[0:3]\n"
    "v_exp_f32 v138, v138\n"
    "v_mfma_f32_16x16x32_bf16 v[0:3], a[100:103], v[208:211], v[0:3]\n"
    "s_add_u32 m0, s71, s59\n"
    "s_nop 0\n"
    "buffer_load_dwordx4 v237, s[44:47], s57 offen offset:0 lds\n"
    "v_mfma_f32_16x16x32_bf16 v[4:7], a[100:103], v[212:215], v[4:7]\n"
    "v_exp_f32 v139, v139\n"
    "v_mfma_f32_16x16x32_bf16 v[4:7], a[96:99], v[196:199], v[4:7]\n"
    "ds_read_b64_tr_b16 a[116:117], v233 offset:8448\n"
    "v_cvt_pk_bf16_f32 v168, v136, v137\n"
    "v_mfma_f32_16x16x32_bf16 v[8:11], a[96:99], v[200:203], v[8:11]\n"
    "ds_read_b64_tr_b16 a[118:119], v233 offset:12544\n"
    "v_cvt_pk_bf16_f32 v169, v138, v139\n"
    "v_mfma_f32_16x16x32_bf16 v[8:11], a[100:103], v[216:219], v[8:11]\n"
    "v_exp_f32 v140, v140\n"
    "v_mfma_f32_16x16x32_bf16 v[12:15], a[100:103], v[220:223], v[12:15]\n"
    "v_exp_f32 v141, v141\n"
    "v_mfma_f32_16x16x32_bf16 v[12:15], a[96:99], v[204:207], v[12:15]\n"
    "buffer_load_dwordx4 v238, s[44:47], s57 offen offset:1024 lds\n"
    "v_mfma_f32_16x16x32_bf16 v[28:31], a[104:107], v[204:207], v[28:31]\n"
    "v_exp_f32 v142, v142\n"
    "v_mfma_f32_16x16x32_bf16 v[28:31], a[108:111], v[220:223], v[28:31]\n"
    "v_exp_f32 v143, v143\n"
    "v_mfma_f32_16x16x32_bf16 v[24:27], a[108:111], v[216:219], v[24:27]\n"
    "ds_read_b64_tr_b16 a[120:121], v234 offset:256\n"
    "v_cvt_pk_bf16_f32 v172, v140, v141\n"
    "v_mfma_f32_16x16x32_bf16 v[24:27], a[104:107], v[200:203], v[24:27]\n"
    "ds_read_b64_tr_b16 a[122:123], v234 offset:4352\n"
    "v_cvt_pk_bf16_f32 v173, v142, v143\n"
    "v_mfma_f32_16x16x32_bf16 v[20:23], a[104:107], v[196:199], v[20:23]\n"
    "ds_read_b64_tr_b16 a[124:125], v234 offset:8448\n"
    "v_mfma_f32_16x16x32_bf16 v[20:23], a[108:111], v[212:215], v[20:23]\n"
    "ds_read_b64_tr_b16 a[126:127], v234 offset:12544\n"
    "v_mfma_f32_16x16x32_bf16 v[16:19], a[108:111], v[208:211], v[16:19]\n"
    "buffer_load_dwordx4 v239, s[44:47], s57 offen offset:2048 lds\n"
    "v_mfma_f32_16x16x32_bf16 v[16:19], a[104:107], v[192:195], v[16:19]\n"
    "ds_read_b128 a[152:155], v253 offset:8192\n"
    "s_waitcnt lgkmcnt(9)\n"
    "v_mfma_f32_16x16x32_bf16 v[156:159], a[80:83], a[48:51], v[124:127]\n"
    "ds_read_b128 a[156:159], v253 offset:8448\n"
    "v_mfma_f32_16x16x32_bf16 v[156:159], a[84:87], a[52:55], v[156:159]\n"
    "ds_read_b128 a[160:163], v246 offset:8192\n"
    "v_mfma_f32_16x16x32_bf16 v[156:159], a[88:91], a[56:59], v[156:159]\n"
    "ds_read_b128 a[164:167], v246 offset:8448\n"
    "v_mfma_f32_16x16x32_bf16 v[156:159], a[92:95], a[60:63], v[156:159]\n"
    "buffer_load_dwordx4 v240, s[44:47], s57 offen offset:3072 lds\n"
    "v_mfma_f32_16x16x32_bf16 v[152:155], a[92:95], a[44:47], v[120:123]\n"
    "ds_read_b128 a[64:67], v232 offset:8192\n"
    "v_mfma_f32_16x16x32_bf16 v[152:155], a[88:91], a[40:43], v[152:155]\n"
    "ds_read_b128 a[68:71], v232 offset:8448\n"
    "v_mfma_f32_16x16x32_bf16 v[152:155], a[84:87], a[36:39], v[152:155]\n"
    "ds_read_b128 a[72:75], v232 offset:8704\n"
    "v_mfma_f32_16x16x32_bf16 v[152:155], a[80:83], a[32:35], v[152:155]\n"
    "v_exp_f32 v156, v156\n"
    "v_mfma_f32_16x16x32_bf16 v[148:151], a[80:83], a[16:19], v[116:119]\n"
    "v_exp_f32 v157, v157\n"
    "v_mfma_f32_16x16x32_bf16 v[148:151], a[84:87], a[20:23], v[148:151]\n"
    "s_add_u32 s57, s57, 33439744\n"
    "s_add_u32 s59, s59, 16384\n"
    "s_cmp_eq_u32 s59, 49152\n"
    "s_cselect_b32 s59, 0, s59\n"
    "v_exp_f32 v158, v158\n"
    "v_mfma_f32_16x16x32_bf16 v[148:151], a[88:91], a[24:27], v[148:151]\n"
    "v_exp_f32 v159, v159\n"
    "v_mfma_f32_16x16x32_bf16 v[148:151], a[92:95], a[28:31], v[148:151]\n"
    "ds_read_b128 a[76:79], v232 offset:8960\n"
    "v_cvt_pk_bf16_f32 v174, v156, v157\n"
    "v_mfma_f32_16x16x32_bf16 v[144:147], a[92:95], a[12:15], v[112:115]\n"
    "ds_read_b64_tr_b16 a[96:97], v233 offset:512\n"
    "v_cvt_pk_bf16_f32 v175, v158, v159\n"
    "v_mfma_f32_16x16x32_bf16 v[144:147], a[88:91], a[8:11], v[144:147]\n"
    "v_exp_f32 v152, v152\n"
    "v_mfma_f32_16x16x32_bf16 v[144:147], a[84:87], a[4:7], v[144:147]\n"
    "v_exp_f32 v153, v153\n"
    "v_mfma_f32_16x16x32_bf16 v[144:147], a[80:83], a[0:3], v[144:147]\n"
    "v_exp_f32 v154, v154\n"
    "s_waitcnt lgkmcnt(9)\n"
    "v_mfma_f32_16x16x32_bf16 v[32:35], a[112:115], v[192:195], v[32:35]\n"
    "v_exp_f32 v155, v155\n"
    "v_mfma_f32_16x16x32_bf16 v[32:35], a[116:119], v[208:211], v[32:35]\n"
    "ds_read_b64_tr_b16 a[98:99], v233 offset:4608\n"
    "v_cvt_pk_bf16_f32 v170, v152, v153\n"
    "s_waitcnt lgkmcnt(9)\n"
    "v_mfma_f32_16x16x32_bf16 v[224:227], a[148:151], a[152:155], 0\n"
    "ds_read_b64_tr_b16 a[100:101], v233 offset:8704\n"
    "v_cvt_pk_bf16_f32 v171, v154, v155\n"
    "v_mfma_f32_16x16x32_bf16 v[36:39], a[116:119], v[212:215], v[36:39]\n"
    "v_exp_f32 v148, v148\n"
    "v_mfma_f32_16x16x32_bf16 v[36:39], a[112:115], v[196:199], v[36:39]\n"
    "v_exp_f32 v149, v149\n"
    "s_waitcnt lgkmcnt(9)\n"
    "v_mfma_f32_16x16x32_bf16 v[228:231], a[148:151], a[156:159], 0\n"
    "v_exp_f32 v150, v150\n"
    "v_mfma_f32_16x16x32_bf16 v[40:43], a[112:115], v[200:203], v[40:43]\n"
    "v_cvt_pk_bf16_f32 v224, v224, v225\n"
    "v_cvt_pk_bf16_f32 v225, v226, v227\n"
    "v_mfma_f32_16x16x32_bf16 v[40:43], a[116:119], v[216:219], v[40:43]\n"
    "v_exp_f32 v151, v151\n"
    "v_mfma_f32_16x16x32_bf16 v[44:47], a[116:119], v[220:223], v[44:47]\n"
    "ds_read_b64_tr_b16 a[102:103], v233 offset:12800\n"
    "v_cvt_pk_bf16_f32 v166, v148, v149\n"
    "v_mfma_f32_16x16x32_bf16 v[44:47], a[112:115], v[204:207], v[44:47]\n"
    "v_cvt_pk_bf16_f32 v228, v228, v229\n"
    "v_cvt_pk_bf16_f32 v229, v230, v231\n"
    "v_mfma_f32_16x16x32_bf16 v[60:63], a[120:123], v[204:207], v[60:63]\n"
    "ds_write2_b64 v247, v[224:225], v[228:229] offset0:0 offset1:32\n"
    "v_mfma_f32_16x16x32_bf16 v[60:63], a[124:127], v[220:223], v[60:63]\n"
    "ds_read_b64_tr_b16 a[104:105], v234 offset:512\n"
    "v_cvt_pk_bf16_f32 v167, v150, v151\n"
    "s_waitcnt lgkmcnt(11)\n"
    "v_mfma_f32_16x16x32_bf16 v[224:227], a[148:151], a[160:163], 0\n"
    "v_exp_f32 v144, v144\n"
    "v_mfma_f32_16x16x32_bf16 v[56:59], a[124:127], v[216:219], v[56:59]\n"
    "v_exp_f32 v145, v145\n"
    "v_mfma_f32_16x16x32_bf16 v[56:59], a[120:123], v[200:203], v[56:59]\n"
    "v_exp_f32 v146, v146\n"
    "s_waitcnt lgkmcnt(10)\n"
    "v_mfma_f32_16x16x32_bf16 v[228:231], a[148:151], a[164:167], 0\n"
    "ds_read_b64_tr_b16 a[106:107], v234 offset:4608\n"
    "v_mfma_f32_16x16x32_bf16 v[52:55], a[120:123], v[196:199], v[52:55]\n"
    "ds_read_b64_tr_b16 a[108:109], v234 offset:8704\n"
    "v_cvt_pk_bf16_f32 v224, v224, v225\n"
    "v_mfma_f32_16x16x32_bf16 v[52:55], a[124:127], v[212:215], v[52:55]\n"
    "ds_read_b64_tr_b16 a[110:111], v234 offset:12800\n"
    "v_cvt_pk_bf16_f32 v225, v226, v227\n"
    "v_mfma_f32_16x16x32_bf16 v[48:51], a[124:127], v[208:211], v[48:51]\n"
    "v_exp_f32 v147, v147\n"
    "v_mfma_f32_16x16x32_bf16 v[48:51], a[120:123], v[192:195], v[48:51]\n"
    "ds_read_b128 a[152:155], v253 offset:8704\n"
    "v_cvt_pk_bf16_f32 v228, v228, v229\n"
    "s_waitcnt lgkmcnt(10)\n"
    "v_mfma_f32_16x16x32_bf16 v[128:131], a[64:67], a[0:3], v[112:115]\n"
    "ds_read_b128 a[156:159], v253 offset:8960\n"
    "s_add_u32 s63, s63, 16777216\n"
    "s_cmp_lt_u32 s36, 2\n"
    "s_cselect_b32 s50, s50, 0\n"
    "s_cselect_b32 s63, s63, 0\n"
    "v_cvt_pk_bf16_f32 v229, v230, v231\n"
    "v_mfma_f32_16x16x32_bf16 v[128:131], a[68:71], a[4:7], v[128:131]\n"
    "ds_read_b128 a[160:163], v246 offset:8704\n"
    "v_cvt_pk_bf16_f32 v162, v144, v145\n"
    "v_mfma_f32_16x16x32_bf16 v[128:131], a[72:75], a[8:11], v[128:131]\n"
    "ds_read_b128 a[164:167], v246 offset:8960\n"
    "v_cvt_pk_bf16_f32 v163, v146, v147\n"
    "v_mfma_f32_16x16x32_bf16 v[128:131], a[76:79], a[12:15], v[128:131]\n"
    "ds_write2_b64 v248, v[224:225], v[228:229] offset0:0 offset1:32\n"
    "v_mfma_f32_16x16x32_bf16 v[132:135], a[76:79], a[28:31], v[116:119]\n"
    "ds_read_b128 a[80:83], v232 offset:12288\n"
    "v_mfma_f32_16x16x32_bf16 v[132:135], a[72:75], a[24:27], v[132:135]\n"
    "ds_read_b128 a[84:87], v232 offset:12544\n"
    "v_mfma_f32_16x16x32_bf16 v[132:135], a[68:71], a[20:23], v[132:135]\n"
    "ds_read_b128 a[88:91], v232 offset:12800\n"
    "v_mfma_f32_16x16x32_bf16 v[132:135], a[64:67], a[16:19], v[132:135]\n"
    "v_exp_f32 v128, v128\n"
    "v_mfma_f32_16x16x32_bf16 v[136:139], a[64:67], a[32:35], v[120:123]\n"
    "v_exp_f32 v129, v129\n"
    "v_mfma_f32_16x16x32_bf16 v[136:139], a[68:71], a[36:39], v[136:139]\n"
    "v_exp_f32 v130, v130\n"
    "v_mfma_f32_16x16x32_bf16 v[136:139], a[72:75], a[40:43], v[136:139]\n"
    "v_exp_f32 v131, v131\n"
    "v_mfma_f32_16x16x32_bf16 v[136:139], a[76:79], a[44:47], v[136:139]\n"
    "ds_read_b128 a[92:95], v232 offset:13056\n"
    "v_cvt_pk_bf16_f32 v176, v128, v129\n"
    "v_mfma_f32_16x16x32_bf16 v[140:143], a[76:79], a[60:63], v[124:127]\n"
    "ds_read_b64_tr_b16 a[112:113], v233 offset:768\n"
    "v_cvt_pk_bf16_f32 v177, v130, v131\n"
    "v_mfma_f32_16x16x32_bf16 v[140:143], a[72:75], a[56:59], v[140:143]\n"
    "v_exp_f32 v132, v132\n"
    "v_mfma_f32_16x16x32_bf16 v[140:143], a[68:71], a[52:55], v[140:143]\n"
    "v_exp_f32 v133, v133\n"
    "v_mfma_f32_16x16x32_bf16 v[140:143], a[64:67], a[48:51], v[140:143]\n"
    "v_exp_f32 v134, v134\n"
    "s_waitcnt lgkmcnt(10)\n"
    "v_mfma_f32_16x16x32_bf16 v[64:67], a[96:99], v[192:195], v[64:67]\n"
    "v_exp_f32 v135, v135\n"
    "v_mfma_f32_16x16x32_bf16 v[64:67], a[100:103], v[208:211], v[64:67]\n"
    "ds_read_b64_tr_b16 a[114:115], v233 offset:4864\n"
    "v_cvt_pk_bf16_f32 v180, v132, v133\n"
    "s_waitcnt lgkmcnt(10)\n"
    "v_mfma_f32_16x16x32_bf16 v[224:227], a[148:151], a[152:155], 0\n"
    "ds_read_b64_tr_b16 a[116:117], v233 offset:8960\n"
    "v_cvt_pk_bf16_f32 v181, v134, v135\n"
    "v_mfma_f32_16x16x32_bf16 v[68:71], a[100:103], v[212:215], v[68:71]\n"
    "v_exp_f32 v136, v136\n"
    "v_mfma_f32_16x16x32_bf16 v[68:71], a[96:99], v[196:199], v[68:71]\n"
    "v_exp_f32 v137, v137\n"
    "s_waitcnt lgkmcnt(10)\n"
    "v_mfma_f32_16x16x32_bf16 v[228:231], a[148:151], a[156:159], 0\n"
    "ds_read_b64_tr_b16 a[118:119], v233 offset:13056\n"
    "v_mfma_f32_16x16x32_bf16 v[72:75], a[96:99], v[200:203], v[72:75]\n"
    "v_cvt_pk_bf16_f32 v224, v224, v225\n"
    "v_cvt_pk_bf16_f32 v225, v226, v227\n"
    "v_mfma_f32_16x16x32_bf16 v[72:75], a[100:103], v[216:219], v[72:75]\n"
    "ds_read_b64_tr_b16 a[120:121], v234 offset:768\n"
    "v_mfma_f32_16x16x32_bf16 v[76:79], a[100:103], v[220:223], v[76:79]\n"
    "ds_read_b64_tr_b16 a[122:123], v234 offset:4864\n"
    "v_mfma_f32_16x16x32_bf16 v[76:79], a[96:99], v[204:207], v[76:79]\n"
    "v_cvt_pk_bf16_f32 v228, v228, v229\n"
    "v_cvt_pk_bf16_f32 v229, v230, v231\n"
    "s_memrealtime s[90:91]\n"
    "v_mfma_f32_16x16x32_bf16 v[92:95], a[104:107], v[204:207], v[92:95]\n"
    "ds_write2_b64 v247, v[224:225], v[228:229] offset0:64 offset1:96\n"
    "v_mfma_f32_16x16x32_bf16 v[92:95], a[108:111], v[220:223], v[92:95]\n"
    "ds_read_b64_tr_b16 a[124:125], v234 offset:8960\n"
    "ds_read_b64_tr_b16 a[126:127], v234 offset:13056\n"
    "s_waitcnt lgkmcnt(15)\n"
    "v_mfma_f32_16x16x32_bf16 v[224:227], a[148:151], a[160:163], 0\n"
    "v_exp_f32 v138, v138\n"
    "v_mfma_f32_16x16x32_bf16 v[88:91], a[108:111], v[216:219], v[88:91]\n"
    "v_exp_f32 v139, v139\n"
    "v_mfma_f32_16x16x32_bf16 v[88:91], a[104:107], v[200:203], v[88:91]\n"
    "v_cvt_pk_bf16_f32 v184, v136, v137\n"
    "v_cvt_pk_bf16_f32 v185, v138, v139\n"
    "s_waitcnt lgkmcnt(14)\n"
    "v_mfma_f32_16x16x32_bf16 v[228:231], a[148:151], a[164:167], 0\n"
    "v_exp_f32 v140, v140\n"
    "v_mfma_f32_16x16x32_bf16 v[84:87], a[104:107], v[196:199], v[84:87]\n"
    "v_cvt_pk_bf16_f32 v224, v224, v225\n"
    "v_cvt_pk_bf16_f32 v225, v226, v227\n"
    "v_mfma_f32_16x16x32_bf16 v[84:87], a[108:111], v[212:215], v[84:87]\n"
    "v_exp_f32 v141, v141\n"
    "v_mfma_f32_16x16x32_bf16 v[80:83], a[108:111], v[208:211], v[80:83]\n"
    "v_exp_f32 v142, v142\n"
    "v_mfma_f32_16x16x32_bf16 v[80:83], a[104:107], v[192:195], v[80:83]\n"
    "v_cvt_pk_bf16_f32 v228, v228, v229\n"
    "v_cvt_pk_bf16_f32 v229, v230, v231\n"
    "v_add_u32 v233, s60, v236\n"
    "s_add_u32 s60, s60, 16384\n"
    "s_cmp_eq_u32 s60, 49152\n"
    "s_cselect_b32 s60, 0, s60\n"
    "v_xor_b32 v234, 32, v233\n"
    "v_add_u32 v232, s60, v235\n"
    "s_waitcnt vmcnt(12) lgkmcnt(0)\n"
    "s_sub_u32 s90, s90, s73\n"
    "s_cmp_ge_i32 s90, 0\n"
    "s_cbranch_scc1 .Lpace6x\n"
    ".Lpace6:\n"
    "s_sleep 1\n"
    "s_memrealtime s[90:91]\n"
    "s_waitcnt lgkmcnt(0)\n"
    "s_sub_u32 s90, s90, s73\n"
    "s_cmp_lt_i32 s90, 8\n"
    "s_cbranch_scc1 .Lpace6\n"
    ".Lpace6x:\n"
    "s_add_u32 s73, s73, s88\n"
    "s_barrier\n"
    "v_mfma_f32_16x16x32_bf16 v[156:159], a[80:83], a[48:51], v[124:127]\n"
    "ds_write2_b64 v248, v[224:225], v[228:229] offset0:64 offset1:96\n"
    "v_mfma_f32_16x16x32_bf16 v[156:159], a[84:87], a[52:55], v[156:159]\n"
    "s_add_u32 m0, s70, s58\n"
    "v_exp_f32 v143, v143\n"
    "v_mfma_f32_16x16x32_bf16 v[156:159], a[88:91], a[56:59], v[156:159]\n"
    "ds_read_b128 a[64:67], v232 offset:0\n"
    "v_cvt_pk_bf16_f32 v188, v140, v141\n"
    "v_mfma_f32_16x16x32_bf16 v[156:159], a[92:95], a[60:63], v[156:159]\n"
    "ds_read_b128 a[68:71], v232 offset:256\n"
    "v_cvt_pk_bf16_f32 v189, v142, v143\n"
    "v_mfma_f32_16x16x32_bf16 v[152:155], a[92:95], a[44:47], v[120:123]\n"
    "ds_read_b128 a[72:75], v232 offset:512\n"
    "v_add_u32 v247, 0x1000, v247\n"
    "v_mfma_f32_16x16x32_bf16 v[152:155], a[88:91], a[40:43], v[152:155]\n"
    "ds_read_b128 a[76:79], v232 offset:768\n"
    "v_add_u32 v248, 0x1000, v248\n"
    "v_mfma_f32_16x16x32_bf16 v[152:155], a[84:87], a[36:39], v[152:155]\n"
    "buffer_load_dwordx4 v237, s[40:43], s56 offen offset:0 nt lds\n"
    "v_mfma_f32_16x16x32_bf16 v[152:155], a[80:83], a[32:35], v[152:155]\n"
    "v_exp_f32 v156, v156\n"
    "v_mfma_f32_16x16x32_bf16 v[148:151], a[80:83], a[16:19], v[116:119]\n"
    "v_exp_f32 v157, v157\n"
    "v_mfma_f32_16x16x32_bf16 v[148:151], a[84:87], a[20:23], v[148:151]\n"
    "v_exp_f32 v158, v158\n"
    "v_mfma_f32_16x16x32_bf16 v[148:151], a[88:91], a[24:27], v[148:151]\n"
    "v_exp_f32 v159, v159\n"
    "v_mfma_f32_16x16x32_bf16 v[148:151], a[92:95], a[28:31], v[148:151]\n"
    "ds_read_b64_tr_b16 a[96:97], v233 offset:0\n"
    "v_cvt_pk_bf16_f32 v190, v156, v157\n"
    "v_mfma_f32_16x16x32_bf16 v[144:147], a[92:95], a[12:15], v[112:115]\n"
    "ds_read_b64_tr_b16 a[98:99], v233 offset:4096\n"
    "v_cvt_pk_bf16_f32 v191, v158, v159\n"
    "v_mfma_f32_16x16x32_bf16 v[144:147], a[88:91], a[8:11], v[144:147]\n"
    "v_exp_f32 v152, v152\n"
    "v_mfma_f32_16x16x32_bf16 v[144:147], a[84:87], a[4:7], v[144:147]\n"
    "v_exp_f32 v153, v153\n"
    "v_mfma_f32_16x16x32_bf16 v[144:147], a[80:83], a[0:3], v[144:147]\n"
    "v_exp_f32 v154, v154\n"
    "v_mfma_f32_16x16x32_bf16 v[96:99], a[112:115], v[192:195], v[96:99]\n"
    "ds_read_b64_tr_b16 a[100:101], v233 offset:8192\n"
    "v_mfma_f32_16x16x32_bf16 v[96:99], a[116:119], v[208:211], v[96:99]\n"
    "ds_read_b64_tr_b16 a[102:103], v233 offset:12288\n"
    "v_mfma_f32_16x16x32_bf16 v[100:103], a[116:119], v[212:215], v[100:103]\n"
    "ds_read_b64_tr_b16 a[104:105], v234 offset:0\n"
    "v_mfma_f32_16x16x32_bf16 v[100:103], a[112:115], v[196:199], v[100:103]\n"
    "ds_read_b64_tr_b16 a[106:107], v234 offset:4096\n"
    "v_mfma_f32_16x16x32_bf16 v[104:107], a[112:115], v[200:203], v[104:107]\n"
    "ds_read_b64_tr_b16 a[108:109], v234 offset:8192\n"
    "v_mfma_f32_16x16x32_bf16 v[104:107], a[116:119], v[216:219], v[104:107]\n"
    "ds_read_b64_tr_b16 a[110:111], v234 offset:12288\n"
    "v_mfma_f32_16x16x32_bf16 v[108:111], a[116:119], v[220:223], v[108:111]\n"
    "v_exp_f32 v155, v155\n"
    "v_mfma_f32_16x16x32_bf16 v[108:111], a[112:115], v[204:207], v[108:111]\n"
    "v_cvt_pk_bf16_f32 v186, v152, v153\n"
    "v_cvt_pk_bf16_f32 v187, v154, v155\n"
    "v_mfma_f32_16x16x32_bf16 a[180:183], a[120:123], v[204:207], a[180:183]\n"
    "v_exp_f32 v148, v148\n"
    "v_mfma_f32_16x16x32_bf16 a[180:183], a[124:127], v[220:223], a[180:183]\n"
    "v_exp_f32 v149, v149\n"
    "v_mfma_f32_16x16x32_bf16 a[176:179], a[124:127], v[216:219], a[176:179]\n"
    "v_exp_f32 v150, v150\n"
    "v_mfma_f32_16x16x32_bf16 a[176:179], a[120:123], v[200:203], a[176:179]\n"
    "buffer_load_dwordx4 v238, s[40:43], s56 offen offset:1024 nt lds\n"
    "v_mfma_f32_16x16x32_bf16 a[172:175], a[120:123], v[196:199], a[172:175]\n"
    "v_exp_f32 v151, v151\n"
    "v_mfma_f32_16x16x32_bf16 a[172:175], a[124:127], v[212:215], a[172:175]\n"
    "v_cvt_pk_bf16_f32 v182, v148, v149\n"
    "v_cvt_pk_bf16_f32 v183, v150, v151\n"
    "v_mfma_f32_16x16x32_bf16 a[168:171], a[124:127], v[208:211], a[168:171]\n"
    "buffer_load_dwordx4 v239, s[40:43], s56 offen offset:2048 nt lds\n"
    "v_mfma_f32_16x16x32_bf16 a[168:171], a[120:123], v[192:195], a[168:171]\n"
    "buffer_load_dwordx4 v240, s[40:43], s56 offen offset:3072 nt lds\n"
    "s_add_u32 s56, s56, 16384\n"
    "s_add_u32 s58, s58, 16384\n"
    "s_cmp_eq_u32 s58, 49152\n"
    "s_cselect_b32 s58, 0, s58\n"
    "s_waitcnt lgkmcnt(0)\n"
    "v_mfma_f32_16x16x32_bf16 v[128:131], a[64:67], a[0:3], v[112:115]\n"
    "v_exp_f32 v144, v144\n"
    "v_mfma_f32_16x16x32_bf16 v[128:131], a[68:71], a[4:7], v[128:131]\n"
    "s_add_u32 s37, s64, 8192\n"
    "v_exp_f32 v145, v145\n"
    "v_mfma_f32_16x16x32_bf16 v[128:131], a[72:75], a[8:11], v[128:131]\n"
    "v_exp_f32 v146, v146\n"
    "v_mfma_f32_16x16x32_bf16 v[128:131], a[76:79], a[12:15], v[128:131]\n"
    "v_exp_f32 v147, v147\n"
    "v_mfma_f32_16x16x32_bf16 v[132:135], a[76:79], a[28:31], v[116:119]\n"
    "ds_read_b128 a[80:83], v232 offset:4096\n"
    "v_cvt_pk_bf16_f32 v178, v144, v145\n"
    "v_mfma_f32_16x16x32_bf16 v[132:135], a[72:75], a[24:27], v[132:135]\n"
    "ds_read_b128 a[84:87], v232 offset:4352\n"
    "v_cvt_pk_bf16_f32 v179, v146, v147\n"
    "v_mfma_f32_16x16x32_bf16 v[132:135], a[68:71], a[20:23], v[132:135]\n"
    "buffer_store_dwordx4 a[216:219], v245, s[52:55], s37 offen offset:0 sc1\n"
    "v_mfma_f32_16x16x32_bf16 v[132:135], a[64:67], a[16:19], v[132:135]\n"
    "buffer_store_dwordx4 a[220:223], v245, s[52:55], s37 offen offset:1024 sc1\n"
    "v_mfma_f32_16x16x32_bf16 v[136:139], a[64:67], a[32:35], v[120:123]\n"
    "v_exp_f32 v128, v128\n"
    "v_mfma_f32_16x16x32_bf16 v[136:139], a[68:71], a[36:39], v[136:139]\n"
    "v_exp_f32 v129, v129\n"
    "v_mfma_f32_16x16x32_bf16 v[136:139], a[72:75], a[40:43], v[136:139]\n"
    "v_exp_f32 v130, v130\n"
    "v_mfma_f32_16x16x32_bf16 v[136:139], a[76:79], a[44:47], v[136:139]\n"
    "v_exp_f32 v131, v131\n"
    "v_mfma_f32_16x16x32_bf16 v[140:143], a[76:79], a[60:63], v[124:127]\n"
    "ds_read_b128 a[88:91], v232 offset:4608\n"
    "v_cvt_pk_bf16_f32 v192, v128, v129\n"
    "v_mfma_f32_16x16x32_bf16 v[140:143], a[72:75], a[56:59], v[140:143]\n"
    "ds_read_b128 a[92:95], v232 offset:4864\n"
    "v_cvt_pk_bf16_f32 v193, v130, v131\n"
    "v_mfma_f32_16x16x32_bf16 v[140:143], a[68:71], a[52:55], v[140:143]\n"
    "buffer_store_dwordx4 a[224:227], v245, s[52:55], s37 offen offset:2048 sc1\n"
    "v_mfma_f32_16x16x32_bf16 v[140:143], a[64:67], a[48:51], v[140:143]\n"
    "v_exp_f32 v132, v132\n"
    "v_mfma_f32_16x16x32_bf16 a[128:131], a[144:147], v[160:163], a[128:131]\n"
    "v_exp_f32 v133, v133\n"
    "v_mfma_f32_16x16x32_bf16 a[128:131], a[144:147], v[176:179], a[128:131]\n"
    "v_exp_f32 v134, v134\n"
    "v_mfma_f32_16x16x32_bf16 a[132:135], a[144:147], v[164:167], a[132:135]\n"
    "v_exp_f32 v135, v135\n"
    "v_mfma_f32_16x16x32_bf16 a[132:135], a[144:147], v[180:183], a[132:135]\n"
    "ds_read_b64_tr_b16 a[112:113], v233 offset:256\n"
    "v_cvt_pk_bf16_f32 v196, v132, v133\n"
    "v_mfma_f32_16x16x32_bf16 a[136:139], a[144:147], v[168:171], a[136:139]\n"
    "ds_read_b64_tr_b16 a[114:115], v233 offset:4352\n"
    "v_cvt_pk_bf16_f32 v197, v134, v135\n"
    "v_mfma_f32_16x16x32_bf16 a[136:139], a[144:147], v[184:187], a[136:139]\n"
    "buffer_store_dwordx4 a[228:231], v245, s[52:55], s37 offen offset:3072 sc1\n"
    "v_mfma_f32_16x16x32_bf16 a[140:143], a[144:147], v[172:175], a[140:143]\n"
    "v_exp_f32 v136, v136\n"
    "v_mfma_f32_16x16x32_bf16 a[140:143], a[144:147], v[188:191], a[140:143]\n"
    "v_exp_f32 v137, v137\n"
    "v_mfma_f32_16x16x32_bf16 v[0:3], a[96:99], v[160:163], v[0:3]\n"
    "v_exp_f32 v138, v138\n"
    "v_mfma_f32_16x16x32_bf16 v[0:3], a[100:103], v[176:179], v[0:3]\n"
    "s_add_u32 m0, s71, s59\n"
    "s_nop 0\n"
    "buffer_load_dwordx4 v237, s[44:47], s57 offen offset:0 lds\n"
    "v_mfma_f32_16x16x32_bf16 v[4:7], a[100:103], v[180:183], v[4:7]\n"
    "v_exp_f32 v139, v139\n"
    "v_mfma_f32_16x16x32_bf16 v[4:7], a[96:99], v[164:167], v[4:7]\n"
    "ds_read_b64_tr_b16 a[116:117], v233 offset:8448\n"
    "v_cvt_pk_bf16_f32 v200, v136, v137\n"
    "v_mfma_f32_16x16x32_bf16 v[8:11], a[96:99], v[168:171], v[8:11]\n"
    "ds_read_b64_tr_b16 a[118:119], v233 offset:12544\n"
    "v_cvt_pk_bf16_f32 v201, v138, v139\n"
    "v_mfma_f32_16x16x32_bf16 v[8:11], a[100:103], v[184:187], v[8:11]\n"
    "v_exp_f32 v140, v140\n"
    "v_mfma_f32_16x16x32_bf16 v[12:15], a[100:103], v[188:191], v[12:15]\n"
    "v_exp_f32 v141, v141\n"
    "v_mfma_f32_16x16x32_bf16 v[12:15], a[96:99], v[172:175], v[12:15]\n"
    "buffer_load_dwordx4 v238, s[44:47], s57 offen offset:1024 lds\n"
    "v_mfma_f32_16x16x32_bf16 v[28:31], a[104:107], v[172:175], v[28:31]\n"
    "v_exp_f32 v142, v142\n"
    "v_mfma_f32_16x16x32_bf16 v[28:31], a[108:111], v[188:191], v[28:31]\n"
    "v_exp_f32 v143, v143\n"
    "v_mfma_f32_16x16x32_bf16 v[24:27], a[108:111], v[184:187], v[24:27]\n"
    "ds_read_b64_tr_b16 a[120:121], v234 offset:256\n"
    "v_cvt_pk_bf16_f32 v204, v140, v141\n"
    "v_mfma_f32_16x16x32_bf16 v[24:27], a[104:107], v[168:171], v[24:27]\n"
    "ds_read_b64_tr_b16 a[122:123], v234 offset:4352\n"
    "v_cvt_pk_bf16_f32 v205, v142, v143\n"
    "v_mfma_f32_16x16x32_bf16 v[20:23], a[104:107], v[164:167], v[20:23]\n"
    "ds_read_b64_tr_b16 a[124:125], v234 offset:8448\n"
    "v_mfma_f32_16x16x32_bf16 v[20:23], a[108:111], v[180:183], v[20:23]\n"
    "ds_read_b64_tr_b16 a[126:127], v234 offset:12544\n"
    "v_mfma_f32_16x16x32_bf16 v[16:19], a[108:111], v[176:179], v[16:19]\n"
    "buffer_load_dwordx4 v239, s[44:47], s57 offen offset:2048 lds\n"
    "v_mfma_f32_16x16x32_bf16 v[16:19], a[104:107], v[160:163], v[16:19]\n"
    "ds_read_b128 a[152:155], v253 offset:12288\n"
    "s_waitcnt lgkmcnt(9)\n"
    "v_mfma_f32_16x16x32_bf16 v[156:159], a[80:83], a[48:51], v[124:127]\n"
    "ds_read_b128 a[156:159], v253 offset:12544\n"
    "v_mfma_f32_16x16x32_bf16 v[156:159], a[84:87], a[52:55], v[156:159]\n"
    "ds_read_b128 a[160:163], v246 offset:12288\n"
    "v_mfma_f32_16x16x32_bf16 v[156:159], a[88:91], a[56:59], v[156:159]\n"
    "ds_read_b128 a[164:167], v246 offset:12544\n"
    "v_mfma_f32_16x16x32_bf16 v[156:159], a[92:95], a[60:63], v[156:159]\n"
    "buffer_load_dwordx4 v240, s[44:47], s57 offen offset:3072 lds\n"
    "v_mfma_f32_16x16x32_bf16 v[152:155], a[92:95], a[44:47], v[120:123]\n"
    "ds_read_b128 a[64:67], v232 offset:8192\n"
    "v_mfma_f32_16x16x32_bf16 v[152:155], a[88:91], a[40:43], v[152:155]\n"
    "ds_read_b128 a[68:71], v232 offset:8448\n"
    "v_mfma_f32_16x16x32_bf16 v[152:155], a[84:87], a[36:39], v[152:155]\n"
    "ds_read_b128 a[72:75], v232 offset:8704\n"
    "v_mfma_f32_16x16x32_bf16 v[152:155], a[80:83], a[32:35], v[152:155]\n"
    "v_exp_f32 v156, v156\n"
    "v_mfma_f32_16x16x32_bf16 v[148:151], a[80:83], a[16:19], v[116:119]\n"
    "v_exp_f32 v157, v157\n"
    "v_mfma_f32_16x16x32_bf16 v[148:151], a[84:87], a[20:23], v[148:151]\n"
    "s_add_u32 s57, s57, 16384\n"
    "s_add_u32 s59, s59, 16384\n"
    "s_cmp_eq_u32 s59, 49152\n"
    "s_cselect_b32 s59, 0, s59\n"
    "v_exp_f32 v158, v158\n"
    "v_mfma_f32_16x16x32_bf16 v[148:151], a[88:91], a[24:27], v[148:151]\n"
    "v_exp_f32 v159, v159\n"
    "v_mfma_f32_16x16x32_bf16 v[148:151], a[92:95], a[28:31], v[148:151]\n"
    "ds_read_b128 a[76:79], v232 offset:8960\n"
    "v_cvt_pk_bf16_f32 v206, v156, v157\n"
    "v_mfma_f32_16x16x32_bf16 v[144:147], a[92:95], a[12:15], v[112:115]\n"
    "ds_read_b64_tr_b16 a[96:97], v233 offset:512\n"
    "v_cvt_pk_bf16_f32 v207, v158, v159\n"
    "v_mfma_f32_16x16x32_bf16 v[144:147], a[88:91], a[8:11], v[144:147]\n"
    "v_exp_f32 v152, v152\n"
    "v_mfma_f32_16x16x32_bf16 v[144:147], a[84:87], a[4:7], v[144:147]\n"
    "v_exp_f32 v153, v153\n"
    "v_mfma_f32_16x16x32_bf16 v[144:147], a[80:83], a[0:3], v[144:147]\n"
    "v_exp_f32 v154, v154\n"
    "s_waitcnt lgkmcnt(9)\n"
    "v_mfma_f32_16x16x32_bf16 v[32:35], a[112:115], v[160:163], v[32:35]\n"
    "v_exp_f32 v155, v155\n"
    "v_mfma_f32_16x16x32_bf16 v[32:35], a[116:119], v[176:179], v[32:35]\n"
    "ds_read_b64_tr_b16 a[98:99], v233 offset:4608\n"
    "v_cvt_pk_bf16_f32 v202, v152, v153\n"
    "s_waitcnt lgkmcnt(9)\n"
    "v_mfma_f32_16x16x32_bf16 v[224:227], a[148:151], a[152:155], 0\n"
    "ds_read_b64_tr_b16 a[100:101], v233 offset:8704\n"
    "v_cvt_pk_bf16_f32 v203, v154, v155\n"
    "v_mfma_f32_16x16x32_bf16 v[36:39], a[116:119], v[180:183], v[36:39]\n"
    "v_exp_f32 v148, v148\n"
    "v_mfma_f32_16x16x32_bf16 v[36:39], a[112:115], v[164:167], v[36:39]\n"
    "v_exp_f32 v149, v149\n"
    "s_waitcnt lgkmcnt(9)\n"
    "v_mfma_f32_16x16x32_bf16 v[228:231], a[148:151], a[156:159], 0\n"
    "v_exp_f32 v150, v150\n"
    "v_mfma_f32_16x16x32_bf16 v[40:43], a[112:115], v[168:171], v[40:43]\n"
    "v_cvt_pk_bf16_f32 v224, v224, v225\n"
    "v_cvt_pk_bf16_f32 v225, v226, v227\n"
    "v_mfma_f32_16x16x32_bf16 v[40:43], a[116:119], v[184:187], v[40:43]\n"
    "v_exp_f32 v151, v151\n"
    "v_mfma_f32_16x16x32_bf16 v[44:47], a[116:119], v[188:191], v[44:47]\n"
    "ds_read_b64_tr_b16 a[102:103], v233 offset:12800\n"
    "v_cvt_pk_bf16_f32 v198, v148, v149\n"
    "v_mfma_f32_16x16x32_bf16 v[44:47], a[112:115], v[172:175], v[44:47]\n"
    "v_cvt_pk_bf16_f32 v228, v228, v229\n"
    "v_cvt_pk_bf16_f32 v229, v230, v231\n"
    "v_mfma_f32_16x16x32_bf16 v[60:63], a[120:123], v[172:175], v[60:63]\n"
    "ds_write2_b64 v247, v[224:225], v[228:229] offset0:0 offset1:32\n"
    "v_mfma_f32_16x16x32_bf16 v[60:63], a[124:127], v[188:191], v[60:63]\n"
    "ds_read_b64_tr_b16 a[104:105], v234 offset:512\n"
    "v_cvt_pk_bf16_f32 v199, v150, v151\n"
    "s_waitcnt lgkmcnt(11)\n"
    "v_mfma_f32_16x16x32_bf16 v[224:227], a[148:151], a[160:163], 0\n"
    "v_exp_f32 v144, v144\n"
    "v_mfma_f32_16x16x32_bf16 v[56:59], a[124:127], v[184:187], v[56:59]\n"
    "v_exp_f32 v145, v145\n"
    "v_mfma_f32_16x16x32_bf16 v[56:59], a[120:123], v[168:171], v[56:59]\n"
    "v_exp_f32 v146, v146\n"
    "s_waitcnt lgkmcnt(10)\n"
    "v_mfma_f32_16x16x32_bf16 v[228:231], a[148:151], a[164:167], 0\n"
    "ds_read_b64_tr_b16 a[106:107], v234 offset:4608\n"
    "v_mfma_f32_16x16x32_bf16 v[52:55], a[120:123], v[164:167], v[52:55]\n"
    "ds_read_b64_tr_b16 a[108:109], v234 offset:8704\n"
    "v_cvt_pk_bf16_f32 v224, v224, v225\n"
    "v_mfma_f32_16x16x32_bf16 v[52:55], a[124:127], v[180:183], v[52:55]\n"
    "ds_read_b64_tr_b16 a[110:111], v234 offset:12800\n"
    "v_cvt_pk_bf16_f32 v225, v226, v227\n"
    "v_mfma_f32_16x16x32_bf16 v[48:51], a[124:127], v[176:179], v[48:51]\n"
    "ds_read_b128 a[152:155], v253 offset:12800\n"
    "v_exp_f32 v147, v147\n"
    "v_mfma_f32_16x16x32_bf16 v[48:51], a[120:123], v[160:163], v[48:51]\n"
    "ds_read_b128 a[156:159], v253 offset:13056\n"
    "v_cvt_pk_bf16_f32 v228, v228, v229\n"
    "s_waitcnt lgkmcnt(11)\n"
    "v_mfma_f32_16x16x32_bf16 v[128:131], a[64:67], a[0:3], v[112:115]\n"
    "ds_read_b128 a[160:163], v246 offset:12800\n"
    "v_cvt_pk_bf16_f32 v229, v230, v231\n"
    "v_mfma_f32_16x16x32_bf16 v[128:131], a[68:71], a[4:7], v[128:131]\n"
    "ds_read_b128 a[164:167], v246 offset:13056\n"
    "v_cvt_pk_bf16_f32 v194, v144, v145\n"
    "v_mfma_f32_16x16x32_bf16 v[128:131], a[72:75], a[8:11], v[128:131]\n"
    "ds_read_b128 a[80:83], v232 offset:12288\n"
    "v_cvt_pk_bf16_f32 v195, v146, v147\n"
    "v_mfma_f32_16x16x32_bf16 v[128:131], a[76:79], a[12:15], v[128:131]\n"
    "ds_write2_b64 v248, v[224:225], v[228:229] offset0:0 offset1:32\n"
    "v_mfma_f32_16x16x32_bf16 v[132:135], a[76:79], a[28:31], v[116:119]\n"
    "ds_read_b128 a[84:87], v232 offset:12544\n"
    "v_mfma_f32_16x16x32_bf16 v[132:135], a[72:75], a[24:27], v[132:135]\n"
    "ds_read_b128 a[88:91], v232 offset:12800\n"
    "v_mfma_f32_16x16x32_bf16 v[132:135], a[68:71], a[20:23], v[132:135]\n"
    "ds_read_b128 a[92:95], v232 offset:13056\n"
    "v_mfma_f32_16x16x32_bf16 v[132:135], a[64:67], a[16:19], v[132:135]\n"
    "v_exp_f32 v128, v128\n"
    "v_mfma_f32_16x16x32_bf16 v[136:139], a[64:67], a[32:35], v[120:123]\n"
    "v_exp_f32 v129, v129\n"
    "v_mfma_f32_16x16x32_bf16 v[136:139], a[68:71], a[36:39], v[136:139]\n"
    "v_exp_f32 v130, v130\n"
    "v_mfma_f32_16x16x32_bf16 v[136:139], a[72:75], a[40:43], v[136:139]\n"
    "v_exp_f32 v131, v131\n"
    "v_mfma_f32_16x16x32_bf16 v[136:139], a[76:79], a[44:47], v[136:139]\n"
    "ds_read_b64_tr_b16 a[112:113], v233 offset:768\n"
    "v_cvt_pk_bf16_f32 v208, v128, v129\n"
    "v_mfma_f32_16x16x32_bf16 v[140:143], a[76:79], a[60:63], v[124:127]\n"
    "ds_read_b64_tr_b16 a[114:115], v233 offset:4864\n"
    "v_cvt_pk_bf16_f32 v209, v130, v131\n"
    "v_mfma_f32_16x16x32_bf16 v[140:143], a[72:75], a[56:59], v[140:143]\n"
    "ds_read_b64_tr_b16 a[116:117], v233 offset:8960\n"
    "v_exp_f32 v132, v132\n"
    "v_mfma_f32_16x16x32_bf16 v[140:143], a[68:71], a[52:55], v[140:143]\n"
    "v_exp_f32 v133, v133\n"
    "v_mfma_f32_16x16x32_bf16 v[140:143], a[64:67], a[48:51], v[140:143]\n"
    "v_exp_f32 v134, v134\n"
    "s_waitcnt lgkmcnt(12)\n"
    "v_mfma_f32_16x16x32_bf16 v[64:67], a[96:99], v[160:163], v[64:67]\n"
    "v_exp_f32 v135, v135\n"
    "v_mfma_f32_16x16x32_bf16 v[64:67], a[100:103], v[176:179], v[64:67]\n"
    "ds_read_b64_tr_b16 a[118:119], v233 offset:13056\n"
    "v_cvt_pk_bf16_f32 v212, v132, v133\n"
    "s_waitcnt lgkmcnt(12)\n"
    "v_mfma_f32_16x16x32_bf16 v[224:227], a[148:151], a[152:155], 0\n"
    "ds_read_b64_tr_b16 a[120:121], v234 offset:768\n"
    "v_cvt_pk_bf16_f32 v213, v134, v135\n"
    "v_mfma_f32_16x16x32_bf16 v[68:71], a[100:103], v[180:183], v[68:71]\n"
    "v_exp_f32 v136, v136\n"
    "v_mfma_f32_16x16x32_bf16 v[68:71], a[96:99], v[164:167], v[68:71]\n"
    "v_exp_f32 v137, v137\n"
    "s_waitcnt lgkmcnt(12)\n"
    "v_mfma_f32_16x16x32_bf16 v[228:231], a[148:151], a[156:159], 0\n"
    "v_exp_f32 v138, v138\n"
    "v_mfma_f32_16x16x32_bf16 v[72:75], a[96:99], v[168:171], v[72:75]\n"
    "ds_read_b64_tr_b16 a[122:123], v234 offset:4864\n"
    "v_cvt_pk_bf16_f32 v224, v224, v225\n"
    "v_cvt_pk_bf16_f32 v225, v226, v227\n"
    "v_mfma_f32_16x16x32_bf16 v[72:75], a[100:103], v[184:187], v[72:75]\n"
    "v_exp_f32 v139, v139\n"
    "v_mfma_f32_16x16x32_bf16 v[76:79], a[100:103], v[188:191], v[76:79]\n"
    "ds_read_b64_tr_b16 a[124:125], v234 offset:8960\n"
    "v_cvt_pk_bf16_f32 v216, v136, v137\n"
    "v_mfma_f32_16x16x32_bf16 v[76:79], a[96:99], v[172:175], v[76:79]\n"
    "v_cvt_pk_bf16_f32 v228, v228, v229\n"
    "v_cvt_pk_bf16_f32 v229, v230, v231\n"
    "s_memrealtime s[90:91]\n"
    "v_mfma_f32_16x16x32_bf16 v[92:95], a[104:107], v[172:175], v[92:95]\n"
    "ds_write2_b64 v247, v[224:225], v[228:229] offset0:64 offset1:96\n"
    "v_mfma_f32_16x16x32_bf16 v[92:95], a[108:111], v[188:191], v[92:95]\n"
    "ds_read_b64_tr_b16 a[126:127], v234 offset:13056\n"
    "v_cvt_pk_bf16_f32 v217, v138, v139\n"
    "s_waitcnt lgkmcnt(15)\n"
    "v_mfma_f32_16x16x32_bf16 v[224:227], a[148:151], a[160:163], 0\n"
    "v_exp_f32 v140, v140\n"
    "v_mfma_f32_16x16x32_bf16 v[88:91], a[108:111], v[184:187], v[88:91]\n"
    "v_exp_f32 v141, v141\n"
    "v_mfma_f32_16x16x32_bf16 v[88:91], a[104:107], v[168:171], v[88:91]\n"
    "v_exp_f32 v142, v142\n"
    "s_waitcnt lgkmcnt(14)\n"
    "v_mfma_f32_16x16x32_bf16 v[228:231], a[148:151], a[164:167], 0\n"
    "v_exp_f32 v143, v143\n"
    "v_mfma_f32_16x16x32_bf16 v[84:87], a[104:107], v[164:167], v[84:87]\n"
    "v_cvt_pk_bf16_f32 v224, v224, v225\n"
    "v_cvt_pk_bf16_f32 v225, v226, v227\n"
    "v_cvt_pk_bf16_f32 v220, v140, v141\n"
    "v_mfma_f32_16x16x32_bf16 v[84:87], a[108:111], v[180:183], v[84:87]\n"
    "v_cvt_pk_bf16_f32 v221, v142, v143\n"
    "v_mfma_f32_16x16x32_bf16 v[80:83], a[108:111], v[176:179], v[80:83]\n"
    "v_mfma_f32_16x16x32_bf16 v[80:83], a[104:107], v[160:163], v[80:83]\n"
    "v_cvt_pk_bf16_f32 v228, v228, v229\n"
    "v_cvt_pk_bf16_f32 v229, v230, v231\n"
    "v_add_u32 v233, s60, v236\n"
    "s_add_u32 s60, s60, 16384\n"
    "s_cmp_eq_u32 s60, 49152\n"
    "s_cselect_b32 s60, 0, s60\n"
    "v_xor_b32 v234, 32, v233\n"
    "v_add_u32 v232, s60, v235\n"
    "s_waitcnt vmcnt(12) lgkmcnt(0)\n"
    "s_sub_u32 s90, s90, s73\n"
    "s_cmp_ge_i32 s90, 0\n"
    "s_cbranch_scc1 .Lpace7x\n"
    ".Lpace7:\n"
    "s_sleep 1\n"
    "s_memrealtime s[90:91]\n"
    "s_waitcnt lgkmcnt(0)\n"
    "s_sub_u32 s90, s90, s73\n"
    "s_cmp_lt_i32 s90, 8\n"
    "s_cbranch_scc1 .Lpace7\n"
    ".Lpace7x:\n"
    "s_add_u32 s73, s73, s88\n"
    "s_barrier\n"
    "v_mfma_f32_16x16x32_bf16 v[156:159], a[80:83], a[48:51], v[124:127]\n"
    "ds_write2_b64 v248, v[224:225], v[228:229] offset0:64 offset1:96\n"
    "v_mfma_f32_16x16x32_bf16 v[156:159], a[84:87], a[52:55], v[156:159]\n"
    "s_add_u32 m0, s70, s58\n"
    "s_nop 0\n"
    "buffer_load_dwordx4 v237, s[40:43], s56 offen offset:0 nt lds\n"
    "v_mfma_f32_16x16x32_bf16 v[156:159], a[88:91], a[56:59], v[156:159]\n"
    "ds_read_b128 a[64:67], v232 offset:0\n"
    "v_mfma_f32_16x16x32_bf16 v[156:159], a[92:95], a[60:63], v[156:159]\n"
    "ds_read_b128 a[68:71], v232 offset:256\n"
    "v_mfma_f32_16x16x32_bf16 v[152:155], a[92:95], a[44:47], v[120:123]\n"
    "ds_read_b128 a[72:75], v232 offset:512\n"
    "v_add_u32 v247, 0x1000, v247\n"
    "v_add_u32 v248, 0x1000, v248\n"
    "v_mfma_f32_16x16x32_bf16 v[152:155], a[88:91], a[40:43], v[152:155]\n"
    "ds_read_b128 a[76:79], v232 offset:768\n"
    "v_mfma_f32_16x16x32_bf16 v[152:155], a[84:87], a[36:39], v[152:155]\n"
    "ds_read_b64_tr_b16 a[96:97], v233 offset:0\n"
    "v_mfma_f32_16x16x32_bf16 v[152:155], a[80:83], a[32:35], v[152:155]\n"
    "ds_read_b64_tr_b16 a[98:99], v233 offset:4096\n"
    "v_exp_f32 v156, v156\n"
    "v_mfma_f32_16x16x32_bf16 v[148:151], a[80:83], a[16:19], v[116:119]\n"
    "v_exp_f32 v157, v157\n"
    "v_mfma_f32_16x16x32_bf16 v[148:151], a[84:87], a[20:23], v[148:151]\n"
    "ds_read_b128 a[48:51], v241 offset:12288\n"
    "v_mfma_f32_16x16x32_bf16 v[148:151], a[88:91], a[24:27], v[148:151]\n"
    "ds_read_b128 a[52:55], v241 offset:12544\n"
    "v_exp_f32 v158, v158\n"
    "v_mfma_f32_16x16x32_bf16 v[148:151], a[92:95], a[28:31], v[148:151]\n"
    "ds_read_b128 a[56:59], v241 offset:12800\n"
    "v_mfma_f32_16x16x32_bf16 v[144:147], a[92:95], a[12:15], v[112:115]\n"
    "ds_read_b128 a[60:63], v241 offset:13056\n"
    "v_exp_f32 v159, v159\n"
    "v_mfma_f32_16x16x32_bf16 v[144:147], a[88:91], a[8:11], v[144:147]\n"
    "ds_read_b128 a[32:35], v241 offset:8192\n"
    "v_cvt_pk_bf16_f32 v222, v156, v157\n"
    "v_mfma_f32_16x16x32_bf16 v[144:147], a[84:87], a[4:7], v[144:147]\n"
    "ds_read_b128 a[36:39], v241 offset:8448\n"
    "v_cvt_pk_bf16_f32 v223, v158, v159\n"
    "v_mfma_f32_16x16x32_bf16 v[144:147], a[80:83], a[0:3], v[144:147]\n"
    "ds_read_b128 a[40:43], v241 offset:8704\n"
    "v_exp_f32 v152, v152\n"
    "v_mfma_f32_16x16x32_bf16 v[96:99], a[112:115], v[160:163], v[96:99]\n"
    "ds_read_b64_tr_b16 a[100:101], v233 offset:8192\n"
    "v_mfma_f32_16x16x32_bf16 v[96:99], a[116:119], v[176:179], v[96:99]\n"
    "ds_read_b64_tr_b16 a[102:103], v233 offset:12288\n"
    "v_exp_f32 v153, v153\n"
    "v_mfma_f32_16x16x32_bf16 v[100:103], a[116:119], v[180:183], v[100:103]\n"
    "ds_read_b64_tr_b16 a[104:105], v234 offset:0\n"
    "ds_read_b128 a[44:47], v241 offset:8960\n"
    "v_mfma_f32_16x16x32_bf16 v[100:103], a[112:115], v[164:167], v[100:103]\n"
    "ds_read_b64_tr_b16 a[106:107], v234 offset:4096\n"
    "ds_read_b128 a[16:19], v241 offset:4096\n"
    "v_mfma_f32_16x16x32_bf16 v[104:107], a[112:115], v[168:171], v[104:107]\n"
    "ds_read_b64_tr_b16 a[108:109], v234 offset:8192\n"
    "ds_read_b128 a[20:23], v241 offset:4352\n"
    "v_mfma_f32_16x16x32_bf16 v[104:107], a[116:119], v[184:187], v[104:107]\n"
    "ds_read_b64_tr_b16 a[110:111], v234 offset:12288\n"
    "ds_read_b128 a[24:27], v241 offset:4608\n"
    "v_mfma_f32_16x16x32_bf16 v[108:111], a[116:119], v[188:191], v[108:111]\n"
    "ds_read_b128 a[28:31], v241 offset:4864\n"
    "v_exp_f32 v154, v154\n"
    "v_mfma_f32_16x16x32_bf16 v[108:111], a[112:115], v[172:175], v[108:111]\n"
    "ds_read_b128 a[0:3], v241 offset:0\n"
    "v_exp_f32 v155, v155\n"
    "v_mfma_f32_16x16x32_bf16 a[180:183], a[120:123], v[172:175], a[180:183]\n"
    "ds_read_b128 a[4:7], v241 offset:256\n"
    "v_cvt_pk_bf16_f32 v218, v152, v153\n"
    "v_mfma_f32_16x16x32_bf16 a[180:183], a[124:127], v[188:191], a[180:183]\n"
    "ds_read_b128 a[8:11], v241 offset:512\n"
    "v_cvt_pk_bf16_f32 v219, v154, v155\n"
    "v_mfma_f32_16x16x32_bf16 a[176:179], a[124:127], v[184:187], a[176:179]\n"
    "ds_read_b128 a[12:15], v241 offset:768\n"
    "v_exp_f32 v148, v148\n"
    "v_mfma_f32_16x16x32_bf16 a[176:179], a[120:123], v[168:171], a[176:179]\n"
    "v_exp_f32 v149, v149\n"
    "v_mfma_f32_16x16x32_bf16 a[172:175], a[120:123], v[164:167], a[172:175]\n"
    "v_exp_f32 v150, v150\n"
    "v_mfma_f32_16x16x32_bf16 a[172:175], a[124:127], v[180:183], a[172:175]\n"
    "v_exp_f32 v151, v151\n"
    "v_cvt_pk_bf16_f32 v214, v148, v149\n"
    "v_cvt_pk_bf16_f32 v215, v150, v151\n"
    "v_mfma_f32_16x16x32_bf16 a[168:171], a[124:127], v[176:179], a[168:171]\n"
    "buffer_load_dwordx4 v238, s[40:43], s56 offen offset:1024 nt lds\n"
    "v_mfma_f32_16x16x32_bf16 a[168:171], a[120:123], v[160:163], a[168:171]\n"
    "buffer_load_dwordx4 v239, s[40:43], s56 offen offset:2048 nt lds\n"
    "buffer_load_dwordx4 v240, s[40:43], s56 offen offset:3072 nt lds\n"
    "s_add_u32 s56, s56, 16384\n"
    "s_add_u32 s58, s58, 16384\n"
    "s_cmp_eq_u32 s58, 49152\n"
    "s_cselect_b32 s58, 0, s58\n"
    "s_add_u32 s36, s36, 1\n"
    "s_cmp_eq_u32 s36, 3\n"
    "s_cselect_b32 s88, 0, 191\n"
    "s_waitcnt lgkmcnt(0)\n"
    "v_mfma_f32_16x16x32_bf16 v[128:131], a[64:67], a[0:3], 0\n"
    "ds_read_b128 a[80:83], v232 offset:4096\n"
    "v_exp_f32 v144, v144\n"
    "v_mfma_f32_16x16x32_bf16 v[128:131], a[68:71], a[4:7], v[128:131]\n"
    "ds_read_b128 a[84:87], v232 offset:4352\n"
    "s_add_u32 s37, s64, 12288\n"
    "v_exp_f32 v145, v145\n"
    "v_mfma_f32_16x16x32_bf16 v[128:131], a[72:75], a[8:11], v[128:131]\n"
    "ds_read_b128 a[88:91], v232 offset:4608\n"
    "v_exp_f32 v146, v146\n"
    "v_mfma_f32_16x16x32_bf16 v[128:131], a[76:79], a[12:15], v[128:131]\n"
    "ds_read_b128 a[92:95], v232 offset:4864\n"
    "v_exp_f32 v147, v147\n"
    "v_mfma_f32_16x16x32_bf16 v[132:135], a[76:79], a[28:31], 0\n"
    "ds_read_b64_tr_b16 a[112:113], v233 offset:256\n"
    "v_cvt_pk_bf16_f32 v210, v144, v145\n"
    "v_cvt_pk_bf16_f32 v211, v146, v147\n"
    "v_mfma_f32_16x16x32_bf16 v[132:135], a[72:75], a[24:27], v[132:135]\n"
    "ds_read_b64_tr_b16 a[114:115], v233 offset:4352\n"
    "buffer_store_dwordx4 a[184:187], v245, s[52:55], s37 offen offset:0 sc1\n"
    "v_mfma_f32_16x16x32_bf16 v[132:135], a[68:71], a[20:23], v[132:135]\n"
    "ds_read_b64_tr_b16 a[116:117], v233 offset:8448\n"
    "v_mfma_f32_16x16x32_bf16 v[132:135], a[64:67], a[16:19], v[132:135]\n"
    "ds_read_b64_tr_b16 a[118:119], v233 offset:12544\n"
    "buffer_store_dwordx4 a[188:191], v245, s[52:55], s37 offen offset:1024 sc1\n"
    "v_mfma_f32_16x16x32_bf16 v[136:139], a[64:67], a[32:35], 0\n"
    "ds_read_b64_tr_b16 a[120:121], v234 offset:256\n"
    "v_mfma_f32_16x16x32_bf16 v[136:139], a[68:71], a[36:39], v[136:139]\n"
    "ds_read_b64_tr_b16 a[122:123], v234 offset:4352\n"
    "v_mfma_f32_16x16x32_bf16 v[136:139], a[72:75], a[40:43], v[136:139]\n"
    "ds_read_b64_tr_b16 a[124:125], v234 offset:8448\n"
    "v_mfma_f32_16x16x32_bf16 v[136:139], a[76:79], a[44:47], v[136:139]\n"
    "ds_read_b64_tr_b16 a[126:127], v234 offset:12544\n"
    "v_mfma_f32_16x16x32_bf16 v[140:143], a[76:79], a[60:63], 0\n"
    "v_mfma_f32_16x16x32_bf16 v[140:143], a[72:75], a[56:59], v[140:143]\n"
    "buffer_store_dwordx4 a[192:195], v245, s[52:55], s37 offen offset:2048 sc1\n"
    "v_mfma_f32_16x16x32_bf16 v[140:143], a[68:71], a[52:55], v[140:143]\n"
    "v_mfma_f32_16x16x32_bf16 v[140:143], a[64:67], a[48:51], v[140:143]\n"
    "v_mfma_f32_16x16x32_bf16 a[128:131], a[144:147], v[192:195], a[128:131]\n"
    "v_mfma_f32_16x16x32_bf16 a[128:131], a[144:147], v[208:211], a[128:131]\n"
    "v_mfma_f32_16x16x32_bf16 a[132:135], a[144:147], v[196:199], a[132:135]\n"
    "v_mfma_f32_16x16x32_bf16 a[132:135], a[144:147], v[212:215], a[132:135]\n"
    "v_max3_f32 v112, v128, v129, v130\n"
    "v_max3_f32 v116, v132, v133, v134\n"
    "v_max3_f32 v120, v136, v137, v138\n"
    "s_nop 0\n"
    "v_max3_f32 v124, v140, v141, v142\n"
    "v_mfma_f32_16x16x32_bf16 a[136:139], a[144:147], v[200:203], a[136:139]\n"
    "v_max_f32 v112, v112, v131\n"
    "v_max_f32 v116, v116, v135\n"
    "v_max_f32 v120, v120, v139\n"
    "v_mfma_f32_16x16x32_bf16 a[136:139], a[144:147], v[216:219], a[136:139]\n"
    "v_max_f32 v124, v124, v143\n"
    "v_permlane32_swap_b32 v112, v116\n"
    "s_nop 0\n"
    "v_permlane32_swap_b32 v120, v124\n"
    "v_mfma_f32_16x16x32_bf16 a[140:143], a[144:147], v[204:207], a[140:143]\n"
    "v_max_f32 v112, v112, v116\n"
    "v_max_f32 v120, v120, v124\n"
    "s_nop 1\n"
    "v_permlane16_swap_b32 v112, v120\n"
    "v_mfma_f32_16x16x32_bf16 a[140:143], a[144:147], v[220:223], a[140:143]\n"
    "v_min_f32 v112, -v112, -v120\n"
    "v_mov_b32 v120, v112\n"
    "s_nop 1\n"
    "v_permlane16_swap_b32 v112, v120\n"
    "v_mov_b32 v116, v112\n"
    "v_mfma_f32_16x16x32_bf16 v[0:3], a[96:99], v[192:195], v[0:3]\n"
    "v_mov_b32 v124, v120\n"
    "v_permlane32_swap_b32 v112, v116\n"
    "s_nop 0\n"
    "v_permlane32_swap_b32 v120, v124\n"
    "v_mfma_f32_16x16x32_bf16 v[0:3], a[100:103], v[208:211], v[0:3]\n"
    "v_mov_b32 v125, v124\n"
    "v_mov_b32 v126, v124\n"
    "v_mov_b32 v127, v124\n"
    "v_mfma_f32_16x16x32_bf16 v[4:7], a[100:103], v[212:215], v[4:7]\n"
    "v_mov_b32 v121, v120\n"
    "v_mov_b32 v122, v120\n"
    "v_mov_b32 v123, v120\n"
    "v_mfma_f32_16x16x32_bf16 v[4:7], a[96:99], v[196:199], v[4:7]\n"
    "v_mov_b32 v117, v116\n"
    "v_mov_b32 v118, v116\n"
    "v_mov_b32 v119, v116\n"
    "v_mov_b32 v113, v112\n"
    "v_mfma_f32_16x16x32_bf16 v[8:11], a[96:99], v[200:203], v[8:11]\n"
    "v_mov_b32 v114, v112\n"
    "v_mov_b32 v115, v112\n"
    "v_add_f32 v128, v128, v112\n"
    "v_mfma_f32_16x16x32_bf16 v[8:11], a[100:103], v[216:219], v[8:11]\n"
    "buffer_store_dwordx4 a[196:199], v245, s[52:55], s37 offen offset:3072 sc1\n"
    "s_cmp_eq_u32 s36, 1\n"
    "s_cselect_b32 s37, 0, 16777216\n"
    "s_add_u32 s64, s64, s37\n"
    "s_mov_b32 s54, 67108864\n"
    "s_add_u32 m0, s71, s59\n"
    "v_add_f32 v129, v129, v112\n"
    "v_mfma_f32_16x16x32_bf16 v[12:15], a[100:103], v[220:223], v[12:15]\n"
    "v_add_f32 v130, v130, v112\n"
    "v_add_f32 v131, v131, v112\n"
    "v_accvgpr_read_b32 v246, a128\n"
    "v_mfma_f32_16x16x32_bf16 v[12:15], a[96:99], v[204:207], v[12:15]\n"
    "v_exp_f32 v128, v128\n"
    "v_exp_f32 v129, v129\n"
    "v_mfma_f32_16x16x32_bf16 v[28:31], a[104:107], v[204:207], v[28:31]\n"
    "v_exp_f32 v130, v130\n"
    "v_accvgpr_read_b32 v247, a132\n"
    "v_mfma_f32_16x16x32_bf16 v[28:31], a[108:111], v[220:223], v[28:31]\n"
    "v_exp_f32 v131, v131\n"
    "v_cvt_pk_bf16_f32 v160, v128, v129\n"
    "v_mfma_f32_16x16x32_bf16 v[24:27], a[108:111], v[216:219], v[24:27]\n"
    "v_cvt_pk_bf16_f32 v161, v130, v131\n"
    "buffer_load_dwordx4 v237, s[44:47], s57 offen offset:0 lds\n"
    "v_mfma_f32_16x16x32_bf16 v[24:27], a[104:107], v[200:203], v[24:27]\n"
    "buffer_load_dwordx4 v238, s[44:47], s57 offen offset:1024 lds\n"
    "v_add_f32 v132, v132, v116\n"
    "v_add_f32 v133, v133, v116\n"
    "v_mfma_f32_16x16x32_bf16 v[20:23], a[104:107], v[196:199], v[20:23]\n"
    "v_add_f32 v134, v134, v116\n"
    "v_add_f32 v135, v135, v116\n"
    "v_accvgpr_read_b32 v248, a136\n"
    "v_mfma_f32_16x16x32_bf16 v[20:23], a[108:111], v[212:215], v[20:23]\n"
    "buffer_load_dwordx4 v239, s[44:47], s57 offen offset:2048 lds\n"
    "v_accvgpr_read_b32 v249, a140\n"
    "v_mfma_f32_16x16x32_bf16 v[16:19], a[108:111], v[208:211], v[16:19]\n"
    "v_exp_f32 v132, v132\n"
    "v_add_f32 v224, v246, v247\n"
    "v_mfma_f32_16x16x32_bf16 v[16:19], a[104:107], v[192:195], v[16:19]\n"
    "v_exp_f32 v133, v133\n"
    "v_exp_f32 v134, v134\n"
    "s_waitcnt lgkmcnt(8)\n"
    "v_mfma_f32_16x16x32_bf16 v[156:159], a[80:83], a[48:51], v[124:127]\n"
    "ds_read_b128 a[64:67], v232 offset:8192\n"
    "v_exp_f32 v135, v135\n"
    "v_mfma_f32_16x16x32_bf16 v[156:159], a[84:87], a[52:55], v[156:159]\n"
    "ds_read_b128 a[68:71], v232 offset:8448\n"
    "v_cvt_pk_bf16_f32 v164, v132, v133\n"
    "v_cvt_pk_bf16_f32 v165, v134, v135\n"
    "v_mfma_f32_16x16x32_bf16 v[156:159], a[88:91], a[56:59], v[156:159]\n"
    "ds_read_b128 a[72:75], v232 offset:8704\n"
    "v_add_f32 v136, v136, v120\n"
    "v_add_f32 v137, v137, v120\n"
    "v_mfma_f32_16x16x32_bf16 v[156:159], a[92:95], a[60:63], v[156:159]\n"
    "ds_read_b128 a[76:79], v232 offset:8960\n"
    "buffer_load_dwordx4 v240, s[44:47], s57 offen offset:3072 lds\n"
    "v_add_f32 v138, v138, v120\n"
    "v_mfma_f32_16x16x32_bf16 v[152:155], a[92:95], a[44:47], v[120:123]\n"
    "ds_read_b64_tr_b16 a[96:97], v233 offset:512\n"
    "v_add_f32 v139, v139, v120\n"
    "v_add_f32 v225, v248, v249\n"
    "v_mfma_f32_16x16x32_bf16 v[152:155], a[88:91], a[40:43], v[152:155]\n"
    "ds_read_b64_tr_b16 a[98:99], v233 offset:4608\n"
    "v_exp_f32 v136, v136\n"
    "v_mfma_f32_16x16x32_bf16 v[152:155], a[84:87], a[36:39], v[152:155]\n"
    "ds_read_b64_tr_b16 a[100:101], v233 offset:8704\n"
    "v_exp_f32 v137, v137\n"
    "v_mfma_f32_16x16x32_bf16 v[152:155], a[80:83], a[32:35], v[152:155]\n"
    "ds_read_b64_tr_b16 a[102:103], v233 offset:12800\n"
    "v_exp_f32 v138, v138\n"
    "v_add_f32 v224, v224, v225\n"
    "v_mfma_f32_16x16x32_bf16 v[148:151], a[80:83], a[16:19], v[116:119]\n"
    "ds_read_b64_tr_b16 a[104:105], v234 offset:512\n"
    "v_exp_f32 v139, v139\n"
    "v_mfma_f32_16x16x32_bf16 v[148:151], a[84:87], a[20:23], v[148:151]\n"
    "ds_read_b64_tr_b16 a[106:107], v234 offset:4608\n"
    "s_add_u32 s57, s57, 16384\n"
    "s_add_u32 s59, s59, 16384\n"
    "s_cmp_eq_u32 s59, 49152\n"
    "s_cselect_b32 s59, 0, s59\n"
    "v_cvt_pk_bf16_f32 v168, v136, v137\n"
    "v_cvt_pk_bf16_f32 v169, v138, v139\n"
    "v_mfma_f32_16x16x32_bf16 v[148:151], a[88:91], a[24:27], v[148:151]\n"
    "ds_read_b64_tr_b16 a[108:109], v234 offset:8704\n"
    "v_add_f32 v140, v140, v124\n"
    "v_add_f32 v141, v141, v124\n"
    "v_mfma_f32_16x16x32_bf16 v[148:151], a[92:95], a[28:31], v[148:151]\n"
    "ds_read_b64_tr_b16 a[110:111], v234 offset:12800\n"
    "v_add_f32 v142, v142, v124\n"
    "v_add_f32 v143, v143, v124\n"
    "v_cmp_ngt_f32 vcc, s67, v224\n"
    "v_mfma_f32_16x16x32_bf16 v[144:147], a[92:95], a[12:15], v[112:115]\n"
    "v_exp_f32 v140, v140\n"
    "v_mfma_f32_16x16x32_bf16 v[144:147], a[88:91], a[8:11], v[144:147]\n"
    "v_exp_f32 v141, v141\n"
    "v_exp_f32 v142, v142\n"
    "v_mfma_f32_16x16x32_bf16 v[144:147], a[84:87], a[4:7], v[144:147]\n"
    "v_exp_f32 v143, v143\n"
    "v_cvt_pk_bf16_f32 v172, v140, v141\n"
    "v_mfma_f32_16x16x32_bf16 v[144:147], a[80:83], a[0:3], v[144:147]\n"
    "v_cvt_pk_bf16_f32 v173, v142, v143\n"
    "v_exp_f32 v156, v156\n"
    "s_waitcnt lgkmcnt(12)\n"
    "v_mfma_f32_16x16x32_bf16 v[32:35], a[112:115], v[192:195], v[32:35]\n"
    "v_exp_f32 v157, v157\n"
    "v_mfma_f32_16x16x32_bf16 v[32:35], a[116:119], v[208:211], v[32:35]\n"
    "v_exp_f32 v158, v158\n"
    "v_exp_f32 v159, v159\n"
    "v_mfma_f32_16x16x32_bf16 v[36:39], a[116:119], v[212:215], v[36:39]\n"
    "v_cvt_pk_bf16_f32 v174, v156, v157\n"
    "v_cvt_pk_bf16_f32 v175, v158, v159\n"
    "v_mfma_f32_16x16x32_bf16 v[36:39], a[112:115], v[196:199], v[36:39]\n"
    "v_exp_f32 v152, v152\n"
    "v_exp_f32 v153, v153\n"
    "v_mfma_f32_16x16x32_bf16 v[40:43], a[112:115], v[200:203], v[40:43]\n"
    "v_exp_f32 v154, v154\n"
    "v_mfma_f32_16x16x32_bf16 v[40:43], a[116:119], v[216:219], v[40:43]\n"
    "v_exp_f32 v155, v155\n"
    "v_cvt_pk_bf16_f32 v170, v152, v153\n"
    "v_cvt_pk_bf16_f32 v171, v154, v155\n"
    "v_mfma_f32_16x16x32_bf16 v[44:47], a[116:119], v[220:223], v[44:47]\n"
    "v_exp_f32 v148, v148\n"
    "v_mfma_f32_16x16x32_bf16 v[44:47], a[112:115], v[204:207], v[44:47]\n"
    "v_exp_f32 v149, v149\n"
    "v_exp_f32 v150, v150\n"
    "v_mfma_f32_16x16x32_bf16 v[60:63], a[120:123], v[204:207], v[60:63]\n"
    "v_exp_f32 v151, v151\n"
    "v_cvt_pk_bf16_f32 v166, v148, v149\n"
    "v_mfma_f32_16x16x32_bf16 v[60:63], a[124:127], v[220:223], v[60:63]\n"
    "v_cvt_pk_bf16_f32 v167, v150, v151\n"
    "v_exp_f32 v144, v144\n"
    "v_mfma_f32_16x16x32_bf16 v[56:59], a[124:127], v[216:219], v[56:59]\n"
    "v_exp_f32 v145, v145\n"
    "v_exp_f32 v146, v146\n"
    "v_mfma_f32_16x16x32_bf16 v[56:59], a[120:123], v[200:203], v[56:59]\n"
    "v_exp_f32 v147, v147\n"
    "v_cvt_pk_bf16_f32 v162, v144, v145\n"
    "v_mfma_f32_16x16x32_bf16 v[52:55], a[120:123], v[196:199], v[52:55]\n"
    "v_cvt_pk_bf16_f32 v163, v146, v147\n"
    "v_rcp_f32 v246, v246\n"
    "v_mfma_f32_16x16x32_bf16 v[52:55], a[124:127], v[212:215], v[52:55]\n"
    "v_rcp_f32 v247, v247\n"
    "v_mfma_f32_16x16x32_bf16 v[48:51], a[124:127], v[208:211], v[48:51]\n"
    "v_rcp_f32 v248, v248\n"
    "v_rcp_f32 v249, v249\n"
    "s_cmp_lg_u64 vcc, 0\n"
    "s_cselect_b32 s38, s69, 0\n"
    "s_or_b32 s68, s68, s38\n"
    "s_lshl_b32 s69, s69, 1\n"
    "v_mfma_f32_16x16x32_bf16 v[48:51], a[120:123], v[192:195], v[48:51]\n"
    "v_mul_f32 v0, v246, v0\n"
    "v_mul_f32 v1, v246, v1\n"
    "v_mul_f32 v2, v246, v2\n"
    "s_waitcnt lgkmcnt(8)\n"
    "v_mfma_f32_16x16x32_bf16 v[128:131], a[64:67], a[0:3], v[112:115]\n"
    "ds_read_b128 a[80:83], v232 offset:12288\n"
    "v_mul_f32 v3, v246, v3\n"
    "v_cvt_pk_bf16_f32 v0, v0, v1\n"
    "v_mfma_f32_16x16x32_bf16 v[128:131], a[68:71], a[4:7], v[128:131]\n"
    "ds_read_b128 a[84:87], v232 offset:12544\n"
    "v_cvt_pk_bf16_f32 v1, v2, v3\n"
    "v_mul_f32 v4, v247, v4\n"
    "v_mul_f32 v5, v247, v5\n"
    "v_mfma_f32_16x16x32_bf16 v[128:131], a[72:75], a[8:11], v[128:131]\n"
    "ds_read_b128 a[88:91], v232 offset:12800\n"
    "v_mul_f32 v6, v247, v6\n"
    "v_mul_f32 v7, v247, v7\n"
    "v_mfma_f32_16x16x32_bf16 v[128:131], a[76:79], a[12:15], v[128:131]\n"
    "ds_read_b128 a[92:95], v232 offset:13056\n"
    "v_cvt_pk_bf16_f32 v4, v4, v5\n"
    "v_cvt_pk_bf16_f32 v5, v6, v7\n"
    "v_mfma_f32_16x16x32_bf16 v[132:135], a[76:79], a[28:31], v[116:119]\n"
    "ds_write2st64_b64 v242, v[0:1], v[4:5] offset0:0 offset1:8\n"
    "v_mul_f32 v8, v248, v8\n"
    "v_mul_f32 v9, v248, v9\n"
    "v_mfma_f32_16x16x32_bf16 v[132:135], a[72:75], a[24:27], v[132:135]\n"
    "ds_read_b64_tr_b16 a[112:113], v233 offset:768\n"
    "v_mul_f32 v10, v248, v10\n"
    "v_mul_f32 v11, v248, v11\n"
    "v_mfma_f32_16x16x32_bf16 v[132:135], a[68:71], a[20:23], v[132:135]\n"
    "ds_read_b64_tr_b16 a[114:115], v233 offset:4864\n"
    "v_cvt_pk_bf16_f32 v8, v8, v9\n"
    "v_cvt_pk_bf16_f32 v9, v10, v11\n"
    "v_mfma_f32_16x16x32_bf16 v[132:135], a[64:67], a[16:19], v[132:135]\n"
    "ds_read_b64_tr_b16 a[116:117], v233 offset:8960\n"
    "v_exp_f32 v128, v128\n"
    "v_mfma_f32_16x16x32_bf16 v[136:139], a[64:67], a[32:35], v[120:123]\n"
    "ds_read_b64_tr_b16 a[118:119], v233 offset:13056\n"
    "v_exp_f32 v129, v129\n"
    "v_mul_f32 v12, v249, v12\n"
    "v_mfma_f32_16x16x32_bf16 v[136:139], a[68:71], a[36:39], v[136:139]\n"
    "ds_read_b64_tr_b16 a[120:121], v234 offset:768\n"
    "v_exp_f32 v130, v130\n"
    "v_mfma_f32_16x16x32_bf16 v[136:139], a[72:75], a[40:43], v[136:139]\n"
    "ds_read_b64_tr_b16 a[122:123], v234 offset:4864\n"
    "v_exp_f32 v131, v131\n"
    "v_mfma_f32_16x16x32_bf16 v[136:139], a[76:79], a[44:47], v[136:139]\n"
    "ds_read_b64_tr_b16 a[124:125], v234 offset:8960\n"
    "v_cvt_pk_bf16_f32 v176, v128, v129\n"
    "v_cvt_pk_bf16_f32 v177, v130, v131\n"
    "v_mul_f32 v13, v249, v13\n"
    "v_mfma_f32_16x16x32_bf16 v[140:143], a[76:79], a[60:63], v[124:127]\n"
    "ds_read_b64_tr_b16 a[126:127], v234 offset:13056\n"
    "v_exp_f32 v132, v132\n"
    "v_mfma_f32_16x16x32_bf16 v[140:143], a[72:75], a[56:59], v[140:143]\n"
    "v_exp_f32 v133, v133\n"
    "v_mul_f32 v14, v249, v14\n"
    "v_mfma_f32_16x16x32_bf16 v[140:143], a[68:71], a[52:55], v[140:143]\n"
    "v_exp_f32 v134, v134\n"
    "v_exp_f32 v135, v135\n"
    "v_mfma_f32_16x16x32_bf16 v[140:143], a[64:67], a[48:51], v[140:143]\n"
    "v_cvt_pk_bf16_f32 v180, v132, v133\n"
    "v_cvt_pk_bf16_f32 v181, v134, v135\n"
    "v_mul_f32 v15, v249, v15\n"
    "s_waitcnt lgkmcnt(13)\n"
    "v_mfma_f32_16x16x32_bf16 v[64:67], a[96:99], v[192:195], v[64:67]\n"
    "v_exp_f32 v136, v136\n"
    "v_cvt_pk_bf16_f32 v12, v12, v13\n"
    "v_mfma_f32_16x16x32_bf16 v[64:67], a[100:103], v[208:211], v[64:67]\n"
    "v_exp_f32 v137, v137\n"
    "v_cvt_pk_bf16_f32 v13, v14, v15\n"
    "v_mfma_f32_16x16x32_bf16 v[68:71], a[100:103], v[212:215], v[68:71]\n"
    "v_exp_f32 v138, v138\n"
    "v_exp_f32 v139, v139\n"
    "v_mfma_f32_16x16x32_bf16 v[68:71], a[96:99], v[196:199], v[68:71]\n"
    "ds_write2st64_b64 v242, v[8:9], v[12:13] offset0:16 offset1:24\n"
    "v_cvt_pk_bf16_f32 v184, v136, v137\n"
    "v_mfma_f32_16x16x32_bf16 v[72:75], a[96:99], v[200:203], v[72:75]\n"
    "v_mul_f32 v28, v249, v28\n"
    "v_mul_f32 v29, v249, v29\n"
    "v_cvt_pk_bf16_f32 v185, v138, v139\n"
    "v_mfma_f32_16x16x32_bf16 v[72:75], a[100:103], v[216:219], v[72:75]\n"
    "v_mul_f32 v30, v249, v30\n"
    "v_mul_f32 v31, v249, v31\n"
    "v_exp_f32 v140, v140\n"
    "s_memrealtime s[90:91]\n"
    "v_mfma_f32_16x16x32_bf16 v[76:79], a[100:103], v[220:223], v[76:79]\n"
    "v_cvt_pk_bf16_f32 v28, v28, v29\n"
    "v_cvt_pk_bf16_f32 v29, v30, v31\n"
    "v_mul_f32 v24, v248, v24\n"
    "v_mfma_f32_16x16x32_bf16 v[76:79], a[96:99], v[204:207], v[76:79]\n"
    "v_mul_f32 v25, v248, v25\n"
    "v_exp_f32 v141, v141\n"
    "v_mfma_f32_16x16x32_bf16 v[92:95], a[104:107], v[204:207], v[92:95]\n"
    "v_mul_f32 v26, v248, v26\n"
    "v_mul_f32 v27, v248, v27\n"
    "v_cvt_pk_bf16_f32 v24, v24, v25\n"
    "v_mfma_f32_16x16x32_bf16 v[92:95], a[108:111], v[220:223], v[92:95]\n"
    "v_cvt_pk_bf16_f32 v25, v26, v27\n"
    "v_exp_f32 v142, v142\n"
    "v_mfma_f32_16x16x32_bf16 v[88:91], a[108:111], v[216:219], v[88:91]\n"
    "ds_write2st64_b64 v250, v[28:29], v[24:25] offset0:24 offset1:16\n"
    "v_exp_f32 v143, v143\n"
    "v_mfma_f32_16x16x32_bf16 v[88:91], a[104:107], v[200:203], v[88:91]\n"
    "v_mul_f32 v20, v247, v20\n"
    "v_mul_f32 v21, v247, v21\n"
    "v_cvt_pk_bf16_f32 v188, v140, v141\n"
    "v_mfma_f32_16x16x32_bf16 v[84:87], a[104:107], v[196:199], v[84:87]\n"
    "v_mul_f32 v22, v247, v22\n"
    "v_mul_f32 v23, v247, v23\n"
    "v_cvt_pk_bf16_f32 v189, v142, v143\n"
    "v_mfma_f32_16x16x32_bf16 v[84:87], a[108:111], v[212:215], v[84:87]\n"
    "v_cvt_pk_bf16_f32 v20, v20, v21\n"
    "v_cvt_pk_bf16_f32 v21, v22, v23\n"
    "v_mul_f32 v16, v246, v16\n"
    "v_mul_f32 v17, v246, v17\n"
    "v_mfma_f32_16x16x32_bf16 v[80:83], a[108:111], v[208:211], v[80:83]\n"
    "v_mul_f32 v18, v246, v18\n"
    "v_mul_f32 v19, v246, v19\n"
    "v_cvt_pk_bf16_f32 v16, v16, v17\n"
    "v_mfma_f32_16x16x32_bf16 v[80:83], a[104:107], v[192:195], v[80:83]\n"
    "v_cvt_pk_bf16_f32 v17, v18, v19\n"
    "ds_write2st64_b64 v250, v[20:21], v[16:17] offset0:8 offset1:0\n"
    "v_add_u32 v233, s60, v236\n"
    "s_add_u32 s60, s60, 16384\n"
    "s_cmp_eq_u32 s60, 49152\n"
    "s_cselect_b32 s60, 0, s60\n"
    "v_xor_b32 v234, 32, v233\n"
    "v_add_u32 v232, s60, v235\n"
    "s_waitcnt vmcnt(12) lgkmcnt(0)\n"
    "s_sub_u32 s90, s90, s73\n"
    "s_cmp_ge_i32 s90, 0\n"
    "s_cbranch_scc1 .Lpace0x\n"
    ".Lpace0:\n"
    "s_sleep 1\n"
    "s_memrealtime s[90:91]\n"
    "s_waitcnt lgkmcnt(0)\n"
    "s_sub_u32 s90, s90, s73\n"
    "s_cmp_lt_i32 s90, 8\n"
    "s_cbranch_scc1 .Lpace0\n"
    ".Lpace0x:\n"
    "s_add_u32 s73, s73, s88\n"
    "s_barrier\n"
    "v_mfma_f32_16x16x32_bf16 v[156:159], a[80:83], a[48:51], v[124:127]\n"
    "ds_read_b128 a[64:67], v232 offset:0\n"
    "v_mul_f32 v32, v246, v32\n"
    "v_mul_f32 v33, v246, v33\n"
    "v_mul_f32 v34, v246, v34\n"
    "v_mfma_f32_16x16x32_bf16 v[156:159], a[84:87], a[52:55], v[156:159]\n"
    "ds_read_b128 a[68:71], v232 offset:256\n"
    "s_add_u32 m0, s70, s58\n"
    "s_nop 0\n"
    "buffer_load_dwordx4 v237, s[40:43], s56 offen offset:0 nt lds\n"
    "v_mfma_f32_16x16x32_bf16 v[156:159], a[88:91], a[56:59], v[156:159]\n"
    "ds_read_b128 a[72:75], v232 offset:512\n"
    "v_mul_f32 v35, v246, v35\n"
    "v_cvt_pk_bf16_f32 v32, v32, v33\n"
    "v_mfma_f32_16x16x32_bf16 v[156:159], a[92:95], a[60:63], v[156:159]\n"
    "ds_read_b128 a[76:79], v232 offset:768\n"
    "v_cvt_pk_bf16_f32 v33, v34, v35\n"
    "v_mul_f32 v36, v247, v36\n"
    "v_mul_f32 v37, v247, v37\n"
    "v_mfma_f32_16x16x32_bf16 v[152:155], a[92:95], a[44:47], v[120:123]\n"
    "ds_read_b64_tr_b16 a[96:97], v233 offset:0\n"
    "v_mul_f32 v38, v247, v38\n"
    "v_mul_f32 v39, v247, v39\n"
    "v_mfma_f32_16x16x32_bf16 v[152:155], a[88:91], a[40:43], v[152:155]\n"
    "ds_read_b64_tr_b16 a[98:99], v233 offset:4096\n"
    "v_cvt_pk_bf16_f32 v36, v36, v37\n"
    "v_cvt_pk_bf16_f32 v37, v38, v39\n"
    "v_mfma_f32_16x16x32_bf16 v[152:155], a[84:87], a[36:39], v[152:155]\n"
    "ds_write2st64_b64 v251, v[32:33], v[36:37] offset0:0 offset1:8\n"
    "v_mul_f32 v40, v248, v40\n"
    "v_mfma_f32_16x16x32_bf16 v[152:155], a[80:83], a[32:35], v[152:155]\n"
    "ds_read_b64_tr_b16 a[100:101], v233 offset:8192\n"
    "v_exp_f32 v156, v156\n"
    "v_mul_f32 v41, v248, v41\n"
    "v_mfma_f32_16x16x32_bf16 v[148:151], a[80:83], a[16:19], v[116:119]\n"
    "ds_read_b64_tr_b16 a[102:103], v233 offset:12288\n"
    "v_exp_f32 v157, v157\n"
    "v_mfma_f32_16x16x32_bf16 v[148:151], a[84:87], a[20:23], v[148:151]\n"
    "ds_read_b64_tr_b16 a[104:105], v234 offset:0\n"
    "v_exp_f32 v158, v158\n"
    "v_mfma_f32_16x16x32_bf16 v[148:151], a[88:91], a[24:27], v[148:151]\n"
    "ds_read_b64_tr_b16 a[106:107], v234 offset:4096\n"
    "v_mul_f32 v42, v248, v42\n"
    "v_mul_f32 v43, v248, v43\n"
    "v_cvt_pk_bf16_f32 v40, v40, v41\n"
    "v_mfma_f32_16x16x32_bf16 v[148:151], a[92:95], a[28:31], v[148:151]\n"
    "ds_read_b64_tr_b16 a[108:109], v234 offset:8192\n"
    "v_cvt_pk_bf16_f32 v41, v42, v43\n"
    "v_mul_f32 v44, v249, v44\n"
    "v_mfma_f32_16x16x32_bf16 v[144:147], a[92:95], a[12:15], v[112:115]\n"
    "ds_read_b64_tr_b16 a[110:111], v234 offset:12288\n"
    "v_mul_f32 v45, v249, v45\n"
    "v_mul_f32 v46, v249, v46\n"
    "v_mfma_f32_16x16x32_bf16 v[144:147], a[88:91], a[8:11], v[144:147]\n"
    "v_mul_f32 v47, v249, v47\n"
    "v_exp_f32 v159, v159\n"
    "v_cvt_pk_bf16_f32 v190, v156, v157\n"
    "v_mfma_f32_16x16x32_bf16 v[144:147], a[84:87], a[4:7], v[144:147]\n"
    "v_cvt_pk_bf16_f32 v44, v44, v45\n"
    "v_cvt_pk_bf16_f32 v45, v46, v47\n"
    "v_cvt_pk_bf16_f32 v191, v158, v159\n"
    "v_mfma_f32_16x16x32_bf16 v[144:147], a[80:83], a[0:3], v[144:147]\n"
    "ds_write2st64_b64 v251, v[40:41], v[44:45] offset0:16 offset1:24\n"
    "v_mul_f32 v60, v249, v60\n"
    "v_mfma_f32_16x16x32_bf16 v[96:99], a[112:115], v[192:195], v[96:99]\n"
    "v_mul_f32 v61, v249, v61\n"
    "v_exp_f32 v152, v152\n"
    "v_mfma_f32_16x16x32_bf16 v[96:99], a[116:119], v[208:211], v[96:99]\n"
    "v_mul_f32 v62, v249, v62\n"
    "v_mul_f32 v63, v249, v63\n"
    "v_exp_f32 v153, v153\n"
    "v_mfma_f32_16x16x32_bf16 v[100:103], a[116:119], v[212:215], v[100:103]\n"
    "v_cvt_pk_bf16_f32 v60, v60, v61\n"
    "v_cvt_pk_bf16_f32 v61, v62, v63\n"
    "v_mul_f32 v56, v248, v56\n"
    "v_mfma_f32_16x16x32_bf16 v[100:103], a[112:115], v[196:199], v[100:103]\n"
    "v_mul_f32 v57, v248, v57\n"
    "v_exp_f32 v154, v154\n"
    "v_mfma_f32_16x16x32_bf16 v[104:107], a[112:115], v[200:203], v[104:107]\n"
    "v_mul_f32 v58, v248, v58\n"
    "v_mul_f32 v59, v248, v59\n"
    "v_exp_f32 v155, v155\n"
    "v_mfma_f32_16x16x32_bf16 v[104:107], a[116:119], v[216:219], v[104:107]\n"
    "v_cvt_pk_bf16_f32 v56, v56, v57\n"
    "v_cvt_pk_bf16_f32 v57, v58, v59\n"
    "v_cvt_pk_bf16_f32 v186, v152, v153\n"
    "v_mfma_f32_16x16x32_bf16 v[108:111], a[116:119], v[220:223], v[108:111]\n"
    "ds_write2st64_b64 v252, v[60:61], v[56:57] offset0:24 offset1:16\n"
    "v_cvt_pk_bf16_f32 v187, v154, v155\n"
    "v_mfma_f32_16x16x32_bf16 v[108:111], a[112:115], v[204:207], v[108:111]\n"
    "v_mul_f32 v52, v247, v52\n"
    "v_mul_f32 v53, v247, v53\n"
    "v_exp_f32 v148, v148\n"
    "v_mfma_f32_16x16x32_bf16 a[180:183], a[120:123], v[204:207], a[180:183]\n"
    "v_mul_f32 v54, v247, v54\n"
    "v_mul_f32 v55, v247, v55\n"
    "v_cvt_pk_bf16_f32 v52, v52, v53\n"
    "v_mfma_f32_16x16x32_bf16 a[180:183], a[124:127], v[220:223], a[180:183]\n"
    "v_cvt_pk_bf16_f32 v53, v54, v55\n"
    "v_exp_f32 v149, v149\n"
    "v_mfma_f32_16x16x32_bf16 a[176:179], a[124:127], v[216:219], a[176:179]\n"
    "v_mul_f32 v48, v246, v48\n"
    "v_mul_f32 v49, v246, v49\n"
    "v_mul_f32 v50, v246, v50\n"
    "v_mfma_f32_16x16x32_bf16 a[176:179], a[120:123], v[200:203], a[176:179]\n"
    "v_exp_f32 v150, v150\n"
    "buffer_load_dwordx4 v238, s[40:43], s56 offen offset:1024 nt lds\n"
    "v_mfma_f32_16x16x32_bf16 a[172:175], a[120:123], v[196:199], a[172:175]\n"
    "v_exp_f32 v151, v151\n"
    "v_mul_f32 v51, v246, v51\n"
    "v_mfma_f32_16x16x32_bf16 a[172:175], a[124:127], v[212:215], a[172:175]\n"
    "v_cvt_pk_bf16_f32 v182, v148, v149\n"
    "v_cvt_pk_bf16_f32 v183, v150, v151\n"
    "v_cvt_pk_bf16_f32 v48, v48, v49\n"
    "v_mfma_f32_16x16x32_bf16 a[168:171], a[124:127], v[208:211], a[168:171]\n"
    "buffer_load_dwordx4 v239, s[40:43], s56 offen offset:2048 nt lds\n"
    "buffer_load_dwordx4 v240, s[40:43], s56 offen offset:3072 nt lds\n"
    "s_add_u32 s56, s56, 16384\n"
    "s_add_u32 s58, s58, 16384\n"
    "s_cmp_eq_u32 s58, 49152\n"
    "s_cselect_b32 s58, 0, s58\n"
    "v_mfma_f32_16x16x32_bf16 a[168:171], a[120:123], v[192:195], a[168:171]\n"
    "v_cvt_pk_bf16_f32 v49, v50, v51\n"
    "ds_write2st64_b64 v252, v[52:53], v[48:49] offset0:8 offset1:0\n"
    "s_branch .Lloop\n"
    ".p2align 6\n"
    ".Ltail:\n"
    "s_waitcnt lgkmcnt(0)\n"
    "v_mfma_f32_16x16x32_bf16 v[128:131], a[64:67], a[0:3], v[112:115]\n"
    "v_exp_f32 v144, v144\n"
    "v_mfma_f32_16x16x32_bf16 v[128:131], a[68:71], a[4:7], v[128:131]\n"
    "v_exp_f32 v145, v145\n"
    "v_mfma_f32_16x16x32_bf16 v[128:131], a[72:75], a[8:11], v[128:131]\n"
    "v_exp_f32 v146, v146\n"
    "v_mfma_f32_16x16x32_bf16 v[128:131], a[76:79], a[12:15], v[128:131]\n"
    "v_exp_f32 v147, v147\n"
    "v_mfma_f32_16x16x32_bf16 v[132:135], a[76:79], a[28:31], v[116:119]\n"
    "ds_read_b128 a[80:83], v232 offset:4096\n"
    "v_cvt_pk_bf16_f32 v210, v144, v145\n"
    "v_mfma_f32_16x16x32_bf16 v[132:135], a[72:75], a[24:27], v[132:135]\n"
    "ds_read_b128 a[84:87], v232 offset:4352\n"
    "v_cvt_pk_bf16_f32 v211, v146, v147\n"
    "v_mfma_f32_16x16x32_bf16 v[132:135], a[68:71], a[20:23], v[132:135]\n"
    "ds_read_b128 a[88:91], v232 offset:4608\n"
    "v_mfma_f32_16x16x32_bf16 v[132:135], a[64:67], a[16:19], v[132:135]\n"
    "v_exp_f32 v128, v128\n"
    "v_mfma_f32_16x16x32_bf16 v[136:139], a[64:67], a[32:35], v[120:123]\n"
    "v_exp_f32 v129, v129\n"
    "v_mfma_f32_16x16x32_bf16 v[136:139], a[68:71], a[36:39], v[136:139]\n"
    "v_exp_f32 v130, v130\n"
    "v_mfma_f32_16x16x32_bf16 v[136:139], a[72:75], a[40:43], v[136:139]\n"
    "v_exp_f32 v131, v131\n"
    "v_mfma_f32_16x16x32_bf16 v[136:139], a[76:79], a[44:47], v[136:139]\n"
    "ds_read_b128 a[92:95], v232 offset:4864\n"
    "v_cvt_pk_bf16_f32 v160, v128, v129\n"
    "v_mfma_f32_16x16x32_bf16 v[140:143], a[76:79], a[60:63], v[124:127]\n"
    "ds_read_b64_tr_b16 a[112:113], v233 offset:256\n"
    "v_cvt_pk_bf16_f32 v161, v130, v131\n"
    "v_mfma_f32_16x16x32_bf16 v[140:143], a[72:75], a[56:59], v[140:143]\n"
    "v_exp_f32 v132, v132\n"
    "v_mfma_f32_16x16x32_bf16 v[140:143], a[68:71], a[52:55], v[140:143]\n"
    "v_exp_f32 v133, v133\n"
    "v_mfma_f32_16x16x32_bf16 v[140:143], a[64:67], a[48:51], v[140:143]\n"
    "v_exp_f32 v134, v134\n"
    "v_mfma_f32_16x16x32_bf16 a[128:131], a[144:147], v[192:195], a[128:131]\n"
    "v_exp_f32 v135, v135\n"
    "v_mfma_f32_16x16x32_bf16 a[128:131], a[144:147], v[208:211], a[128:131]\n"
    "ds_read_b64_tr_b16 a[114:115], v233 offset:4352\n"
    "v_cvt_pk_bf16_f32 v164, v132, v133\n"
    "v_mfma_f32_16x16x32_bf16 a[132:135], a[144:147], v[196:199], a[132:135]\n"
    "ds_read_b64_tr_b16 a[116:117], v233 offset:8448\n"
    "v_cvt_pk_bf16_f32 v165, v134, v135\n"
    "v_mfma_f32_16x16x32_bf16 a[132:135], a[144:147], v[212:215], a[132:135]\n"
    "v_exp_f32 v136, v136\n"
    "v_mfma_f32_16x16x32_bf16 a[136:139], a[144:147], v[200:203], a[136:139]\n"
    "v_exp_f32 v137, v137\n"
    "v_mfma_f32_16x16x32_bf16 a[136:139], a[144:147], v[216:219], a[136:139]\n"
    "v_exp_f32 v138, v138\n"
    "v_mfma_f32_16x16x32_bf16 a[140:143], a[144:147], v[204:207], a[140:143]\n"
    "v_exp_f32 v139, v139\n"
    "v_mfma_f32_16x16x32_bf16 a[140:143], a[144:147], v[220:223], a[140:143]\n"
    "ds_read_b64_tr_b16 a[118:119], v233 offset:12544\n"
    "v_cvt_pk_bf16_f32 v168, v136, v137\n"
    "v_mfma_f32_16x16x32_bf16 v[0:3], a[96:99], v[192:195], v[0:3]\n"
    "ds_read_b64_tr_b16 a[120:121], v234 offset:256\n"
    "v_cvt_pk_bf16_f32 v169, v138, v139\n"
    "v_mfma_f32_16x16x32_bf16 v[0:3], a[100:103], v[208:211], v[0:3]\n"
    "s_add_u32 m0, s71, s59\n"
    "s_nop 0\n"
    "buffer_load_dwordx4 v237, s[44:47], s57 offen offset:0 lds\n"
    "v_mfma_f32_16x16x32_bf16 v[4:7], a[100:103], v[212:215], v[4:7]\n"
    "v_exp_f32 v140, v140\n"
    "v_mfma_f32_16x16x32_bf16 v[4:7], a[96:99], v[196:199], v[4:7]\n"
    "v_exp_f32 v141, v141\n"
    "v_mfma_f32_16x16x32_bf16 v[8:11], a[96:99], v[200:203], v[8:11]\n"
    "v_exp_f32 v142, v142\n"
    "v_mfma_f32_16x16x32_bf16 v[8:11], a[100:103], v[216:219], v[8:11]\n"
    "v_exp_f32 v143, v143\n"
    "v_mfma_f32_16x16x32_bf16 v[12:15], a[100:103], v[220:223], v[12:15]\n"
    "ds_read_b64_tr_b16 a[122:123], v234 offset:4352\n"
    "v_cvt_pk_bf16_f32 v172, v140, v141\n"
    "v_mfma_f32_16x16x32_bf16 v[12:15], a[96:99], v[204:207], v[12:15]\n"
    "ds_read_b64_tr_b16 a[124:125], v234 offset:8448\n"
    "v_cvt_pk_bf16_f32 v173, v142, v143\n"
    "v_mfma_f32_16x16x32_bf16 v[28:31], a[104:107], v[204:207], v[28:31]\n"
    "buffer_load_dwordx4 v238, s[44:47], s57 offen offset:1024 lds\n"
    "v_mfma_f32_16x16x32_bf16 v[28:31], a[108:111], v[220:223], v[28:31]\n"
    "ds_read_b64_tr_b16 a[126:127], v234 offset:12544\n"
    "v_mfma_f32_16x16x32_bf16 v[24:27], a[108:111], v[216:219], v[24:27]\n"
    "v_mfma_f32_16x16x32_bf16 v[24:27], a[104:107], v[200:203], v[24:27]\n"
    "v_mfma_f32_16x16x32_bf16 v[20:23], a[104:107], v[196:199], v[20:23]\n"
    "v_mfma_f32_16x16x32_bf16 v[20:23], a[108:111], v[212:215], v[20:23]\n"
    "buffer_load_dwordx4 v239, s[44:47], s57 offen offset:2048 lds\n"
    "v_mfma_f32_16x16x32_bf16 v[16:19], a[108:111], v[208:211], v[16:19]\n"
    "v_mfma_f32_16x16x32_bf16 v[16:19], a[104:107], v[192:195], v[16:19]\n"
    "s_waitcnt lgkmcnt(8)\n"
    "v_mfma_f32_16x16x32_bf16 v[156:159], a[80:83], a[48:51], v[124:127]\n"
    "ds_read_b128 a[64:67], v232 offset:8192\n"
    "v_mfma_f32_16x16x32_bf16 v[156:159], a[84:87], a[52:55], v[156:159]\n"
    "ds_read_b128 a[68:71], v232 offset:8448\n"
    "v_mfma_f32_16x16x32_bf16 v[156:159], a[88:91], a[56:59], v[156:159]\n"
    "ds_read_b128 a[72:75], v232 offset:8704\n"
    "v_mfma_f32_16x16x32_bf16 v[156:159], a[92:95], a[60:63], v[156:159]\n"
    "buffer_load_dwordx4 v240, s[44:47], s57 offen offset:3072 lds\n"
    "v_mfma_f32_16x16x32_bf16 v[152:155], a[92:95], a[44:47], v[120:123]\n"
    "ds_read_b128 a[76:79], v232 offset:8960\n"
    "v_mfma_f32_16x16x32_bf16 v[152:155], a[88:91], a[40:43], v[152:155]\n"
    "ds_read_b64_tr_b16 a[96:97], v233 offset:512\n"
    "v_mfma_f32_16x16x32_bf16 v[152:155], a[84:87], a[36:39], v[152:155]\n"
    "ds_read_b64_tr_b16 a[98:99], v233 offset:4608\n"
    "v_mfma_f32_16x16x32_bf16 v[152:155], a[80:83], a[32:35], v[152:155]\n"
    "v_exp_f32 v156, v156\n"
    "v_mfma_f32_16x16x32_bf16 v[148:151], a[80:83], a[16:19], v[116:119]\n"
    "v_exp_f32 v157, v157\n"
    "v_mfma_f32_16x16x32_bf16 v[148:151], a[84:87], a[20:23], v[148:151]\n"
    "s_add_u32 s57, s57, 16384\n"
    "s_add_u32 s59, s59, 16384\n"
    "s_cmp_eq_u32 s59, 49152\n"
    "s_cselect_b32 s59, 0, s59\n"
    "v_exp_f32 v158, v158\n"
    "v_mfma_f32_16x16x32_bf16 v[148:151], a[88:91], a[24:27], v[148:151]\n"
    "v_exp_f32 v159, v159\n"
    "v_mfma_f32_16x16x32_bf16 v[148:151], a[92:95], a[28:31], v[148:151]\n"
    "ds_read_b64_tr_b16 a[100:101], v233 offset:8704\n"
    "v_cvt_pk_bf16_f32 v174, v156, v157\n"
    "v_mfma_f32_16x16x32_bf16 v[144:147], a[92:95], a[12:15], v[112:115]\n"
    "ds_read_b64_tr_b16 a[102:103], v233 offset:12800\n"
    "v_cvt_pk_bf16_f32 v175, v158, v159\n"
    "v_mfma_f32_16x16x32_bf16 v[144:147], a[88:91], a[8:11], v[144:147]\n"
    "v_exp_f32 v152, v152\n"
    "v_mfma_f32_16x16x32_bf16 v[144:147], a[84:87], a[4:7], v[144:147]\n"
    "v_exp_f32 v153, v153\n"
    "v_mfma_f32_16x16x32_bf16 v[144:147], a[80:83], a[0:3], v[144:147]\n"
    "v_exp_f32 v154, v154\n"
    "s_waitcnt lgkmcnt(8)\n"
    "v_mfma_f32_16x16x32_bf16 v[32:35], a[112:115], v[192:195], v[32:35]\n"
    "v_exp_f32 v155, v155\n"
    "v_mfma_f32_16x16x32_bf16 v[32:35], a[116:119], v[208:211], v[32:35]\n"
    "ds_read_b64_tr_b16 a[104:105], v234 offset:512\n"
    "v_cvt_pk_bf16_f32 v170, v152, v153\n"
    "v_mfma_f32_16x16x32_bf16 v[36:39], a[116:119], v[212:215], v[36:39]\n"
    "ds_read_b64_tr_b16 a[106:107], v234 offset:4608\n"
    "v_cvt_pk_bf16_f32 v171, v154, v155\n"
    "v_mfma_f32_16x16x32_bf16 v[36:39], a[112:115], v[196:199], v[36:39]\n"
    "v_exp_f32 v148, v148\n"
    "v_mfma_f32_16x16x32_bf16 v[40:43], a[112:115], v[200:203], v[40:43]\n"
    "v_exp_f32 v149, v149\n"
    "v_mfma_f32_16x16x32_bf16 v[40:43], a[116:119], v[216:219], v[40:43]\n"
    "v_exp_f32 v150, v150\n"
    "v_mfma_f32_16x16x32_bf16 v[44:47], a[116:119], v[220:223], v[44:47]\n"
    "v_exp_f32 v151, v151\n"
    "v_mfma_f32_16x16x32_bf16 v[44:47], a[112:115], v[204:207], v[44:47]\n"
    "ds_read_b64_tr_b16 a[108:109], v234 offset:8704\n"
    "v_cvt_pk_bf16_f32 v166, v148, v149\n"
    "v_mfma_f32_16x16x32_bf16 v[60:63], a[120:123], v[204:207], v[60:63]\n"
    "ds_read_b64_tr_b16 a[110:111], v234 offset:12800\n"
    "v_cvt_pk_bf16_f32 v167, v150, v151\n"
    "v_mfma_f32_16x16x32_bf16 v[60:63], a[124:127], v[220:223], v[60:63]\n"
    "v_exp_f32 v144, v144\n"
    "v_mfma_f32_16x16x32_bf16 v[56:59], a[124:127], v[216:219], v[56:59]\n"
    "v_exp_f32 v145, v145\n"
    "v_mfma_f32_16x16x32_bf16 v[56:59], a[120:123], v[200:203], v[56:59]\n"
    "v_exp_f32 v146, v146\n"
    "v_mfma_f32_16x16x32_bf16 v[52:55], a[120:123], v[196:199], v[52:55]\n"
    "v_exp_f32 v147, v147\n"
    "v_mfma_f32_16x16x32_bf16 v[52:55], a[124:127], v[212:215], v[52:55]\n"
    "v_cvt_pk_bf16_f32 v162, v144, v145\n"
    "v_cvt_pk_bf16_f32 v163, v146, v147\n"
    "v_mfma_f32_16x16x32_bf16 v[48:51], a[124:127], v[208:211], v[48:51]\n"
    "v_mfma_f32_16x16x32_bf16 v[48:51], a[120:123], v[192:195], v[48:51]\n"
    "s_waitcnt lgkmcnt(8)\n"
    "v_mfma_f32_16x16x32_bf16 v[128:131], a[64:67], a[0:3], v[112:115]\n"
    "ds_read_b128 a[184:187], v243 offset:12288\n"
    "v_mfma_f32_16x16x32_bf16 v[128:131], a[68:71], a[4:7], v[128:131]\n"
    "ds_read_b128 a[188:191], v244 offset:13312\n"
    "v_mfma_f32_16x16x32_bf16 v[128:131], a[72:75], a[8:11], v[128:131]\n"
    "ds_read_b128 a[192:195], v243 offset:14336\n"
    "v_mfma_f32_16x16x32_bf16 v[128:131], a[76:79], a[12:15], v[128:131]\n"
    "ds_read_b128 a[196:199], v244 offset:15360\n"
    "v_mfma_f32_16x16x32_bf16 v[132:135], a[76:79], a[28:31], v[116:119]\n"
    "ds_read_b128 a[80:83], v232 offset:12288\n"
    "v_mfma_f32_16x16x32_bf16 v[132:135], a[72:75], a[24:27], v[132:135]\n"
    "ds_read_b128 a[84:87], v232 offset:12544\n"
    "v_mfma_f32_16x16x32_bf16 v[132:135], a[68:71], a[20:23], v[132:135]\n"
    "ds_read_b128 a[88:91], v232 offset:12800\n"
    "v_mfma_f32_16x16x32_bf16 v[132:135], a[64:67], a[16:19], v[132:135]\n"
    "v_exp_f32 v128, v128\n"
    "v_mfma_f32_16x16x32_bf16 v[136:139], a[64:67], a[32:35], v[120:123]\n"
    "v_exp_f32 v129, v129\n"
    "v_mfma_f32_16x16x32_bf16 v[136:139], a[68:71], a[36:39], v[136:139]\n"
    "v_exp_f32 v130, v130\n"
    "v_mfma_f32_16x16x32_bf16 v[136:139], a[72:75], a[40:43], v[136:139]\n"
    "v_exp_f32 v131, v131\n"
    "v_mfma_f32_16x16x32_bf16 v[136:139], a[76:79], a[44:47], v[136:139]\n"
    "ds_read_b128 a[92:95], v232 offset:13056\n"
    "v_cvt_pk_bf16_f32 v176, v128, v129\n"
    "v_mfma_f32_16x16x32_bf16 v[140:143], a[76:79], a[60:63], v[124:127]\n"
    "ds_read_b64_tr_b16 a[112:113], v233 offset:768\n"
    "v_cvt_pk_bf16_f32 v177, v130, v131\n"
    "v_mfma_f32_16x16x32_bf16 v[140:143], a[72:75], a[56:59], v[140:143]\n"
    "v_exp_f32 v132, v132\n"
    "v_mfma_f32_16x16x32_bf16 v[140:143], a[68:71], a[52:55], v[140:143]\n"
    "v_exp_f32 v133, v133\n"
    "v_mfma_f32_16x16x32_bf16 v[140:143], a[64:67], a[48:51], v[140:143]\n"
    "ds_read_b64_tr_b16 a[114:115], v233 offset:4864\n"
    "s_waitcnt lgkmcnt(10)\n"
    "v_mfma_f32_16x16x32_bf16 v[64:67], a[96:99], v[192:195], v[64:67]\n"
    "ds_read_b64_tr_b16 a[116:117], v233 offset:8960\n"
    "v_mfma_f32_16x16x32_bf16 v[64:67], a[100:103], v[208:211], v[64:67]\n"
    "ds_read_b64_tr_b16 a[118:119], v233 offset:13056\n"
    "v_mfma_f32_16x16x32_bf16 v[68:71], a[100:103], v[212:215], v[68:71]\n"
    "ds_read_b64_tr_b16 a[120:121], v234 offset:768\n"
    "v_mfma_f32_16x16x32_bf16 v[68:71], a[96:99], v[196:199], v[68:71]\n"
    "ds_read_b64_tr_b16 a[122:123], v234 offset:4864\n"
    "v_mfma_f32_16x16x32_bf16 v[72:75], a[96:99], v[200:203], v[72:75]\n"
    "ds_read_b64_tr_b16 a[124:125], v234 offset:8960\n"
    "v_mfma_f32_16x16x32_bf16 v[72:75], a[100:103], v[216:219], v[72:75]\n"
    "ds_read_b64_tr_b16 a[126:127], v234 offset:13056\n"
    "v_mfma_f32_16x16x32_bf16 v[76:79], a[100:103], v[220:223], v[76:79]\n"
    "v_exp_f32 v134, v134\n"
    "v_mfma_f32_16x16x32_bf16 v[76:79], a[96:99], v[204:207], v[76:79]\n"
    "v_exp_f32 v135, v135\n"
    "v_mfma_f32_16x16x32_bf16 v[92:95], a[104:107], v[204:207], v[92:95]\n"
    "v_cvt_pk_bf16_f32 v180, v132, v133\n"
    "v_cvt_pk_bf16_f32 v181, v134, v135\n"
    "v_mfma_f32_16x16x32_bf16 v[92:95], a[108:111], v[220:223], v[92:95]\n"
    "v_exp_f32 v136, v136\n"
    "v_mfma_f32_16x16x32_bf16 v[88:91], a[108:111], v[216:219], v[88:91]\n"
    "v_exp_f32 v137, v137\n"
    "v_mfma_f32_16x16x32_bf16 v[88:91], a[104:107], v[200:203], v[88:91]\n"
    "v_exp_f32 v138, v138\n"
    "v_mfma_f32_16x16x32_bf16 v[84:87], a[104:107], v[196:199], v[84:87]\n"
    "v_exp_f32 v139, v139\n"
    "v_mfma_f32_16x16x32_bf16 v[84:87], a[108:111], v[212:215], v[84:87]\n"
    "v_cvt_pk_bf16_f32 v184, v136, v137\n"
    "v_cvt_pk_bf16_f32 v185, v138, v139\n"
    "v_mfma_f32_16x16x32_bf16 v[80:83], a[108:111], v[208:211], v[80:83]\n"
    "v_exp_f32 v140, v140\n"
    "v_mfma_f32_16x16x32_bf16 v[80:83], a[104:107], v[192:195], v[80:83]\n"
    "v_exp_f32 v141, v141\n"
    "v_add_u32 v233, s60, v236\n"
    "s_add_u32 s60, s60, 16384\n"
    "s_cmp_eq_u32 s60, 49152\n"
    "s_cselect_b32 s60, 0, s60\n"
    "v_xor_b32 v234, 32, v233\n"
    "v_add_u32 v232, s60, v235\n"
    "s_waitcnt vmcnt(8) lgkmcnt(0)\n"
    "s_barrier\n"
    "v_mfma_f32_16x16x32_bf16 v[156:159], a[80:83], a[48:51], v[124:127]\n"
    "v_exp_f32 v142, v142\n"
    "v_mfma_f32_16x16x32_bf16 v[156:159], a[84:87], a[52:55], v[156:159]\n"
    "s_add_u32 m0, s70, s58\n"
    "v_exp_f32 v143, v143\n"
    "v_mfma_f32_16x16x32_bf16 v[156:159], a[88:91], a[56:59], v[156:159]\n"
    "ds_read_b128 a[64:67], v232 offset:0\n"
    "v_cvt_pk_bf16_f32 v188, v140, v141\n"
    "v_mfma_f32_16x16x32_bf16 v[156:159], a[92:95], a[60:63], v[156:159]\n"
    "ds_read_b128 a[68:71], v232 offset:256\n"
    "v_cvt_pk_bf16_f32 v189, v142, v143\n"
    "v_mfma_f32_16x16x32_bf16 v[152:155], a[92:95], a[44:47], v[120:123]\n"
    "buffer_load_dwordx4 v237, s[40:43], s56 offen offset:0 nt lds\n"
    "v_mfma_f32_16x16x32_bf16 v[152:155], a[88:91], a[40:43], v[152:155]\n"
    "ds_read_b128 a[72:75], v232 offset:512\n"
    "v_mfma_f32_16x16x32_bf16 v[152:155], a[84:87], a[36:39], v[152:155]\n"
    "ds_read_b128 a[76:79], v232 offset:768\n"
    "v_mfma_f32_16x16x32_bf16 v[152:155], a[80:83], a[32:35], v[152:155]\n"
    "v_exp_f32 v156, v156\n"
    "v_mfma_f32_16x16x32_bf16 v[148:151], a[80:83], a[16:19], v[116:119]\n"
    "v_exp_f32 v157, v157\n"
    "v_mfma_f32_16x16x32_bf16 v[148:151], a[84:87], a[20:23], v[148:151]\n"
    "v_exp_f32 v158, v158\n"
    "v_mfma_f32_16x16x32_bf16 v[148:151], a[88:91], a[24:27], v[148:151]\n"
    "v_exp_f32 v159, v159\n"
    "v_mfma_f32_16x16x32_bf16 v[148:151], a[92:95], a[28:31], v[148:151]\n"
    "ds_read_b64_tr_b16 a[96:97], v233 offset:0\n"
    "v_cvt_pk_bf16_f32 v190, v156, v157\n"
    "v_mfma_f32_16x16x32_bf16 v[144:147], a[92:95], a[12:15], v[112:115]\n"
    "ds_read_b64_tr_b16 a[98:99], v233 offset:4096\n"
    "v_cvt_pk_bf16_f32 v191, v158, v159\n"
    "v_mfma_f32_16x16x32_bf16 v[144:147], a[88:91], a[8:11], v[144:147]\n"
    "v_exp_f32 v152, v152\n"
    "v_mfma_f32_16x16x32_bf16 v[144:147], a[84:87], a[4:7], v[144:147]\n"
    "v_exp_f32 v153, v153\n"
    "v_mfma_f32_16x16x32_bf16 v[144:147], a[80:83], a[0:3], v[144:147]\n"
    "v_exp_f32 v154, v154\n"
    "v_mfma_f32_16x16x32_bf16 v[96:99], a[112:115], v[192:195], v[96:99]\n"
    "ds_read_b64_tr_b16 a[100:101], v233 offset:8192\n"
    "v_mfma_f32_16x16x32_bf16 v[96:99], a[116:119], v[208:211], v[96:99]\n"
    "ds_read_b64_tr_b16 a[102:103], v233 offset:12288\n"
    "v_mfma_f32_16x16x32_bf16 v[100:103], a[116:119], v[212:215], v[100:103]\n"
    "ds_read_b64_tr_b16 a[104:105], v234 offset:0\n"
    "v_mfma_f32_16x16x32_bf16 v[100:103], a[112:115], v[196:199], v[100:103]\n"
    "ds_read_b64_tr_b16 a[106:107], v234 offset:4096\n"
    "v_mfma_f32_16x16x32_bf16 v[104:107], a[112:115], v[200:203], v[104:107]\n"
    "ds_read_b64_tr_b16 a[108:109], v234 offset:8192\n"
    "v_mfma_f32_16x16x32_bf16 v[104:107], a[116:119], v[216:219], v[104:107]\n"
    "ds_read_b64_tr_b16 a[110:111], v234 offset:12288\n"
    "v_mfma_f32_16x16x32_bf16 v[108:111], a[116:119], v[220:223], v[108:111]\n"
    "v_exp_f32 v155, v155\n"
    "v_mfma_f32_16x16x32_bf16 v[108:111], a[112:115], v[204:207], v[108:111]\n"
    "v_cvt_pk_bf16_f32 v186, v152, v153\n"
    "v_cvt_pk_bf16_f32 v187, v154, v155\n"
    "v_mfma_f32_16x16x32_bf16 a[180:183], a[120:123], v[204:207], a[180:183]\n"
    "v_exp_f32 v148, v148\n"
    "v_mfma_f32_16x16x32_bf16 a[180:183], a[124:127], v[220:223], a[180:183]\n"
    "v_exp_f32 v149, v149\n"
    "v_mfma_f32_16x16x32_bf16 a[176:179], a[124:127], v[216:219], a[176:179]\n"
    "v_exp_f32 v150, v150\n"
    "v_mfma_f32_16x16x32_bf16 a[176:179], a[120:123], v[200:203], a[176:179]\n"
    "buffer_load_dwordx4 v238, s[40:43], s56 offen offset:1024 nt lds\n"
    "v_mfma_f32_16x16x32_bf16 a[172:175], a[120:123], v[196:199], a[172:175]\n"
    "v_exp_f32 v151, v151\n"
    "v_mfma_f32_16x16x32_bf16 a[172:175], a[124:127], v[212:215], a[172:175]\n"
    "v_cvt_pk_bf16_f32 v182, v148, v149\n"
    "v_cvt_pk_bf16_f32 v183, v150, v151\n"
    "v_mfma_f32_16x16x32_bf16 a[168:171], a[124:127], v[208:211], a[168:171]\n"
    "buffer_load_dwordx4 v239, s[40:43], s56 offen offset:2048 nt lds\n"
    "v_mfma_f32_16x16x32_bf16 a[168:171], a[120:123], v[192:195], a[168:171]\n"
    "buffer_load_dwordx4 v240, s[40:43], s56 offen offset:3072 nt lds\n"
    "s_add_u32 s56, s56, 33439744\n"
    "s_add_u32 s58, s58, 16384\n"
    "s_cmp_eq_u32 s58, 49152\n"
    "s_cselect_b32 s58, 0, s58\n"
    "s_waitcnt lgkmcnt(0)\n"
    "v_mfma_f32_16x16x32_bf16 v[128:131], a[64:67], a[0:3], v[112:115]\n"
    "v_exp_f32 v144, v144\n"
    "v_mfma_f32_16x16x32_bf16 v[128:131], a[68:71], a[4:7], v[128:131]\n"
    "s_add_u32 s37, s64, 4096\n"
    "v_exp_f32 v145, v145\n"
    "v_mfma_f32_16x16x32_bf16 v[128:131], a[72:75], a[8:11], v[128:131]\n"
    "v_exp_f32 v146, v146\n"
    "v_mfma_f32_16x16x32_bf16 v[128:131], a[76:79], a[12:15], v[128:131]\n"
    "v_exp_f32 v147, v147\n"
    "v_mfma_f32_16x16x32_bf16 v[132:135], a[76:79], a[28:31], v[116:119]\n"
    "ds_read_b128 a[80:83], v232 offset:4096\n"
    "v_cvt_pk_bf16_f32 v178, v144, v145\n"
    "v_mfma_f32_16x16x32_bf16 v[132:135], a[72:75], a[24:27], v[132:135]\n"
    "ds_read_b128 a[84:87], v232 offset:4352\n"
    "v_cvt_pk_bf16_f32 v179, v146, v147\n"
    "v_mfma_f32_16x16x32_bf16 v[132:135], a[68:71], a[20:23], v[132:135]\n"
    "buffer_store_dwordx4 a[200:203], v245, s[52:55], s37 offen offset:0 sc1\n"
    "v_mfma_f32_16x16x32_bf16 v[132:135], a[64:67], a[16:19], v[132:135]\n"
    "buffer_store_dwordx4 a[204:207], v245, s[52:55], s37 offen offset:1024 sc1\n"
    "v_mfma_f32_16x16x32_bf16 v[136:139], a[64:67], a[32:35], v[120:123]\n"
    "v_exp_f32 v128, v128\n"
    "v_mfma_f32_16x16x32_bf16 v[136:139], a[68:71], a[36:39], v[136:139]\n"
    "v_exp_f32 v129, v129\n"
    "v_mfma_f32_16x16x32_bf16 v[136:139], a[72:75], a[40:43], v[136:139]\n"
    "v_exp_f32 v130, v130\n"
    "v_mfma_f32_16x16x32_bf16 v[136:139], a[76:79], a[44:47], v[136:139]\n"
    "v_exp_f32 v131, v131\n"
    "v_mfma_f32_16x16x32_bf16 v[140:143], a[76:79], a[60:63], v[124:127]\n"
    "ds_read_b128 a[88:91], v232 offset:4608\n"
    "v_cvt_pk_bf16_f32 v192, v128, v129\n"
    "v_mfma_f32_16x16x32_bf16 v[140:143], a[72:75], a[56:59], v[140:143]\n"
    "ds_read_b128 a[92:95], v232 offset:4864\n"
    "v_cvt_pk_bf16_f32 v193, v130, v131\n"
    "v_mfma_f32_16x16x32_bf16 v[140:143], a[68:71], a[52:55], v[140:143]\n"
    "buffer_store_dwordx4 a[208:211], v245, s[52:55], s37 offen offset:2048 sc1\n"
    "v_mfma_f32_16x16x32_bf16 v[140:143], a[64:67], a[48:51], v[140:143]\n"
    "v_exp_f32 v132, v132\n"
    "v_mfma_f32_16x16x32_bf16 a[128:131], a[144:147], v[160:163], a[128:131]\n"
    "v_exp_f32 v133, v133\n"
    "v_mfma_f32_16x16x32_bf16 a[128:131], a[144:147], v[176:179], a[128:131]\n"
    "v_exp_f32 v134, v134\n"
    "v_mfma_f32_16x16x32_bf16 a[132:135], a[144:147], v[164:167], a[132:135]\n"
    "v_exp_f32 v135, v135\n"
    "v_mfma_f32_16x16x32_bf16 a[132:135], a[144:147], v[180:183], a[132:135]\n"
    "ds_read_b64_tr_b16 a[112:113], v233 offset:256\n"
    "v_cvt_pk_bf16_f32 v196, v132, v133\n"
    "v_mfma_f32_16x16x32_bf16 a[136:139], a[144:147], v[168:171], a[136:139]\n"
    "ds_read_b64_tr_b16 a[114:115], v233 offset:4352\n"
    "v_cvt_pk_bf16_f32 v197, v134, v135\n"
    "v_mfma_f32_16x16x32_bf16 a[136:139], a[144:147], v[184:187], a[136:139]\n"
    "buffer_store_dwordx4 a[212:215], v245, s[52:55], s37 offen offset:3072 sc1\n"
    "v_mfma_f32_16x16x32_bf16 a[140:143], a[144:147], v[172:175], a[140:143]\n"
    "v_exp_f32 v136, v136\n"
    "v_mfma_f32_16x16x32_bf16 a[140:143], a[144:147], v[188:191], a[140:143]\n"
    "v_exp_f32 v137, v137\n"
    "v_mfma_f32_16x16x32_bf16 v[0:3], a[96:99], v[160:163], v[0:3]\n"
    "v_exp_f32 v138, v138\n"
    "v_mfma_f32_16x16x32_bf16 v[0:3], a[100:103], v[176:179], v[0:3]\n"
    "s_add_u32 m0, s71, s59\n"
    "s_nop 0\n"
    "buffer_load_dwordx4 v237, s[44:47], s57 offen offset:0 lds\n"
    "v_mfma_f32_16x16x32_bf16 v[4:7], a[100:103], v[180:183], v[4:7]\n"
    "v_exp_f32 v139, v139\n"
    "v_mfma_f32_16x16x32_bf16 v[4:7], a[96:99], v[164:167], v[4:7]\n"
    "ds_read_b64_tr_b16 a[116:117], v233 offset:8448\n"
    "v_cvt_pk_bf16_f32 v200, v136, v137\n"
    "v_mfma_f32_16x16x32_bf16 v[8:11], a[96:99], v[168:171], v[8:11]\n"
    "ds_read_b64_tr_b16 a[118:119], v233 offset:12544\n"
    "v_cvt_pk_bf16_f32 v201, v138, v139\n"
    "v_mfma_f32_16x16x32_bf16 v[8:11], a[100:103], v[184:187], v[8:11]\n"
    "v_exp_f32 v140, v140\n"
    "v_mfma_f32_16x16x32_bf16 v[12:15], a[100:103], v[188:191], v[12:15]\n"
    "v_exp_f32 v141, v141\n"
    "v_mfma_f32_16x16x32_bf16 v[12:15], a[96:99], v[172:175], v[12:15]\n"
    "buffer_load_dwordx4 v238, s[44:47], s57 offen offset:1024 lds\n"
    "v_mfma_f32_16x16x32_bf16 v[28:31], a[104:107], v[172:175], v[28:31]\n"
    "v_exp_f32 v142, v142\n"
    "v_mfma_f32_16x16x32_bf16 v[28:31], a[108:111], v[188:191], v[28:31]\n"
    "v_exp_f32 v143, v143\n"
    "v_mfma_f32_16x16x32_bf16 v[24:27], a[108:111], v[184:187], v[24:27]\n"
    "ds_read_b64_tr_b16 a[120:121], v234 offset:256\n"
    "v_cvt_pk_bf16_f32 v204, v140, v141\n"
    "v_mfma_f32_16x16x32_bf16 v[24:27], a[104:107], v[168:171], v[24:27]\n"
    "ds_read_b64_tr_b16 a[122:123], v234 offset:4352\n"
    "v_cvt_pk_bf16_f32 v205, v142, v143\n"
    "v_mfma_f32_16x16x32_bf16 v[20:23], a[104:107], v[164:167], v[20:23]\n"
    "ds_read_b64_tr_b16 a[124:125], v234 offset:8448\n"
    "v_mfma_f32_16x16x32_bf16 v[20:23], a[108:111], v[180:183], v[20:23]\n"
    "ds_read_b64_tr_b16 a[126:127], v234 offset:12544\n"
    "v_mfma_f32_16x16x32_bf16 v[16:19], a[108:111], v[176:179], v[16:19]\n"
    "buffer_load_dwordx4 v239, s[44:47], s57 offen offset:2048 lds\n"
    "v_mfma_f32_16x16x32_bf16 v[16:19], a[104:107], v[160:163], v[16:19]\n"
    "s_waitcnt lgkmcnt(8)\n"
    "v_mfma_f32_16x16x32_bf16 v[156:159], a[80:83], a[48:51], v[124:127]\n"
    "ds_read_b128 a[64:67], v232 offset:8192\n"
    "v_mfma_f32_16x16x32_bf16 v[156:159], a[84:87], a[52:55], v[156:159]\n"
    "ds_read_b128 a[68:71], v232 offset:8448\n"
    "v_mfma_f32_16x16x32_bf16 v[156:159], a[88:91], a[56:59], v[156:159]\n"
    "ds_read_b128 a[72:75], v232 offset:8704\n"
    "v_mfma_f32_16x16x32_bf16 v[156:159], a[92:95], a[60:63], v[156:159]\n"
    "buffer_load_dwordx4 v240, s[44:47], s57 offen offset:3072 lds\n"
    "v_mfma_f32_16x16x32_bf16 v[152:155], a[92:95], a[44:47], v[120:123]\n"
    "ds_read_b128 a[76:79], v232 offset:8960\n"
    "v_mfma_f32_16x16x32_bf16 v[152:155], a[88:91], a[40:43], v[152:155]\n"
    "ds_read_b64_tr_b16 a[96:97], v233 offset:512\n"
    "v_mfma_f32_16x16x32_bf16 v[152:155], a[84:87], a[36:39], v[152:155]\n"
    "ds_read_b64_tr_b16 a[98:99], v233 offset:4608\n"
    "v_mfma_f32_16x16x32_bf16 v[152:155], a[80:83], a[32:35], v[152:155]\n"
    "v_exp_f32 v156, v156\n"
    "v_mfma_f32_16x16x32_bf16 v[148:151], a[80:83], a[16:19], v[116:119]\n"
    "v_exp_f32 v157, v157\n"
    "v_mfma_f32_16x16x32_bf16 v[148:151], a[84:87], a[20:23], v[148:151]\n"
    "s_add_u32 s57, s57, 16384\n"
    "s_add_u32 s59, s59, 16384\n"
    "s_cmp_eq_u32 s59, 49152\n"
    "s_cselect_b32 s59, 0, s59\n"
    "v_exp_f32 v158, v158\n"
    "v_mfma_f32_16x16x32_bf16 v[148:151], a[88:91], a[24:27], v[148:151]\n"
    "v_exp_f32 v159, v159\n"
    "v_mfma_f32_16x16x32_bf16 v[148:151], a[92:95], a[28:31], v[148:151]\n"
    "ds_read_b64_tr_b16 a[100:101], v233 offset:8704\n"
    "v_cvt_pk_bf16_f32 v206, v156, v157\n"
    "v_mfma_f32_16x16x32_bf16 v[144:147], a[92:95], a[12:15], v[112:115]\n"
    "ds_read_b64_tr_b16 a[102:103], v233 offset:12800\n"
    "v_cvt_pk_bf16_f32 v207, v158, v159\n"
    "v_mfma_f32_16x16x32_bf16 v[144:147], a[88:91], a[8:11], v[144:147]\n"
    "v_exp_f32 v152, v152\n"
    "v_mfma_f32_16x16x32_bf16 v[144:147], a[84:87], a[4:7], v[144:147]\n"
    "v_exp_f32 v153, v153\n"
    "v_mfma_f32_16x16x32_bf16 v[144:147], a[80:83], a[0:3], v[144:147]\n"
    "v_exp_f32 v154, v154\n"
    "s_waitcnt lgkmcnt(8)\n"
    "v_mfma_f32_16x16x32_bf16 v[32:35], a[112:115], v[160:163], v[32:35]\n"
    "v_exp_f32 v155, v155\n"
    "v_mfma_f32_16x16x32_bf16 v[32:35], a[116:119], v[176:179], v[32:35]\n"
    "ds_read_b64_tr_b16 a[104:105], v234 offset:512\n"
    "v_cvt_pk_bf16_f32 v202, v152, v153\n"
    "v_mfma_f32_16x16x32_bf16 v[36:39], a[116:119], v[180:183], v[36:39]\n"
    "ds_read_b64_tr_b16 a[106:107], v234 offset:4608\n"
    "v_cvt_pk_bf16_f32 v203, v154, v155\n"
    "v_mfma_f32_16x16x32_bf16 v[36:39], a[112:115], v[164:167], v[36:39]\n"
    "v_exp_f32 v148, v148\n"
    "v_mfma_f32_16x16x32_bf16 v[40:43], a[112:115], v[168:171], v[40:43]\n"
    "v_exp_f32 v149, v149\n"
    "v_mfma_f32_16x16x32_bf16 v[40:43], a[116:119], v[184:187], v[40:43]\n"
    "v_exp_f32 v150, v150\n"
    "v_mfma_f32_16x16x32_bf16 v[44:47], a[116:119], v[188:191], v[44:47]\n"
    "v_exp_f32 v151, v151\n"
    "v_mfma_f32_16x16x32_bf16 v[44:47], a[112:115], v[172:175], v[44:47]\n"
    "ds_read_b64_tr_b16 a[108:109], v234 offset:8704\n"
    "v_cvt_pk_bf16_f32 v198, v148, v149\n"
    "v_mfma_f32_16x16x32_bf16 v[60:63], a[120:123], v[172:175], v[60:63]\n"
    "ds_read_b64_tr_b16 a[110:111], v234 offset:12800\n"
    "v_cvt_pk_bf16_f32 v199, v150, v151\n"
    "v_mfma_f32_16x16x32_bf16 v[60:63], a[124:127], v[188:191], v[60:63]\n"
    "v_exp_f32 v144, v144\n"
    "v_mfma_f32_16x16x32_bf16 v[56:59], a[124:127], v[184:187], v[56:59]\n"
    "v_exp_f32 v145, v145\n"
    "v_mfma_f32_16x16x32_bf16 v[56:59], a[120:123], v[168:171], v[56:59]\n"
    "v_exp_f32 v146, v146\n"
    "v_mfma_f32_16x16x32_bf16 v[52:55], a[120:123], v[164:167], v[52:55]\n"
    "v_exp_f32 v147, v147\n"
    "v_mfma_f32_16x16x32_bf16 v[52:55], a[124:127], v[180:183], v[52:55]\n"
    "v_cvt_pk_bf16_f32 v194, v144, v145\n"
    "v_cvt_pk_bf16_f32 v195, v146, v147\n"
    "v_mfma_f32_16x16x32_bf16 v[48:51], a[124:127], v[176:179], v[48:51]\n"
    "v_mfma_f32_16x16x32_bf16 v[48:51], a[120:123], v[160:163], v[48:51]\n"
    "s_waitcnt lgkmcnt(8)\n"
    "v_mfma_f32_16x16x32_bf16 v[128:131], a[64:67], a[0:3], v[112:115]\n"
    "ds_read_b128 a[80:83], v232 offset:12288\n"
    "v_mfma_f32_16x16x32_bf16 v[128:131], a[68:71], a[4:7], v[128:131]\n"
    "ds_read_b128 a[84:87], v232 offset:12544\n"
    "v_mfma_f32_16x16x32_bf16 v[128:131], a[72:75], a[8:11], v[128:131]\n"
    "ds_read_b128 a[88:91], v232 offset:12800\n"
    "v_mfma_f32_16x16x32_bf16 v[128:131], a[76:79], a[12:15], v[128:131]\n"
    "ds_read_b128 a[92:95], v232 offset:13056\n"
    "v_mfma_f32_16x16x32_bf16 v[132:135], a[76:79], a[28:31], v[116:119]\n"
    "ds_read_b64_tr_b16 a[112:113], v233 offset:768\n"
    "v_mfma_f32_16x16x32_bf16 v[132:135], a[72:75], a[24:27], v[132:135]\n"
    "ds_read_b64_tr_b16 a[114:115], v233 offset:4864\n"
    "v_mfma_f32_16x16x32_bf16 v[132:135], a[68:71], a[20:23], v[132:135]\n"
    "ds_read_b64_tr_b16 a[116:117], v233 offset:8960\n"
    "v_mfma_f32_16x16x32_bf16 v[132:135], a[64:67], a[16:19], v[132:135]\n"
    "v_exp_f32 v128, v128\n"
    "v_mfma_f32_16x16x32_bf16 v[136:139], a[64:67], a[32:35], v[120:123]\n"
    "v_exp_f32 v129, v129\n"
    "v_mfma_f32_16x16x32_bf16 v[136:139], a[68:71], a[36:39], v[136:139]\n"
    "v_exp_f32 v130, v130\n"
    "v_mfma_f32_16x16x32_bf16 v[136:139], a[72:75], a[40:43], v[136:139]\n"
    "v_exp_f32 v131, v131\n"
    "v_mfma_f32_16x16x32_bf16 v[136:139], a[76:79], a[44:47], v[136:139]\n"
    "ds_read_b64_tr_b16 a[118:119], v233 offset:13056\n"
    "v_cvt_pk_bf16_f32 v208, v128, v129\n"
    "v_mfma_f32_16x16x32_bf16 v[140:143], a[76:79], a[60:63], v[124:127]\n"
    "ds_read_b64_tr_b16 a[120:121], v234 offset:768\n"
    "v_cvt_pk_bf16_f32 v209, v130, v131\n"
    "v_mfma_f32_16x16x32_bf16 v[140:143], a[72:75], a[56:59], v[140:143]\n"
    "v_exp_f32 v132, v132\n"
    "v_mfma_f32_16x16x32_bf16 v[140:143], a[68:71], a[52:55], v[140:143]\n"
    "v_exp_f32 v133, v133\n"
    "v_mfma_f32_16x16x32_bf16 v[140:143], a[64:67], a[48:51], v[140:143]\n"
    "v_exp_f32 v134, v134\n"
    "s_waitcnt lgkmcnt(9)\n"
    "v_mfma_f32_16x16x32_bf16 v[64:67], a[96:99], v[160:163], v[64:67]\n"
    "v_exp_f32 v135, v135\n"
    "v_mfma_f32_16x16x32_bf16 v[64:67], a[100:103], v[176:179], v[64:67]\n"
    "ds_read_b64_tr_b16 a[122:123], v234 offset:4864\n"
    "v_cvt_pk_bf16_f32 v212, v132, v133\n"
    "v_mfma_f32_16x16x32_bf16 v[68:71], a[100:103], v[180:183], v[68:71]\n"
    "ds_read_b64_tr_b16 a[124:125], v234 offset:8960\n"
    "v_cvt_pk_bf16_f32 v213, v134, v135\n"
    "v_mfma_f32_16x16x32_bf16 v[68:71], a[96:99], v[164:167], v[68:71]\n"
    "v_exp_f32 v136, v136\n"
    "v_mfma_f32_16x16x32_bf16 v[72:75], a[96:99], v[168:171], v[72:75]\n"
    "v_exp_f32 v137, v137\n"
    "v_mfma_f32_16x16x32_bf16 v[72:75], a[100:103], v[184:187], v[72:75]\n"
    "ds_read_b64_tr_b16 a[126:127], v234 offset:13056\n"
    "v_mfma_f32_16x16x32_bf16 v[76:79], a[100:103], v[188:191], v[76:79]\n"
    "v_exp_f32 v138, v138\n"
    "v_mfma_f32_16x16x32_bf16 v[76:79], a[96:99], v[172:175], v[76:79]\n"
    "v_exp_f32 v139, v139\n"
    "v_mfma_f32_16x16x32_bf16 v[92:95], a[104:107], v[172:175], v[92:95]\n"
    "v_cvt_pk_bf16_f32 v216, v136, v137\n"
    "v_cvt_pk_bf16_f32 v217, v138, v139\n"
    "v_mfma_f32_16x16x32_bf16 v[92:95], a[108:111], v[188:191], v[92:95]\n"
    "v_exp_f32 v140, v140\n"
    "v_mfma_f32_16x16x32_bf16 v[88:91], a[108:111], v[184:187], v[88:91]\n"
    "v_exp_f32 v141, v141\n"
    "v_mfma_f32_16x16x32_bf16 v[88:91], a[104:107], v[168:171], v[88:91]\n"
    "v_exp_f32 v142, v142\n"
    "v_mfma_f32_16x16x32_bf16 v[84:87], a[104:107], v[164:167], v[84:87]\n"
    "v_exp_f32 v143, v143\n"
    "v_mfma_f32_16x16x32_bf16 v[84:87], a[108:111], v[180:183], v[84:87]\n"
    "v_cvt_pk_bf16_f32 v220, v140, v141\n"
    "v_cvt_pk_bf16_f32 v221, v142, v143\n"
    "v_mfma_f32_16x16x32_bf16 v[80:83], a[108:111], v[176:179], v[80:83]\n"
    "v_mfma_f32_16x16x32_bf16 v[80:83], a[104:107], v[160:163], v[80:83]\n"
    "v_add_u32 v233, s60, v236\n"
    "s_add_u32 s60, s60, 16384\n"
    "s_cmp_eq_u32 s60, 49152\n"
    "s_cselect_b32 s60, 0, s60\n"
    "v_xor_b32 v234, 32, v233\n"
    "v_add_u32 v232, s60, v235\n"
    "s_waitcnt vmcnt(12) lgkmcnt(0)\n"
    "s_barrier\n"
    "v_mfma_f32_16x16x32_bf16 v[156:159], a[80:83], a[48:51], v[124:127]\n"
    "ds_read_b128 a[64:67], v232 offset:0\n"
    "v_mfma_f32_16x16x32_bf16 v[156:159], a[84:87], a[52:55], v[156:159]\n"
    "ds_read_b128 a[68:71], v232 offset:256\n"
    "v_mfma_f32_16x16x32_bf16 v[156:159], a[88:91], a[56:59], v[156:159]\n"
    "ds_read_b128 a[72:75], v232 offset:512\n"
    "v_mfma_f32_16x16x32_bf16 v[156:159], a[92:95], a[60:63], v[156:159]\n"
    "ds_read_b128 a[76:79], v232 offset:768\n"
    "v_mfma_f32_16x16x32_bf16 v[152:155], a[92:95], a[44:47], v[120:123]\n"
    "ds_read_b64_tr_b16 a[96:97], v233 offset:0\n"
    "v_mfma_f32_16x16x32_bf16 v[152:155], a[88:91], a[40:43], v[152:155]\n"
    "ds_read_b64_tr_b16 a[98:99], v233 offset:4096\n"
    "v_mfma_f32_16x16x32_bf16 v[152:155], a[84:87], a[36:39], v[152:155]\n"
    "ds_read_b64_tr_b16 a[100:101], v233 offset:8192\n"
    "v_mfma_f32_16x16x32_bf16 v[152:155], a[80:83], a[32:35], v[152:155]\n"
    "v_exp_f32 v156, v156\n"
    "v_mfma_f32_16x16x32_bf16 v[148:151], a[80:83], a[16:19], v[116:119]\n"
    "v_exp_f32 v157, v157\n"
    "v_mfma_f32_16x16x32_bf16 v[148:151], a[84:87], a[20:23], v[148:151]\n"
    "v_exp_f32 v158, v158\n"
    "v_mfma_f32_16x16x32_bf16 v[148:151], a[88:91], a[24:27], v[148:151]\n"
    "v_exp_f32 v159, v159\n"
    "v_mfma_f32_16x16x32_bf16 v[148:151], a[92:95], a[28:31], v[148:151]\n"
    "ds_read_b64_tr_b16 a[102:103], v233 offset:12288\n"
    "v_cvt_pk_bf16_f32 v222, v156, v157\n"
    "v_mfma_f32_16x16x32_bf16 v[144:147], a[92:95], a[12:15], v[112:115]\n"
    "ds_read_b64_tr_b16 a[104:105], v234 offset:0\n"
    "v_cvt_pk_bf16_f32 v223, v158, v159\n"
    "v_mfma_f32_16x16x32_bf16 v[144:147], a[88:91], a[8:11], v[144:147]\n"
    "v_exp_f32 v152, v152\n"
    "v_mfma_f32_16x16x32_bf16 v[144:147], a[84:87], a[4:7], v[144:147]\n"
    "v_exp_f32 v153, v153\n"
    "v_mfma_f32_16x16x32_bf16 v[144:147], a[80:83], a[0:3], v[144:147]\n"
    "v_exp_f32 v154, v154\n"
    "v_mfma_f32_16x16x32_bf16 v[96:99], a[112:115], v[160:163], v[96:99]\n"
    "v_exp_f32 v155, v155\n"
    "v_mfma_f32_16x16x32_bf16 v[96:99], a[116:119], v[176:179], v[96:99]\n"
    "ds_read_b64_tr_b16 a[106:107], v234 offset:4096\n"
    "v_cvt_pk_bf16_f32 v218, v152, v153\n"
    "v_mfma_f32_16x16x32_bf16 v[100:103], a[116:119], v[180:183], v[100:103]\n"
    "ds_read_b64_tr_b16 a[108:109], v234 offset:8192\n"
    "v_cvt_pk_bf16_f32 v219, v154, v155\n"
    "v_mfma_f32_16x16x32_bf16 v[100:103], a[112:115], v[164:167], v[100:103]\n"
    "v_exp_f32 v148, v148\n"
    "v_mfma_f32_16x16x32_bf16 v[104:107], a[112:115], v[168:171], v[104:107]\n"
    "v_exp_f32 v149, v149\n"
    "v_mfma_f32_16x16x32_bf16 v[104:107], a[116:119], v[184:187], v[104:107]\n"
    "ds_read_b64_tr_b16 a[110:111], v234 offset:12288\n"
    "v_mfma_f32_16x16x32_bf16 v[108:111], a[116:119], v[188:191], v[108:111]\n"
    "v_exp_f32 v150, v150\n"
    "v_mfma_f32_16x16x32_bf16 v[108:111], a[112:115], v[172:175], v[108:111]\n"
    "v_exp_f32 v151, v151\n"
    "v_mfma_f32_16x16x32_bf16 a[180:183], a[120:123], v[172:175], a[180:183]\n"
    "v_cvt_pk_bf16_f32 v214, v148, v149\n"
    "v_cvt_pk_bf16_f32 v215, v150, v151\n"
    "v_mfma_f32_16x16x32_bf16 a[180:183], a[124:127], v[188:191], a[180:183]\n"
    "v_mfma_f32_16x16x32_bf16 a[176:179], a[124:127], v[184:187], a[176:179]\n"
    "v_mfma_f32_16x16x32_bf16 a[176:179], a[120:123], v[168:171], a[176:179]\n"
    "v_mfma_f32_16x16x32_bf16 a[172:175], a[120:123], v[164:167], a[172:175]\n"
    "v_mfma_f32_16x16x32_bf16 a[172:175], a[124:127], v[180:183], a[172:175]\n"
    "v_mfma_f32_16x16x32_bf16 a[168:171], a[124:127], v[176:179], a[168:171]\n"
    "v_mfma_f32_16x16x32_bf16 a[168:171], a[120:123], v[160:163], a[168:171]\n"
    "s_waitcnt lgkmcnt(0)\n"
    "v_mfma_f32_16x16x32_bf16 v[128:131], a[64:67], a[0:3], v[112:115]\n"
    "v_exp_f32 v144, v144\n"
    "v_mfma_f32_16x16x32_bf16 v[128:131], a[68:71], a[4:7], v[128:131]\n"
    "s_add_u32 s37, s64, 8192\n"
    "v_exp_f32 v145, v145\n"
    "v_mfma_f32_16x16x32_bf16 v[128:131], a[72:75], a[8:11], v[128:131]\n"
    "v_exp_f32 v146, v146\n"
    "v_mfma_f32_16x16x32_bf16 v[128:131], a[76:79], a[12:15], v[128:131]\n"
    "v_exp_f32 v147, v147\n"
    "v_mfma_f32_16x16x32_bf16 v[132:135], a[76:79], a[28:31], v[116:119]\n"
    "ds_read_b128 a[80:83], v232 offset:4096\n"
    "v_cvt_pk_bf16_f32 v210, v144, v145\n"
    "v_mfma_f32_16x16x32_bf16 v[132:135], a[72:75], a[24:27], v[132:135]\n"
    "ds_read_b128 a[84:87], v232 offset:4352\n"
    "v_cvt_pk_bf16_f32 v211, v146, v147\n"
    "v_mfma_f32_16x16x32_bf16 v[132:135], a[68:71], a[20:23], v[132:135]\n"
    "buffer_store_dwordx4 a[216:219], v245, s[52:55], s37 offen offset:0 sc1\n"
    "v_mfma_f32_16x16x32_bf16 v[132:135], a[64:67], a[16:19], v[132:135]\n"
    "buffer_store_dwordx4 a[220:223], v245, s[52:55], s37 offen offset:1024 sc1\n"
    "v_mfma_f32_16x16x32_bf16 v[136:139], a[64:67], a[32:35], v[120:123]\n"
    "v_exp_f32 v128, v128\n"
    "v_mfma_f32_16x16x32_bf16 v[136:139], a[68:71], a[36:39], v[136:139]\n"
    "v_exp_f32 v129, v129\n"
    "v_mfma_f32_16x16x32_bf16 v[136:139], a[72:75], a[40:43], v[136:139]\n"
    "v_exp_f32 v130, v130\n"
    "v_mfma_f32_16x16x32_bf16 v[136:139], a[76:79], a[44:47], v[136:139]\n"
    "v_exp_f32 v131, v131\n"
    "v_mfma_f32_16x16x32_bf16 v[140:143], a[76:79], a[60:63], v[124:127]\n"
    "ds_read_b128 a[88:91], v232 offset:4608\n"
    "v_cvt_pk_bf16_f32 v160, v128, v129\n"
    "v_mfma_f32_16x16x32_bf16 v[140:143], a[72:75], a[56:59], v[140:143]\n"
    "ds_read_b128 a[92:95], v232 offset:4864\n"
    "v_cvt_pk_bf16_f32 v161, v130, v131\n"
    "v_mfma_f32_16x16x32_bf16 v[140:143], a[68:71], a[52:55], v[140:143]\n"
    "buffer_store_dwordx4 a[224:227], v245, s[52:55], s37 offen offset:2048 sc1\n"
    "v_mfma_f32_16x16x32_bf16 v[140:143], a[64:67], a[48:51], v[140:143]\n"
    "v_exp_f32 v132, v132\n"
    "v_mfma_f32_16x16x32_bf16 a[128:131], a[144:147], v[192:195], a[128:131]\n"
    "v_exp_f32 v133, v133\n"
    "v_mfma_f32_16x16x32_bf16 a[128:131], a[144:147], v[208:211], a[128:131]\n"
    "v_exp_f32 v134, v134\n"
    "v_mfma_f32_16x16x32_bf16 a[132:135], a[144:147], v[196:199], a[132:135]\n"
    "v_exp_f32 v135, v135\n"
    "v_mfma_f32_16x16x32_bf16 a[132:135], a[144:147], v[212:215], a[132:135]\n"
    "ds_read_b64_tr_b16 a[112:113], v233 offset:256\n"
    "v_cvt_pk_bf16_f32 v164, v132, v133\n"
    "v_mfma_f32_16x16x32_bf16 a[136:139], a[144:147], v[200:203], a[136:139]\n"
    "ds_read_b64_tr_b16 a[114:115], v233 offset:4352\n"
    "v_cvt_pk_bf16_f32 v165, v134, v135\n"
    "v_mfma_f32_16x16x32_bf16 a[136:139], a[144:147], v[216:219], a[136:139]\n"
    "buffer_store_dwordx4 a[228:231], v245, s[52:55], s37 offen offset:3072 sc1\n"
    "v_mfma_f32_16x16x32_bf16 a[140:143], a[144:147], v[204:207], a[140:143]\n"
    "v_exp_f32 v136, v136\n"
    "v_mfma_f32_16x16x32_bf16 a[140:143], a[144:147], v[220:223], a[140:143]\n"
    "v_exp_f32 v137, v137\n"
    "v_mfma_f32_16x16x32_bf16 v[0:3], a[96:99], v[192:195], v[0:3]\n"
    "v_exp_f32 v138, v138\n"
    "v_mfma_f32_16x16x32_bf16 v[0:3], a[100:103], v[208:211], v[0:3]\n"
    "s_add_u32 m0, s71, s59\n"
    "s_nop 0\n"
    "buffer_load_dwordx4 v237, s[44:47], s57 offen offset:0 lds\n"
    "v_mfma_f32_16x16x32_bf16 v[4:7], a[100:103], v[212:215], v[4:7]\n"
    "v_exp_f32 v139, v139\n"
    "v_mfma_f32_16x16x32_bf16 v[4:7], a[96:99], v[196:199], v[4:7]\n"
    "ds_read_b64_tr_b16 a[116:117], v233 offset:8448\n"
    "v_cvt_pk_bf16_f32 v168, v136, v137\n"
    "v_mfma_f32_16x16x32_bf16 v[8:11], a[96:99], v[200:203], v[8:11]\n"
    "ds_read_b64_tr_b16 a[118:119], v233 offset:12544\n"
    "v_cvt_pk_bf16_f32 v169, v138, v139\n"
    "v_mfma_f32_16x16x32_bf16 v[8:11], a[100:103], v[216:219], v[8:11]\n"
    "v_exp_f32 v140, v140\n"
    "v_mfma_f32_16x16x32_bf16 v[12:15], a[100:103], v[220:223], v[12:15]\n"
    "v_exp_f32 v141, v141\n"
    "v_mfma_f32_16x16x32_bf16 v[12:15], a[96:99], v[204:207], v[12:15]\n"
    "buffer_load_dwordx4 v238, s[44:47], s57 offen offset:1024 lds\n"
    "v_mfma_f32_16x16x32_bf16 v[28:31], a[104:107], v[204:207], v[28:31]\n"
    "v_exp_f32 v142, v142\n"
    "v_mfma_f32_16x16x32_bf16 v[28:31], a[108:111], v[220:223], v[28:31]\n"
    "v_exp_f32 v143, v143\n"
    "v_mfma_f32_16x16x32_bf16 v[24:27], a[108:111], v[216:219], v[24:27]\n"
    "ds_read_b64_tr_b16 a[120:121], v234 offset:256\n"
    "v_cvt_pk_bf16_f32 v172, v140, v141\n"
    "v_mfma_f32_16x16x32_bf16 v[24:27], a[104:107], v[200:203], v[24:27]\n"
    "ds_read_b64_tr_b16 a[122:123], v234 offset:4352\n"
    "v_cvt_pk_bf16_f32 v173, v142, v143\n"
    "v_mfma_f32_16x16x32_bf16 v[20:23], a[104:107], v[196:199], v[20:23]\n"
    "ds_read_b64_tr_b16 a[124:125], v234 offset:8448\n"
    "v_mfma_f32_16x16x32_bf16 v[20:23], a[108:111], v[212:215], v[20:23]\n"
    "ds_read_b64_tr_b16 a[126:127], v234 offset:12544\n"
    "v_mfma_f32_16x16x32_bf16 v[16:19], a[108:111], v[208:211], v[16:19]\n"
    "buffer_load_dwordx4 v239, s[44:47], s57 offen offset:2048 lds\n"
    "v_mfma_f32_16x16x32_bf16 v[16:19], a[104:107], v[192:195], v[16:19]\n"
    "s_waitcnt lgkmcnt(8)\n"
    "v_mfma_f32_16x16x32_bf16 v[156:159], a[80:83], a[48:51], v[124:127]\n"
    "ds_read_b128 a[64:67], v232 offset:8192\n"
    "v_mfma_f32_16x16x32_bf16 v[156:159], a[84:87], a[52:55], v[156:159]\n"
    "ds_read_b128 a[68:71], v232 offset:8448\n"
    "v_mfma_f32_16x16x32_bf16 v[156:159], a[88:91], a[56:59], v[156:159]\n"
    "ds_read_b128 a[72:75], v232 offset:8704\n"
    "v_mfma_f32_16x16x32_bf16 v[156:159], a[92:95], a[60:63], v[156:159]\n"
    "buffer_load_dwordx4 v240, s[44:47], s57 offen offset:3072 lds\n"
    "v_mfma_f32_16x16x32_bf16 v[152:155], a[92:95], a[44:47], v[120:123]\n"
    "ds_read_b128 a[76:79], v232 offset:8960\n"
    "v_mfma_f32_16x16x32_bf16 v[152:155], a[88:91], a[40:43], v[152:155]\n"
    "ds_read_b64_tr_b16 a[96:97], v233 offset:512\n"
    "v_mfma_f32_16x16x32_bf16 v[152:155], a[84:87], a[36:39], v[152:155]\n"
    "ds_read_b64_tr_b16 a[98:99], v233 offset:4608\n"
    "v_mfma_f32_16x16x32_bf16 v[152:155], a[80:83], a[32:35], v[152:155]\n"
    "v_exp_f32 v156, v156\n"
    "v_mfma_f32_16x16x32_bf16 v[148:151], a[80:83], a[16:19], v[116:119]\n"
    "v_exp_f32 v157, v157\n"
    "v_mfma_f32_16x16x32_bf16 v[148:151], a[84:87], a[20:23], v[148:151]\n"
    "s_add_u32 s57, s57, 33439744\n"
    "s_add_u32 s59, s59, 16384\n"
    "s_cmp_eq_u32 s59, 49152\n"
    "s_cselect_b32 s59, 0, s59\n"
    "v_exp_f32 v158, v158\n"
    "v_mfma_f32_16x16x32_bf16 v[148:151], a[88:91], a[24:27], v[148:151]\n"
    "v_exp_f32 v159, v159\n"
    "v_mfma_f32_16x16x32_bf16 v[148:151], a[92:95], a[28:31], v[148:151]\n"
    "ds_read_b64_tr_b16 a[100:101], v233 offset:8704\n"
    "v_cvt_pk_bf16_f32 v174, v156, v157\n"
    "v_mfma_f32_16x16x32_bf16 v[144:147], a[92:95], a[12:15], v[112:115]\n"
    "ds_read_b64_tr_b16 a[102:103], v233 offset:12800\n"
    "v_cvt_pk_bf16_f32 v175, v158, v159\n"
    "v_mfma_f32_16x16x32_bf16 v[144:147], a[88:91], a[8:11], v[144:147]\n"
    "v_exp_f32 v152, v152\n"
    "v_mfma_f32_16x16x32_bf16 v[144:147], a[84:87], a[4:7], v[144:147]\n"
    "v_exp_f32 v153, v153\n"
    "v_mfma_f32_16x16x32_bf16 v[144:147], a[80:83], a[0:3], v[144:147]\n"
    "v_exp_f32 v154, v154\n"
    "s_waitcnt lgkmcnt(8)\n"
    "v_mfma_f32_16x16x32_bf16 v[32:35], a[112:115], v[192:195], v[32:35]\n"
    "v_exp_f32 v155, v155\n"
    "v_mfma_f32_16x16x32_bf16 v[32:35], a[116:119], v[208:211], v[32:35]\n"
    "ds_read_b64_tr_b16 a[104:105], v234 offset:512\n"
    "v_cvt_pk_bf16_f32 v170, v152, v153\n"
    "v_mfma_f32_16x16x32_bf16 v[36:39], a[116:119], v[212:215], v[36:39]\n"
    "ds_read_b64_tr_b16 a[106:107], v234 offset:4608\n"
    "v_cvt_pk_bf16_f32 v171, v154, v155\n"
    "v_mfma_f32_16x16x32_bf16 v[36:39], a[112:115], v[196:199], v[36:39]\n"
    "v_exp_f32 v148, v148\n"
    "v_mfma_f32_16x16x32_bf16 v[40:43], a[112:115], v[200:203], v[40:43]\n"
    "v_exp_f32 v149, v149\n"
    "v_mfma_f32_16x16x32_bf16 v[40:43], a[116:119], v[216:219], v[40:43]\n"
    "v_exp_f32 v150, v150\n"
    "v_mfma_f32_16x16x32_bf16 v[44:47], a[116:119], v[220:223], v[44:47]\n"
    "v_exp_f32 v151, v151\n"
    "v_mfma_f32_16x16x32_bf16 v[44:47], a[112:115], v[204:207], v[44:47]\n"
    "ds_read_b64_tr_b16 a[108:109], v234 offset:8704\n"
    "v_cvt_pk_bf16_f32 v166, v148, v149\n"
    "v_mfma_f32_16x16x32_bf16 v[60:63], a[120:123], v[204:207], v[60:63]\n"
    "ds_read_b64_tr_b16 a[110:111], v234 offset:12800\n"
    "v_cvt_pk_bf16_f32 v167, v150, v151\n"
    "v_mfma_f32_16x16x32_bf16 v[60:63], a[124:127], v[220:223], v[60:63]\n"
    "v_exp_f32 v144, v144\n"
    "v_mfma_f32_16x16x32_bf16 v[56:59], a[124:127], v[216:219], v[56:59]\n"
    "v_exp_f32 v145, v145\n"
    "v_mfma_f32_16x16x32_bf16 v[56:59], a[120:123], v[200:203], v[56:59]\n"
    "v_exp_f32 v146, v146\n"
    "v_mfma_f32_16x16x32_bf16 v[52:55], a[120:123], v[196:199], v[52:55]\n"
    "v_exp_f32 v147, v147\n"
    "v_mfma_f32_16x16x32_bf16 v[52:55], a[124:127], v[212:215], v[52:55]\n"
    "v_cvt_pk_bf16_f32 v162, v144, v145\n"
    "v_cvt_pk_bf16_f32 v163, v146, v147\n"
    "v_mfma_f32_16x16x32_bf16 v[48:51], a[124:127], v[208:211], v[48:51]\n"
    "v_mfma_f32_16x16x32_bf16 v[48:51], a[120:123], v[192:195], v[48:51]\n"
    "s_waitcnt lgkmcnt(8)\n"
    "v_mfma_f32_16x16x32_bf16 v[128:131], a[64:67], a[0:3], v[112:115]\n"
    "ds_read_b128 a[80:83], v232 offset:12288\n"
    "v_mfma_f32_16x16x32_bf16 v[128:131], a[68:71], a[4:7], v[128:131]\n"
    "ds_read_b128 a[84:87], v232 offset:12544\n"
    "v_mfma_f32_16x16x32_bf16 v[128:131], a[72:75], a[8:11], v[128:131]\n"
    "ds_read_b128 a[88:91], v232 offset:12800\n"
    "v_mfma_f32_16x16x32_bf16 v[128:131], a[76:79], a[12:15], v[128:131]\n"
    "ds_read_b128 a[92:95], v232 offset:13056\n"
    "v_mfma_f32_16x16x32_bf16 v[132:135], a[76:79], a[28:31], v[116:119]\n"
    "ds_read_b64_tr_b16 a[112:113], v233 offset:768\n"
    "v_mfma_f32_16x16x32_bf16 v[132:135], a[72:75], a[24:27], v[132:135]\n"
    "ds_read_b64_tr_b16 a[114:115], v233 offset:4864\n"
    "v_mfma_f32_16x16x32_bf16 v[132:135], a[68:71], a[20:23], v[132:135]\n"
    "ds_read_b64_tr_b16 a[116:117], v233 offset:8960\n"
    "v_mfma_f32_16x16x32_bf16 v[132:135], a[64:67], a[16:19], v[132:135]\n"
    "v_exp_f32 v128, v128\n"
    "v_mfma_f32_16x16x32_bf16 v[136:139], a[64:67], a[32:35], v[120:123]\n"
    "v_exp_f32 v129, v129\n"
    "v_mfma_f32_16x16x32_bf16 v[136:139], a[68:71], a[36:39], v[136:139]\n"
    "v_exp_f32 v130, v130\n"
    "v_mfma_f32_16x16x32_bf16 v[136:139], a[72:75], a[40:43], v[136:139]\n"
    "v_exp_f32 v131, v131\n"
    "v_mfma_f32_16x16x32_bf16 v[136:139], a[76:79], a[44:47], v[136:139]\n"
    "ds_read_b64_tr_b16 a[118:119], v233 offset:13056\n"
    "v_cvt_pk_bf16_f32 v176, v128, v129\n"
    "v_mfma_f32_16x16x32_bf16 v[140:143], a[76:79], a[60:63], v[124:127]\n"
    "ds_read_b64_tr_b16 a[120:121], v234 offset:768\n"
    "v_cvt_pk_bf16_f32 v177, v130, v131\n"
    "v_mfma_f32_16x16x32_bf16 v[140:143], a[72:75], a[56:59], v[140:143]\n"
    "v_exp_f32 v132, v132\n"
    "v_mfma_f32_16x16x32_bf16 v[140:143], a[68:71], a[52:55], v[140:143]\n"
    "v_exp_f32 v133, v133\n"
    "v_mfma_f32_16x16x32_bf16 v[140:143], a[64:67], a[48:51], v[140:143]\n"
    "v_exp_f32 v134, v134\n"
    "s_waitcnt lgkmcnt(9)\n"
    "v_mfma_f32_16x16x32_bf16 v[64:67], a[96:99], v[192:195], v[64:67]\n"
    "v_exp_f32 v135, v135\n"
    "v_mfma_f32_16x16x32_bf16 v[64:67], a[100:103], v[208:211], v[64:67]\n"
    "ds_read_b64_tr_b16 a[122:123], v234 offset:4864\n"
    "v_cvt_pk_bf16_f32 v180, v132, v133\n"
    "v_mfma_f32_16x16x32_bf16 v[68:71], a[100:103], v[212:215], v[68:71]\n"
    "ds_read_b64_tr_b16 a[124:125], v234 offset:8960\n"
    "v_cvt_pk_bf16_f32 v181, v134, v135\n"
    "v_mfma_f32_16x16x32_bf16 v[68:71], a[96:99], v[196:199], v[68:71]\n"
    "v_exp_f32 v136, v136\n"
    "v_mfma_f32_16x16x32_bf16 v[72:75], a[96:99], v[200:203], v[72:75]\n"
    "v_exp_f32 v137, v137\n"
    "v_mfma_f32_16x16x32_bf16 v[72:75], a[100:103], v[216:219], v[72:75]\n"
    "ds_read_b64_tr_b16 a[126:127], v234 offset:13056\n"
    "v_mfma_f32_16x16x32_bf16 v[76:79], a[100:103], v[220:223], v[76:79]\n"
    "v_exp_f32 v138, v138\n"
    "v_mfma_f32_16x16x32_bf16 v[76:79], a[96:99], v[204:207], v[76:79]\n"
    "v_exp_f32 v139, v139\n"
    "v_mfma_f32_16x16x32_bf16 v[92:95], a[104:107], v[204:207], v[92:95]\n"
    "v_cvt_pk_bf16_f32 v184, v136, v137\n"
    "v_cvt_pk_bf16_f32 v185, v138, v139\n"
    "v_mfma_f32_16x16x32_bf16 v[92:95], a[108:111], v[220:223], v[92:95]\n"
    "v_exp_f32 v140, v140\n"
    "v_mfma_f32_16x16x32_bf16 v[88:91], a[108:111], v[216:219], v[88:91]\n"
    "v_exp_f32 v141, v141\n"
    "v_mfma_f32_16x16x32_bf16 v[88:91], a[104:107], v[200:203], v[88:91]\n"
    "v_exp_f32 v142, v142\n"
    "v_mfma_f32_16x16x32_bf16 v[84:87], a[104:107], v[196:199], v[84:87]\n"
    "v_exp_f32 v143, v143\n"
    "v_mfma_f32_16x16x32_bf16 v[84:87], a[108:111], v[212:215], v[84:87]\n"
    "v_cvt_pk_bf16_f32 v188, v140, v141\n"
    "v_cvt_pk_bf16_f32 v189, v142, v143\n"
    "v_mfma_f32_16x16x32_bf16 v[80:83], a[108:111], v[208:211], v[80:83]\n"
    "v_mfma_f32_16x16x32_bf16 v[80:83], a[104:107], v[192:195], v[80:83]\n"
    "v_add_u32 v233, s60, v236\n"
    "s_add_u32 s60, s60, 16384\n"
    "s_cmp_eq_u32 s60, 49152\n"
    "s_cselect_b32 s60, 0, s60\n"
    "v_xor_b32 v234, 32, v233\n"
    "v_add_u32 v232, s60, v235\n"
    "s_waitcnt vmcnt(8) lgkmcnt(0)\n"
    "s_barrier\n"
    "v_mfma_f32_16x16x32_bf16 v[156:159], a[80:83], a[48:51], v[124:127]\n"
    "ds_read_b128 a[64:67], v232 offset:0\n"
    "v_mfma_f32_16x16x32_bf16 v[156:159], a[84:87], a[52:55], v[156:159]\n"
    "ds_read_b128 a[68:71], v232 offset:256\n"
    "v_mfma_f32_16x16x32_bf16 v[156:159], a[88:91], a[56:59], v[156:159]\n"
    "ds_read_b128 a[72:75], v232 offset:512\n"
    "v_mfma_f32_16x16x32_bf16 v[156:159], a[92:95], a[60:63], v[156:159]\n"
    "ds_read_b128 a[76:79], v232 offset:768\n"
    "v_mfma_f32_16x16x32_bf16 v[152:155], a[92:95], a[44:47], v[120:123]\n"
    "ds_read_b64_tr_b16 a[96:97], v233 offset:0\n"
    "v_mfma_f32_16x16x32_bf16 v[152:155], a[88:91], a[40:43], v[152:155]\n"
    "ds_read_b64_tr_b16 a[98:99], v233 offset:4096\n"
    "v_mfma_f32_16x16x32_bf16 v[152:155], a[84:87], a[36:39], v[152:155]\n"
    "ds_read_b64_tr_b16 a[100:101], v233 offset:8192\n"
    "v_mfma_f32_16x16x32_bf16 v[152:155], a[80:83], a[32:35], v[152:155]\n"
    "v_exp_f32 v156, v156\n"
    "v_mfma_f32_16x16x32_bf16 v[148:151], a[80:83], a[16:19], v[116:119]\n"
    "v_exp_f32 v157, v157\n"
    "v_mfma_f32_16x16x32_bf16 v[148:151], a[84:87], a[20:23], v[148:151]\n"
    "v_exp_f32 v158, v158\n"
    "v_mfma_f32_16x16x32_bf16 v[148:151], a[88:91], a[24:27], v[148:151]\n"
    "v_exp_f32 v159, v159\n"
    "v_mfma_f32_16x16x32_bf16 v[148:151], a[92:95], a[28:31], v[148:151]\n"
    "ds_read_b64_tr_b16 a[102:103], v233 offset:12288\n"
    "v_cvt_pk_bf16_f32 v190, v156, v157\n"
    "v_mfma_f32_16x16x32_bf16 v[144:147], a[92:95], a[12:15], v[112:115]\n"
    "ds_read_b64_tr_b16 a[104:105], v234 offset:0\n"
    "v_cvt_pk_bf16_f32 v191, v158, v159\n"
    "v_mfma_f32_16x16x32_bf16 v[144:147], a[88:91], a[8:11], v[144:147]\n"
    "v_exp_f32 v152, v152\n"
    "v_mfma_f32_16x16x32_bf16 v[144:147], a[84:87], a[4:7], v[144:147]\n"
    "v_exp_f32 v153, v153\n"
    "v_mfma_f32_16x16x32_bf16 v[144:147], a[80:83], a[0:3], v[144:147]\n"
    "v_exp_f32 v154, v154\n"
    "v_mfma_f32_16x16x32_bf16 v[96:99], a[112:115], v[192:195], v[96:99]\n"
    "v_exp_f32 v155, v155\n"
    "v_mfma_f32_16x16x32_bf16 v[96:99], a[116:119], v[208:211], v[96:99]\n"
    "ds_read_b64_tr_b16 a[106:107], v234 offset:4096\n"
    "v_cvt_pk_bf16_f32 v186, v152, v153\n"
    "v_mfma_f32_16x16x32_bf16 v[100:103], a[116:119], v[212:215], v[100:103]\n"
    "ds_read_b64_tr_b16 a[108:109], v234 offset:8192\n"
    "v_cvt_pk_bf16_f32 v187, v154, v155\n"
    "v_mfma_f32_16x16x32_bf16 v[100:103], a[112:115], v[196:199], v[100:103]\n"
    "v_exp_f32 v148, v148\n"
    "v_mfma_f32_16x16x32_bf16 v[104:107], a[112:115], v[200:203], v[104:107]\n"
    "v_exp_f32 v149, v149\n"
    "v_mfma_f32_16x16x32_bf16 v[104:107], a[116:119], v[216:219], v[104:107]\n"
    "ds_read_b64_tr_b16 a[110:111], v234 offset:12288\n"
    "v_mfma_f32_16x16x32_bf16 v[108:111], a[116:119], v[220:223], v[108:111]\n"
    "v_exp_f32 v150, v150\n"
    "v_mfma_f32_16x16x32_bf16 v[108:111], a[112:115], v[204:207], v[108:111]\n"
    "v_exp_f32 v151, v151\n"
    "v_mfma_f32_16x16x32_bf16 a[180:183], a[120:123], v[204:207], a[180:183]\n"
    "v_cvt_pk_bf16_f32 v182, v148, v149\n"
    "v_cvt_pk_bf16_f32 v183, v150, v151\n"
    "v_mfma_f32_16x16x32_bf16 a[180:183], a[124:127], v[220:223], a[180:183]\n"
    "v_mfma_f32_16x16x32_bf16 a[176:179], a[124:127], v[216:219], a[176:179]\n"
    "v_mfma_f32_16x16x32_bf16 a[176:179], a[120:123], v[200:203], a[176:179]\n"
    "v_mfma_f32_16x16x32_bf16 a[172:175], a[120:123], v[196:199], a[172:175]\n"
    "v_mfma_f32_16x16x32_bf16 a[172:175], a[124:127], v[212:215], a[172:175]\n"
    "v_mfma_f32_16x16x32_bf16 a[168:171], a[124:127], v[208:211], a[168:171]\n"
    "v_mfma_f32_16x16x32_bf16 a[168:171], a[120:123], v[192:195], a[168:171]\n"
    "s_waitcnt lgkmcnt(0)\n"
    "v_mfma_f32_16x16x32_bf16 v[128:131], a[64:67], a[0:3], v[112:115]\n"
    "v_exp_f32 v144, v144\n"
    "v_mfma_f32_16x16x32_bf16 v[128:131], a[68:71], a[4:7], v[128:131]\n"
    "s_add_u32 s37, s64, 12288\n"
    "v_exp_f32 v145, v145\n"
    "v_mfma_f32_16x16x32_bf16 v[128:131], a[72:75], a[8:11], v[128:131]\n"
    "v_exp_f32 v146, v146\n"
    "v_mfma_f32_16x16x32_bf16 v[128:131], a[76:79], a[12:15], v[128:131]\n"
    "v_exp_f32 v147, v147\n"
    "v_mfma_f32_16x16x32_bf16 v[132:135], a[76:79], a[28:31], v[116:119]\n"
    "ds_read_b128 a[80:83], v232 offset:4096\n"
    "v_cvt_pk_bf16_f32 v178, v144, v145\n"
    "v_mfma_f32_16x16x32_bf16 v[132:135], a[72:75], a[24:27], v[132:135]\n"
    "ds_read_b128 a[84:87], v232 offset:4352\n"
    "v_cvt_pk_bf16_f32 v179, v146, v147\n"
    "v_mfma_f32_16x16x32_bf16 v[132:135], a[68:71], a[20:23], v[132:135]\n"
    "buffer_store_dwordx4 a[184:187], v245, s[52:55], s37 offen offset:0 sc1\n"
    "v_mfma_f32_16x16x32_bf16 v[132:135], a[64:67], a[16:19], v[132:135]\n"
    "buffer_store_dwordx4 a[188:191], v245, s[52:55], s37 offen offset:1024 sc1\n"
    "v_mfma_f32_16x16x32_bf16 v[136:139], a[64:67], a[32:35], v[120:123]\n"
    "v_exp_f32 v128, v128\n"
    "v_mfma_f32_16x16x32_bf16 v[136:139], a[68:71], a[36:39], v[136:139]\n"
    "v_exp_f32 v129, v129\n"
    "v_mfma_f32_16x16x32_bf16 v[136:139], a[72:75], a[40:43], v[136:139]\n"
    "v_exp_f32 v130, v130\n"
    "v_mfma_f32_16x16x32_bf16 v[136:139], a[76:79], a[44:47], v[136:139]\n"
    "v_exp_f32 v131, v131\n"
    "v_mfma_f32_16x16x32_bf16 v[140:143], a[76:79], a[60:63], v[124:127]\n"
    "ds_read_b128 a[88:91], v232 offset:4608\n"
    "v_cvt_pk_bf16_f32 v192, v128, v129\n"
    "v_mfma_f32_16x16x32_bf16 v[140:143], a[72:75], a[56:59], v[140:143]\n"
    "ds_read_b128 a[92:95], v232 offset:4864\n"
    "v_cvt_pk_bf16_f32 v193, v130, v131\n"
    "v_mfma_f32_16x16x32_bf16 v[140:143], a[68:71], a[52:55], v[140:143]\n"
    "buffer_store_dwordx4 a[192:195], v245, s[52:55], s37 offen offset:2048 sc1\n"
    "v_mfma_f32_16x16x32_bf16 v[140:143], a[64:67], a[48:51], v[140:143]\n"
    "v_exp_f32 v132, v132\n"
    "v_mfma_f32_16x16x32_bf16 a[128:131], a[144:147], v[160:163], a[128:131]\n"
    "v_exp_f32 v133, v133\n"
    "v_mfma_f32_16x16x32_bf16 a[128:131], a[144:147], v[176:179], a[128:131]\n"
    "v_exp_f32 v134, v134\n"
    "v_mfma_f32_16x16x32_bf16 a[132:135], a[144:147], v[164:167], a[132:135]\n"
    "v_exp_f32 v135, v135\n"
    "v_mfma_f32_16x16x32_bf16 a[132:135], a[144:147], v[180:183], a[132:135]\n"
    "ds_read_b64_tr_b16 a[112:113], v233 offset:256\n"
    "v_cvt_pk_bf16_f32 v196, v132, v133\n"
    "v_mfma_f32_16x16x32_bf16 a[136:139], a[144:147], v[168:171], a[136:139]\n"
    "ds_read_b64_tr_b16 a[114:115], v233 offset:4352\n"
    "v_cvt_pk_bf16_f32 v197, v134, v135\n"
    "v_mfma_f32_16x16x32_bf16 a[136:139], a[144:147], v[184:187], a[136:139]\n"
    "buffer_store_dwordx4 a[196:199], v245, s[52:55], s37 offen offset:3072 sc1\n"
    "v_mfma_f32_16x16x32_bf16 a[140:143], a[144:147], v[172:175], a[140:143]\n"
    "v_exp_f32 v136, v136\n"
    "v_mfma_f32_16x16x32_bf16 a[140:143], a[144:147], v[188:191], a[140:143]\n"
    "v_exp_f32 v137, v137\n"
    "v_mfma_f32_16x16x32_bf16 v[0:3], a[96:99], v[160:163], v[0:3]\n"
    "v_exp_f32 v138, v138\n"
    "v_mfma_f32_16x16x32_bf16 v[0:3], a[100:103], v[176:179], v[0:3]\n"
    "v_exp_f32 v139, v139\n"
    "v_mfma_f32_16x16x32_bf16 v[4:7], a[100:103], v[180:183], v[4:7]\n"
    "ds_read_b64_tr_b16 a[116:117], v233 offset:8448\n"
    "v_cvt_pk_bf16_f32 v200, v136, v137\n"
    "v_mfma_f32_16x16x32_bf16 v[4:7], a[96:99], v[164:167], v[4:7]\n"
    "ds_read_b64_tr_b16 a[118:119], v233 offset:12544\n"
    "v_cvt_pk_bf16_f32 v201, v138, v139\n"
    "v_mfma_f32_16x16x32_bf16 v[8:11], a[96:99], v[168:171], v[8:11]\n"
    "v_exp_f32 v140, v140\n"
    "v_mfma_f32_16x16x32_bf16 v[8:11], a[100:103], v[184:187], v[8:11]\n"
    "v_exp_f32 v141, v141\n"
    "v_mfma_f32_16x16x32_bf16 v[12:15], a[100:103], v[188:191], v[12:15]\n"
    "v_exp_f32 v142, v142\n"
    "v_mfma_f32_16x16x32_bf16 v[12:15], a[96:99], v[172:175], v[12:15]\n"
    "v_exp_f32 v143, v143\n"
    "v_mfma_f32_16x16x32_bf16 v[28:31], a[104:107], v[172:175], v[28:31]\n"
    "ds_read_b64_tr_b16 a[120:121], v234 offset:256\n"
    "v_cvt_pk_bf16_f32 v204, v140, v141\n"
    "v_mfma_f32_16x16x32_bf16 v[28:31], a[108:111], v[188:191], v[28:31]\n"
    "ds_read_b64_tr_b16 a[122:123], v234 offset:4352\n"
    "v_cvt_pk_bf16_f32 v205, v142, v143\n"
    "v_mfma_f32_16x16x32_bf16 v[24:27], a[108:111], v[184:187], v[24:27]\n"
    "ds_read_b64_tr_b16 a[124:125], v234 offset:8448\n"
    "v_mfma_f32_16x16x32_bf16 v[24:27], a[104:107], v[168:171], v[24:27]\n"
    "ds_read_b64_tr_b16 a[126:127], v234 offset:12544\n"
    "v_mfma_f32_16x16x32_bf16 v[20:23], a[104:107], v[164:167], v[20:23]\n"
    "v_mfma_f32_16x16x32_bf16 v[20:23], a[108:111], v[180:183], v[20:23]\n"
    "v_mfma_f32_16x16x32_bf16 v[16:19], a[108:111], v[176:179], v[16:19]\n"
    "v_mfma_f32_16x16x32_bf16 v[16:19], a[104:107], v[160:163], v[16:19]\n"
    "s_waitcnt lgkmcnt(8)\n"
    "v_mfma_f32_16x16x32_bf16 v[156:159], a[80:83], a[48:51], v[124:127]\n"
    "ds_read_b128 a[64:67], v232 offset:8192\n"
    "v_mfma_f32_16x16x32_bf16 v[156:159], a[84:87], a[52:55], v[156:159]\n"
    "ds_read_b128 a[68:71], v232 offset:8448\n"
    "v_mfma_f32_16x16x32_bf16 v[156:159], a[88:91], a[56:59], v[156:159]\n"
    "ds_read_b128 a[72:75], v232 offset:8704\n"
    "v_mfma_f32_16x16x32_bf16 v[156:159], a[92:95], a[60:63], v[156:159]\n"
    "ds_read_b128 a[76:79], v232 offset:8960\n"
    "v_mfma_f32_16x16x32_bf16 v[152:155], a[92:95], a[44:47], v[120:123]\n"
    "ds_read_b64_tr_b16 a[96:97], v233 offset:512\n"
    "v_mfma_f32_16x16x32_bf16 v[152:155], a[88:91], a[40:43], v[152:155]\n"
    "ds_read_b64_tr_b16 a[98:99], v233 offset:4608\n"
    "v_mfma_f32_16x16x32_bf16 v[152:155], a[84:87], a[36:39], v[152:155]\n"
    "ds_read_b64_tr_b16 a[100:101], v233 offset:8704\n"
    "v_mfma_f32_16x16x32_bf16 v[152:155], a[80:83], a[32:35], v[152:155]\n"
    "v_exp_f32 v156, v156\n"
    "v_mfma_f32_16x16x32_bf16 v[148:151], a[80:83], a[16:19], v[116:119]\n"
    "v_exp_f32 v157, v157\n"
    "v_mfma_f32_16x16x32_bf16 v[148:151], a[84:87], a[20:23], v[148:151]\n"
    "v_exp_f32 v158, v158\n"
    "v_mfma_f32_16x16x32_bf16 v[148:151], a[88:91], a[24:27], v[148:151]\n"
    "v_exp_f32 v159, v159\n"
    "v_mfma_f32_16x16x32_bf16 v[148:151], a[92:95], a[28:31], v[148:151]\n"
    "ds_read_b64_tr_b16 a[102:103], v233 offset:12800\n"
    "v_cvt_pk_bf16_f32 v206, v156, v157\n"
    "v_mfma_f32_16x16x32_bf16 v[144:147], a[92:95], a[12:15], v[112:115]\n"
    "ds_read_b64_tr_b16 a[104:105], v234 offset:512\n"
    "v_cvt_pk_bf16_f32 v207, v158, v159\n"
    "v_mfma_f32_16x16x32_bf16 v[144:147], a[88:91], a[8:11], v[144:147]\n"
    "v_exp_f32 v152, v152\n"
    "v_mfma_f32_16x16x32_bf16 v[144:147], a[84:87], a[4:7], v[144:147]\n"
    "v_exp_f32 v153, v153\n"
    "v_mfma_f32_16x16x32_bf16 v[144:147], a[80:83], a[0:3], v[144:147]\n"
    "v_exp_f32 v154, v154\n"
    "s_waitcnt lgkmcnt(9)\n"
    "v_mfma_f32_16x16x32_bf16 v[32:35], a[112:115], v[160:163], v[32:35]\n"
    "v_exp_f32 v155, v155\n"
    "v_mfma_f32_16x16x32_bf16 v[32:35], a[116:119], v[176:179], v[32:35]\n"
    "ds_read_b64_tr_b16 a[106:107], v234 offset:4608\n"
    "v_cvt_pk_bf16_f32 v202, v152, v153\n"
    "v_mfma_f32_16x16x32_bf16 v[36:39], a[116:119], v[180:183], v[36:39]\n"
    "ds_read_b64_tr_b16 a[108:109], v234 offset:8704\n"
    "v_cvt_pk_bf16_f32 v203, v154, v155\n"
    "v_mfma_f32_16x16x32_bf16 v[36:39], a[112:115], v[164:167], v[36:39]\n"
    "v_exp_f32 v148, v148\n"
    "v_mfma_f32_16x16x32_bf16 v[40:43], a[112:115], v[168:171], v[40:43]\n"
    "v_exp_f32 v149, v149\n"
    "v_mfma_f32_16x16x32_bf16 v[40:43], a[116:119], v[184:187], v[40:43]\n"
    "v_exp_f32 v150, v150\n"
    "v_mfma_f32_16x16x32_bf16 v[44:47], a[116:119], v[188:191], v[44:47]\n"
    "v_exp_f32 v151, v151\n"
    "v_mfma_f32_16x16x32_bf16 v[44:47], a[112:115], v[172:175], v[44:47]\n"
    "ds_read_b64_tr_b16 a[110:111], v234 offset:12800\n"
    "v_cvt_pk_bf16_f32 v198, v148, v149\n"
    "v_mfma_f32_16x16x32_bf16 v[60:63], a[120:123], v[172:175], v[60:63]\n"
    "v_cvt_pk_bf16_f32 v199, v150, v151\n"
    "v_mfma_f32_16x16x32_bf16 v[60:63], a[124:127], v[188:191], v[60:63]\n"
    "v_exp_f32 v144, v144\n"
    "v_mfma_f32_16x16x32_bf16 v[56:59], a[124:127], v[184:187], v[56:59]\n"
    "v_exp_f32 v145, v145\n"
    "v_mfma_f32_16x16x32_bf16 v[56:59], a[120:123], v[168:171], v[56:59]\n"
    "v_exp_f32 v146, v146\n"
    "v_mfma_f32_16x16x32_bf16 v[52:55], a[120:123], v[164:167], v[52:55]\n"
    "v_exp_f32 v147, v147\n"
    "v_mfma_f32_16x16x32_bf16 v[52:55], a[124:127], v[180:183], v[52:55]\n"
    "v_cvt_pk_bf16_f32 v194, v144, v145\n"
    "v_cvt_pk_bf16_f32 v195, v146, v147\n"
    "v_mfma_f32_16x16x32_bf16 v[48:51], a[124:127], v[176:179], v[48:51]\n"
    "v_mfma_f32_16x16x32_bf16 v[48:51], a[120:123], v[160:163], v[48:51]\n"
    "s_waitcnt lgkmcnt(8)\n"
    "v_mfma_f32_16x16x32_bf16 v[128:131], a[64:67], a[0:3], v[112:115]\n"
    "ds_read_b128 a[80:83], v232 offset:12288\n"
    "v_mfma_f32_16x16x32_bf16 v[128:131], a[68:71], a[4:7], v[128:131]\n"
    "ds_read_b128 a[84:87], v232 offset:12544\n"
    "v_mfma_f32_16x16x32_bf16 v[128:131], a[72:75], a[8:11], v[128:131]\n"
    "ds_read_b128 a[88:91], v232 offset:12800\n"
    "v_mfma_f32_16x16x32_bf16 v[128:131], a[76:79], a[12:15], v[128:131]\n"
    "ds_read_b128 a[92:95], v232 offset:13056\n"
    "v_mfma_f32_16x16x32_bf16 v[132:135], a[76:79], a[28:31], v[116:119]\n"
    "ds_read_b64_tr_b16 a[112:113], v233 offset:768\n"
    "v_mfma_f32_16x16x32_bf16 v[132:135], a[72:75], a[24:27], v[132:135]\n"
    "ds_read_b64_tr_b16 a[114:115], v233 offset:4864\n"
    "v_mfma_f32_16x16x32_bf16 v[132:135], a[68:71], a[20:23], v[132:135]\n"
    "ds_read_b64_tr_b16 a[116:117], v233 offset:8960\n"
    "v_mfma_f32_16x16x32_bf16 v[132:135], a[64:67], a[16:19], v[132:135]\n"
    "v_exp_f32 v128, v128\n"
    "v_mfma_f32_16x16x32_bf16 v[136:139], a[64:67], a[32:35], v[120:123]\n"
    "v_exp_f32 v129, v129\n"
    "v_mfma_f32_16x16x32_bf16 v[136:139], a[68:71], a[36:39], v[136:139]\n"
    "v_exp_f32 v130, v130\n"
    "v_mfma_f32_16x16x32_bf16 v[136:139], a[72:75], a[40:43], v[136:139]\n"
    "v_exp_f32 v131, v131\n"
    "v_mfma_f32_16x16x32_bf16 v[136:139], a[76:79], a[44:47], v[136:139]\n"
    "ds_read_b64_tr_b16 a[118:119], v233 offset:13056\n"
    "v_cvt_pk_bf16_f32 v208, v128, v129\n"
    "v_mfma_f32_16x16x32_bf16 v[140:143], a[76:79], a[60:63], v[124:127]\n"
    "ds_read_b64_tr_b16 a[120:121], v234 offset:768\n"
    "v_cvt_pk_bf16_f32 v209, v130, v131\n"
    "v_mfma_f32_16x16x32_bf16 v[140:143], a[72:75], a[56:59], v[140:143]\n"
    "v_exp_f32 v132, v132\n"
    "v_mfma_f32_16x16x32_bf16 v[140:143], a[68:71], a[52:55], v[140:143]\n"
    "v_exp_f32 v133, v133\n"
    "v_mfma_f32_16x16x32_bf16 v[140:143], a[64:67], a[48:51], v[140:143]\n"
    "v_exp_f32 v134, v134\n"
    "s_waitcnt lgkmcnt(9)\n"
    "v_mfma_f32_16x16x32_bf16 v[64:67], a[96:99], v[160:163], v[64:67]\n"
    "v_exp_f32 v135, v135\n"
    "v_mfma_f32_16x16x32_bf16 v[64:67], a[100:103], v[176:179], v[64:67]\n"
    "ds_read_b64_tr_b16 a[122:123], v234 offset:4864\n"
    "v_cvt_pk_bf16_f32 v212, v132, v133\n"
    "v_mfma_f32_16x16x32_bf16 v[68:71], a[100:103], v[180:183], v[68:71]\n"
    "ds_read_b64_tr_b16 a[124:125], v234 offset:8960\n"
    "v_cvt_pk_bf16_f32 v213, v134, v135\n"
    "v_mfma_f32_16x16x32_bf16 v[68:71], a[96:99], v[164:167], v[68:71]\n"
    "v_exp_f32 v136, v136\n"
    "v_mfma_f32_16x16x32_bf16 v[72:75], a[96:99], v[168:171], v[72:75]\n"
    "v_exp_f32 v137, v137\n"
    "v_mfma_f32_16x16x32_bf16 v[72:75], a[100:103], v[184:187], v[72:75]\n"
    "ds_read_b64_tr_b16 a[126:127], v234 offset:13056\n"
    "v_mfma_f32_16x16x32_bf16 v[76:79], a[100:103], v[188:191], v[76:79]\n"
    "v_exp_f32 v138, v138\n"
    "v_mfma_f32_16x16x32_bf16 v[76:79], a[96:99], v[172:175], v[76:79]\n"
    "v_exp_f32 v139, v139\n"
    "v_mfma_f32_16x16x32_bf16 v[92:95], a[104:107], v[172:175], v[92:95]\n"
    "v_cvt_pk_bf16_f32 v216, v136, v137\n"
    "v_cvt_pk_bf16_f32 v217, v138, v139\n"
    "v_mfma_f32_16x16x32_bf16 v[92:95], a[108:111], v[188:191], v[92:95]\n"
    "v_exp_f32 v140, v140\n"
    "v_mfma_f32_16x16x32_bf16 v[88:91], a[108:111], v[184:187], v[88:91]\n"
    "v_exp_f32 v141, v141\n"
    "v_mfma_f32_16x16x32_bf16 v[88:91], a[104:107], v[168:171], v[88:91]\n"
    "v_exp_f32 v142, v142\n"
    "v_mfma_f32_16x16x32_bf16 v[84:87], a[104:107], v[164:167], v[84:87]\n"
    "v_exp_f32 v143, v143\n"
    "v_mfma_f32_16x16x32_bf16 v[84:87], a[108:111], v[180:183], v[84:87]\n"
    "v_cvt_pk_bf16_f32 v220, v140, v141\n"
    "v_cvt_pk_bf16_f32 v221, v142, v143\n"
    "v_mfma_f32_16x16x32_bf16 v[80:83], a[108:111], v[176:179], v[80:83]\n"
    "v_mfma_f32_16x16x32_bf16 v[80:83], a[104:107], v[160:163], v[80:83]\n"
    "v_add_u32 v233, s60, v236\n"
    "s_add_u32 s60, s60, 16384\n"
    "s_cmp_eq_u32 s60, 49152\n"
    "s_cselect_b32 s60, 0, s60\n"
    "v_xor_b32 v234, 32, v233\n"
    "v_add_u32 v232, s60, v235\n"
    "s_waitcnt vmcnt(4) lgkmcnt(0)\n"
    "s_barrier\n"
    "v_mfma_f32_16x16x32_bf16 v[156:159], a[80:83], a[48:51], v[124:127]\n"
    "ds_read_b64_tr_b16 a[96:97], v233 offset:0\n"
    "v_mfma_f32_16x16x32_bf16 v[156:159], a[84:87], a[52:55], v[156:159]\n"
    "ds_read_b64_tr_b16 a[98:99], v233 offset:4096\n"
    "v_mfma_f32_16x16x32_bf16 v[156:159], a[88:91], a[56:59], v[156:159]\n"
    "ds_read_b64_tr_b16 a[100:101], v233 offset:8192\n"
    "v_mfma_f32_16x16x32_bf16 v[156:159], a[92:95], a[60:63], v[156:159]\n"
    "ds_read_b64_tr_b16 a[102:103], v233 offset:12288\n"
    "v_mfma_f32_16x16x32_bf16 v[152:155], a[92:95], a[44:47], v[120:123]\n"
    "ds_read_b64_tr_b16 a[104:105], v234 offset:0\n"
    "v_mfma_f32_16x16x32_bf16 v[152:155], a[88:91], a[40:43], v[152:155]\n"
    "ds_read_b64_tr_b16 a[106:107], v234 offset:4096\n"
    "v_mfma_f32_16x16x32_bf16 v[152:155], a[84:87], a[36:39], v[152:155]\n"
    "ds_read_b64_tr_b16 a[108:109], v234 offset:8192\n"
    "v_mfma_f32_16x16x32_bf16 v[152:155], a[80:83], a[32:35], v[152:155]\n"
    "ds_read_b64_tr_b16 a[110:111], v234 offset:12288\n"
    "v_mfma_f32_16x16x32_bf16 v[148:151], a[80:83], a[16:19], v[116:119]\n"
    "ds_read_b64_tr_b16 a[152:153], v233 offset:256\n"
    "v_mfma_f32_16x16x32_bf16 v[148:151], a[84:87], a[20:23], v[148:151]\n"
    "ds_read_b64_tr_b16 a[154:155], v233 offset:4352\n"
    "v_exp_f32 v156, v156\n"
    "v_mfma_f32_16x16x32_bf16 v[148:151], a[88:91], a[24:27], v[148:151]\n"
    "ds_read_b64_tr_b16 a[156:157], v233 offset:8448\n"
    "v_mfma_f32_16x16x32_bf16 v[148:151], a[92:95], a[28:31], v[148:151]\n"
    "ds_read_b64_tr_b16 a[158:159], v233 offset:12544\n"
    "v_mfma_f32_16x16x32_bf16 v[144:147], a[92:95], a[12:15], v[112:115]\n"
    "ds_read_b64_tr_b16 a[160:161], v234 offset:256\n"
    "v_exp_f32 v157, v157\n"
    "v_mfma_f32_16x16x32_bf16 v[144:147], a[88:91], a[8:11], v[144:147]\n"
    "ds_read_b64_tr_b16 a[162:163], v234 offset:4352\n"
    "v_mfma_f32_16x16x32_bf16 v[144:147], a[84:87], a[4:7], v[144:147]\n"
    "ds_read_b64_tr_b16 a[164:165], v234 offset:8448\n"
    "v_mfma_f32_16x16x32_bf16 v[144:147], a[80:83], a[0:3], v[144:147]\n"
    "ds_read_b64_tr_b16 a[166:167], v234 offset:12544\n"
    "v_exp_f32 v158, v158\n"
    "v_mfma_f32_16x16x32_bf16 v[96:99], a[112:115], v[160:163], v[96:99]\n"
    "ds_read_b64_tr_b16 a[64:65], v233 offset:512\n"
    "v_mfma_f32_16x16x32_bf16 v[96:99], a[116:119], v[176:179], v[96:99]\n"
    "ds_read_b64_tr_b16 a[66:67], v233 offset:4608\n"
    "v_exp_f32 v159, v159\n"
    "v_mfma_f32_16x16x32_bf16 v[100:103], a[116:119], v[180:183], v[100:103]\n"
    "ds_read_b64_tr_b16 a[68:69], v233 offset:8704\n"
    "v_cvt_pk_bf16_f32 v222, v156, v157\n"
    "v_cvt_pk_bf16_f32 v223, v158, v159\n"
    "v_mfma_f32_16x16x32_bf16 v[100:103], a[112:115], v[164:167], v[100:103]\n"
    "ds_read_b64_tr_b16 a[70:71], v233 offset:12800\n"
    "v_mfma_f32_16x16x32_bf16 v[104:107], a[112:115], v[168:171], v[104:107]\n"
    "ds_read_b64_tr_b16 a[72:73], v234 offset:512\n"
    "v_exp_f32 v152, v152\n"
    "v_mfma_f32_16x16x32_bf16 v[104:107], a[116:119], v[184:187], v[104:107]\n"
    "ds_read_b64_tr_b16 a[74:75], v234 offset:4608\n"
    "v_mfma_f32_16x16x32_bf16 v[108:111], a[116:119], v[188:191], v[108:111]\n"
    "ds_read_b64_tr_b16 a[76:77], v234 offset:8704\n"
    "v_exp_f32 v153, v153\n"
    "v_mfma_f32_16x16x32_bf16 v[108:111], a[112:115], v[172:175], v[108:111]\n"
    "ds_read_b64_tr_b16 a[78:79], v234 offset:12800\n"
    "v_exp_f32 v154, v154\n"
    "v_mfma_f32_16x16x32_bf16 a[180:183], a[120:123], v[172:175], a[180:183]\n"
    "v_exp_f32 v155, v155\n"
    "v_cvt_pk_bf16_f32 v218, v152, v153\n"
    "v_mfma_f32_16x16x32_bf16 a[180:183], a[124:127], v[188:191], a[180:183]\n"
    "v_cvt_pk_bf16_f32 v219, v154, v155\n"
    "v_exp_f32 v148, v148\n"
    "v_mfma_f32_16x16x32_bf16 a[176:179], a[124:127], v[184:187], a[176:179]\n"
    "v_exp_f32 v149, v149\n"
    "v_mfma_f32_16x16x32_bf16 a[176:179], a[120:123], v[168:171], a[176:179]\n"
    "ds_read_b64_tr_b16 a[80:81], v233 offset:768\n"
    "v_exp_f32 v150, v150\n"
    "v_mfma_f32_16x16x32_bf16 a[172:175], a[120:123], v[164:167], a[172:175]\n"
    "ds_read_b64_tr_b16 a[82:83], v233 offset:4864\n"
    "v_exp_f32 v151, v151\n"
    "v_mfma_f32_16x16x32_bf16 a[172:175], a[124:127], v[180:183], a[172:175]\n"
    "v_cvt_pk_bf16_f32 v214, v148, v149\n"
    "v_cvt_pk_bf16_f32 v215, v150, v151\n"
    "v_mfma_f32_16x16x32_bf16 a[168:171], a[124:127], v[176:179], a[168:171]\n"
    "ds_read_b64_tr_b16 a[84:85], v233 offset:8960\n"
    "ds_read_b64_tr_b16 a[86:87], v233 offset:13056\n"
    "ds_read_b64_tr_b16 a[88:89], v234 offset:768\n"
    "v_mfma_f32_16x16x32_bf16 a[168:171], a[120:123], v[160:163], a[168:171]\n"
    "ds_read_b64_tr_b16 a[90:91], v234 offset:4864\n"
    "ds_read_b64_tr_b16 a[92:93], v234 offset:8960\n"
    "ds_read_b64_tr_b16 a[94:95], v234 offset:13056\n"
    "v_mfma_f32_16x16x32_bf16 a[140:143], a[144:147], v[204:207], a[140:143]\n"
    "v_exp_f32 v144, v144\n"
    "v_exp_f32 v145, v145\n"
    "v_mfma_f32_16x16x32_bf16 a[140:143], a[144:147], v[220:223], a[140:143]\n"
    "v_exp_f32 v146, v146\n"
    "v_exp_f32 v147, v147\n"
    "s_waitcnt lgkmcnt(0)\n"
    "v_mfma_f32_16x16x32_bf16 v[12:15], a[96:99], v[204:207], v[12:15]\n"
    "v_cvt_pk_bf16_f32 v210, v144, v145\n"
    "v_cvt_pk_bf16_f32 v211, v146, v147\n"
    "s_add_u32 s64, s64, 16777216\n"
    "v_mfma_f32_16x16x32_bf16 v[12:15], a[100:103], v[220:223], v[12:15]\n"
    "v_mfma_f32_16x16x32_bf16 v[28:31], a[108:111], v[220:223], v[28:31]\n"
    "v_mfma_f32_16x16x32_bf16 v[28:31], a[104:107], v[204:207], v[28:31]\n"
    "v_accvgpr_read_b32 v249, a140\n"
    "v_cmp_ngt_f32 vcc, s67, v249\n"
    "v_rcp_f32 v249, v249\n"
    "s_cmp_lg_u64 vcc, 0\n"
    "s_cselect_b32 s38, s69, 0\n"
    "s_or_b32 s68, s68, s38\n"
    "v_mfma_f32_16x16x32_bf16 v[44:47], a[152:155], v[204:207], v[44:47]\n"
    "v_mfma_f32_16x16x32_bf16 v[44:47], a[156:159], v[220:223], v[44:47]\n"
    "v_mul_f32 v12, v249, v12\n"
    "v_mul_f32 v13, v249, v13\n"
    "v_mul_f32 v14, v249, v14\n"
    "v_mul_f32 v15, v249, v15\n"
    "v_cvt_pk_bf16_f32 v12, v12, v13\n"
    "v_mfma_f32_16x16x32_bf16 v[60:63], a[164:167], v[220:223], v[60:63]\n"
    "v_cvt_pk_bf16_f32 v13, v14, v15\n"
    "ds_write_b64 v242, v[12:13] offset:12288\n"
    "v_mfma_f32_16x16x32_bf16 v[60:63], a[160:163], v[204:207], v[60:63]\n"
    "v_mul_f32 v28, v249, v28\n"
    "v_mul_f32 v29, v249, v29\n"
    "v_mul_f32 v30, v249, v30\n"
    "v_mul_f32 v31, v249, v31\n"
    "v_cvt_pk_bf16_f32 v28, v28, v29\n"
    "v_mfma_f32_16x16x32_bf16 v[76:79], a[64:67], v[204:207], v[76:79]\n"
    "v_cvt_pk_bf16_f32 v29, v30, v31\n"
    "ds_write_b64 v250, v[28:29] offset:12288\n"
    "v_mfma_f32_16x16x32_bf16 v[76:79], a[68:71], v[220:223], v[76:79]\n"
    "v_mul_f32 v44, v249, v44\n"
    "v_mul_f32 v45, v249, v45\n"
    "v_mul_f32 v46, v249, v46\n"
    "v_mul_f32 v47, v249, v47\n"
    "v_cvt_pk_bf16_f32 v44, v44, v45\n"
    "v_mfma_f32_16x16x32_bf16 v[92:95], a[76:79], v[220:223], v[92:95]\n"
    "v_cvt_pk_bf16_f32 v45, v46, v47\n"
    "ds_write_b64 v251, v[44:45] offset:12288\n"
    "v_mfma_f32_16x16x32_bf16 v[92:95], a[72:75], v[204:207], v[92:95]\n"
    "v_mul_f32 v60, v249, v60\n"
    "v_mul_f32 v61, v249, v61\n"
    "v_mul_f32 v62, v249, v62\n"
    "v_mul_f32 v63, v249, v63\n"
    "v_cvt_pk_bf16_f32 v60, v60, v61\n"
    "v_mfma_f32_16x16x32_bf16 v[108:111], a[80:83], v[204:207], v[108:111]\n"
    "v_cvt_pk_bf16_f32 v61, v62, v63\n"
    "ds_write_b64 v252, v[60:61] offset:12288\n"
    "v_mfma_f32_16x16x32_bf16 v[108:111], a[84:87], v[220:223], v[108:111]\n"
    "v_mul_f32 v76, v249, v76\n"
    "v_mul_f32 v77, v249, v77\n"
    "v_mul_f32 v78, v249, v78\n"
    "v_mul_f32 v79, v249, v79\n"
    "v_cvt_pk_bf16_f32 v76, v76, v77\n"
    "v_mfma_f32_16x16x32_bf16 a[180:183], a[92:95], v[220:223], a[180:183]\n"
    "v_cvt_pk_bf16_f32 v77, v78, v79\n"
    "ds_write_b64 v242, v[76:77] offset:12416\n"
    "v_mfma_f32_16x16x32_bf16 a[180:183], a[88:91], v[204:207], a[180:183]\n"
    "v_mul_f32 v92, v249, v92\n"
    "v_mul_f32 v93, v249, v93\n"
    "v_mul_f32 v94, v249, v94\n"
    "v_mul_f32 v95, v249, v95\n"
    "v_cvt_pk_bf16_f32 v92, v92, v93\n"
    "v_mfma_f32_16x16x32_bf16 a[136:139], a[144:147], v[200:203], a[136:139]\n"
    "v_cvt_pk_bf16_f32 v93, v94, v95\n"
    "ds_write_b64 v250, v[92:93] offset:12416\n"
    "v_mfma_f32_16x16x32_bf16 a[136:139], a[144:147], v[216:219], a[136:139]\n"
    "v_mul_f32 v108, v249, v108\n"
    "v_mul_f32 v109, v249, v109\n"
    "v_mul_f32 v110, v249, v110\n"
    "v_mul_f32 v111, v249, v111\n"
    "v_cvt_pk_bf16_f32 v108, v108, v109\n"
    "v_mfma_f32_16x16x32_bf16 v[8:11], a[96:99], v[200:203], v[8:11]\n"
    "v_cvt_pk_bf16_f32 v109, v110, v111\n"
    "ds_write_b64 v251, v[108:109] offset:12416\n"
    "v_mfma_f32_16x16x32_bf16 v[8:11], a[100:103], v[216:219], v[8:11]\n"
    "v_accvgpr_read_b32 v228, a180\n"
    "v_accvgpr_read_b32 v229, a181\n"
    "v_accvgpr_read_b32 v230, a182\n"
    "v_accvgpr_read_b32 v231, a183\n"
    "v_mul_f32 v228, v249, v228\n"
    "v_mfma_f32_16x16x32_bf16 v[24:27], a[108:111], v[216:219], v[24:27]\n"
    "v_mul_f32 v229, v249, v229\n"
    "v_mul_f32 v230, v249, v230\n"
    "v_mul_f32 v231, v249, v231\n"
    "v_cvt_pk_bf16_f32 v228, v228, v229\n"
    "v_cvt_pk_bf16_f32 v229, v230, v231\n"
    "v_mfma_f32_16x16x32_bf16 v[24:27], a[104:107], v[200:203], v[24:27]\n"
    "ds_write_b64 v252, v[228:229] offset:12416\n"
    "v_mfma_f32_16x16x32_bf16 v[40:43], a[152:155], v[200:203], v[40:43]\n"
    "ds_read_b128 a[48:51], v243 offset:12288\n"
    "v_mfma_f32_16x16x32_bf16 v[40:43], a[156:159], v[216:219], v[40:43]\n"
    "ds_read_b128 a[52:55], v244 offset:13312\n"
    "v_mfma_f32_16x16x32_bf16 v[56:59], a[164:167], v[216:219], v[56:59]\n"
    "ds_read_b128 a[56:59], v243 offset:14336\n"
    "v_mfma_f32_16x16x32_bf16 v[56:59], a[160:163], v[200:203], v[56:59]\n"
    "ds_read_b128 a[60:63], v244 offset:15360\n"
    "v_accvgpr_read_b32 v248, a136\n"
    "v_cmp_ngt_f32 vcc, s67, v248\n"
    "v_rcp_f32 v248, v248\n"
    "s_cmp_lg_u64 vcc, 0\n"
    "s_cselect_b32 s38, s69, 0\n"
    "s_or_b32 s68, s68, s38\n"
    "v_mul_f32 v8, v248, v8\n"
    "v_mfma_f32_16x16x32_bf16 v[72:75], a[64:67], v[200:203], v[72:75]\n"
    "v_mul_f32 v9, v248, v9\n"
    "v_mul_f32 v10, v248, v10\n"
    "v_mul_f32 v11, v248, v11\n"
    "v_cvt_pk_bf16_f32 v8, v8, v9\n"
    "v_cvt_pk_bf16_f32 v9, v10, v11\n"
    "v_mfma_f32_16x16x32_bf16 v[72:75], a[68:71], v[216:219], v[72:75]\n"
    "ds_write_b64 v242, v[8:9] offset:8192\n"
    "v_mul_f32 v24, v248, v24\n"
    "v_mul_f32 v25, v248, v25\n"
    "v_mul_f32 v26, v248, v26\n"
    "v_mul_f32 v27, v248, v27\n"
    "v_mfma_f32_16x16x32_bf16 v[88:91], a[76:79], v[216:219], v[88:91]\n"
    "v_cvt_pk_bf16_f32 v24, v24, v25\n"
    "v_cvt_pk_bf16_f32 v25, v26, v27\n"
    "ds_write_b64 v250, v[24:25] offset:8192\n"
    "v_mul_f32 v40, v248, v40\n"
    "v_mul_f32 v41, v248, v41\n"
    "v_mfma_f32_16x16x32_bf16 v[88:91], a[72:75], v[200:203], v[88:91]\n"
    "v_mul_f32 v42, v248, v42\n"
    "v_mul_f32 v43, v248, v43\n"
    "v_cvt_pk_bf16_f32 v40, v40, v41\n"
    "v_cvt_pk_bf16_f32 v41, v42, v43\n"
    "ds_write_b64 v251, v[40:41] offset:8192\n"
    "s_add_u32 s37, s64, 12288\n"
    "v_mfma_f32_16x16x32_bf16 v[104:107], a[80:83], v[200:203], v[104:107]\n"
    "s_waitcnt lgkmcnt(6)\n"
    "buffer_store_dwordx4 a[48:51], v245, s[52:55], s37 offen offset:0 sc1\n"
    "s_waitcnt lgkmcnt(5)\n"
    "buffer_store_dwordx4 a[52:55], v245, s[52:55], s37 offen offset:1024 sc1\n"
    "v_mfma_f32_16x16x32_bf16 v[104:107], a[84:87], v[216:219], v[104:107]\n"
    "s_waitcnt lgkmcnt(4)\n"
    "buffer_store_dwordx4 a[56:59], v245, s[52:55], s37 offen offset:2048 sc1\n"
    "s_waitcnt lgkmcnt(3)\n"
    "buffer_store_dwordx4 a[60:63], v245, s[52:55], s37 offen offset:3072 sc1\n"
    "v_mul_f32 v56, v248, v56\n"
    "v_mul_f32 v57, v248, v57\n"
    "v_mfma_f32_16x16x32_bf16 a[176:179], a[92:95], v[216:219], a[176:179]\n"
    "v_mul_f32 v58, v248, v58\n"
    "v_mul_f32 v59, v248, v59\n"
    "v_cvt_pk_bf16_f32 v56, v56, v57\n"
    "v_cvt_pk_bf16_f32 v57, v58, v59\n"
    "ds_write_b64 v252, v[56:57] offset:8192\n"
    "v_mfma_f32_16x16x32_bf16 a[176:179], a[88:91], v[200:203], a[176:179]\n"
    "v_mul_f32 v72, v248, v72\n"
    "v_mul_f32 v73, v248, v73\n"
    "v_mul_f32 v74, v248, v74\n"
    "v_mul_f32 v75, v248, v75\n"
    "v_cvt_pk_bf16_f32 v72, v72, v73\n"
    "v_mfma_f32_16x16x32_bf16 a[132:135], a[144:147], v[196:199], a[132:135]\n"
    "v_cvt_pk_bf16_f32 v73, v74, v75\n"
    "ds_write_b64 v242, v[72:73] offset:8320\n"
    "v_mul_f32 v88, v248, v88\n"
    "v_mul_f32 v89, v248, v89\n"
    "v_mul_f32 v90, v248, v90\n"
    "v_mfma_f32_16x16x32_bf16 a[132:135], a[144:147], v[212:215], a[132:135]\n"
    "v_mul_f32 v91, v248, v91\n"
    "v_cvt_pk_bf16_f32 v88, v88, v89\n"
    "v_cvt_pk_bf16_f32 v89, v90, v91\n"
    "ds_write_b64 v250, v[88:89] offset:8320\n"
    "v_mul_f32 v104, v248, v104\n"
    "v_mfma_f32_16x16x32_bf16 v[4:7], a[96:99], v[196:199], v[4:7]\n"
    "v_mul_f32 v105, v248, v105\n"
    "v_mul_f32 v106, v248, v106\n"
    "v_mul_f32 v107, v248, v107\n"
    "v_cvt_pk_bf16_f32 v104, v104, v105\n"
    "v_cvt_pk_bf16_f32 v105, v106, v107\n"
    "v_mfma_f32_16x16x32_bf16 v[4:7], a[100:103], v[212:215], v[4:7]\n"
    "ds_write_b64 v251, v[104:105] offset:8320\n"
    "v_accvgpr_read_b32 v224, a176\n"
    "v_accvgpr_read_b32 v225, a177\n"
    "v_accvgpr_read_b32 v226, a178\n"
    "v_accvgpr_read_b32 v227, a179\n"
    "v_mfma_f32_16x16x32_bf16 v[20:23], a[108:111], v[212:215], v[20:23]\n"
    "v_mul_f32 v224, v248, v224\n"
    "v_mul_f32 v225, v248, v225\n"
    "v_mul_f32 v226, v248, v226\n"
    "v_mul_f32 v227, v248, v227\n"
    "v_cvt_pk_bf16_f32 v224, v224, v225\n"
    "v_mfma_f32_16x16x32_bf16 v[20:23], a[104:107], v[196:199], v[20:23]\n"
    "v_cvt_pk_bf16_f32 v225, v226, v227\n"
    "ds_write_b64 v252, v[224:225] offset:8320\n"
    "v_mfma_f32_16x16x32_bf16 v[36:39], a[152:155], v[196:199], v[36:39]\n"
    "ds_read_b128 a[32:35], v243 offset:8192\n"
    "v_mfma_f32_16x16x32_bf16 v[36:39], a[156:159], v[212:215], v[36:39]\n"
    "ds_read_b128 a[36:39], v244 offset:9216\n"
    "v_mfma_f32_16x16x32_bf16 v[52:55], a[164:167], v[212:215], v[52:55]\n"
    "ds_read_b128 a[40:43], v243 offset:10240\n"
    "v_mfma_f32_16x16x32_bf16 v[52:55], a[160:163], v[196:199], v[52:55]\n"
    "ds_read_b128 a[44:47], v244 offset:11264\n"
    "v_accvgpr_read_b32 v247, a132\n"
    "v_cmp_ngt_f32 vcc, s67, v247\n"
    "v_rcp_f32 v247, v247\n"
    "s_cmp_lg_u64 vcc, 0\n"
    "s_cselect_b32 s38, s69, 0\n"
    "s_or_b32 s68, s68, s38\n"
    "v_mul_f32 v4, v247, v4\n"
    "v_mfma_f32_16x16x32_bf16 v[68:71], a[64:67], v[196:199], v[68:71]\n"
    "v_mul_f32 v5, v247, v5\n"
    "v_mul_f32 v6, v247, v6\n"
    "v_mul_f32 v7, v247, v7\n"
    "v_cvt_pk_bf16_f32 v4, v4, v5\n"
    "v_cvt_pk_bf16_f32 v5, v6, v7\n"
    "ds_write_b64 v242, v[4:5] offset:4096\n"
    "v_mfma_f32_16x16x32_bf16 v[68:71], a[68:71], v[212:215], v[68:71]\n"
    "v_mul_f32 v20, v247, v20\n"
    "v_mul_f32 v21, v247, v21\n"
    "v_mul_f32 v22, v247, v22\n"
    "v_mul_f32 v23, v247, v23\n"
    "v_cvt_pk_bf16_f32 v20, v20, v21\n"
    "v_mfma_f32_16x16x32_bf16 v[84:87], a[76:79], v[212:215], v[84:87]\n"
    "v_cvt_pk_bf16_f32 v21, v22, v23\n"
    "ds_write_b64 v250, v[20:21] offset:4096\n"
    "v_mul_f32 v36, v247, v36\n"
    "v_mul_f32 v37, v247, v37\n"
    "v_mul_f32 v38, v247, v38\n"
    "v_mul_f32 v39, v247, v39\n"
    "v_mfma_f32_16x16x32_bf16 v[84:87], a[72:75], v[196:199], v[84:87]\n"
    "v_cvt_pk_bf16_f32 v36, v36, v37\n"
    "v_cvt_pk_bf16_f32 v37, v38, v39\n"
    "ds_write_b64 v251, v[36:37] offset:4096\n"
    "s_add_u32 s37, s64, 8192\n"
    "s_waitcnt lgkmcnt(6)\n"
    "buffer_store_dwordx4 a[32:35], v245, s[52:55], s37 offen offset:0 sc1\n"
    "v_mfma_f32_16x16x32_bf16 v[100:103], a[80:83], v[196:199], v[100:103]\n"
    "s_waitcnt lgkmcnt(5)\n"
    "buffer_store_dwordx4 a[36:39], v245, s[52:55], s37 offen offset:1024 sc1\n"
    "s_waitcnt lgkmcnt(4)\n"
    "buffer_store_dwordx4 a[40:43], v245, s[52:55], s37 offen offset:2048 sc1\n"
    "s_waitcnt lgkmcnt(3)\n"
    "buffer_store_dwordx4 a[44:47], v245, s[52:55], s37 offen offset:3072 sc1\n"
    "v_mfma_f32_16x16x32_bf16 v[100:103], a[84:87], v[212:215], v[100:103]\n"
    "v_mul_f32 v52, v247, v52\n"
    "v_mul_f32 v53, v247, v53\n"
    "v_mul_f32 v54, v247, v54\n"
    "v_mul_f32 v55, v247, v55\n"
    "v_cvt_pk_bf16_f32 v52, v52, v53\n"
    "v_cvt_pk_bf16_f32 v53, v54, v55\n"
    "v_mfma_f32_16x16x32_bf16 a[172:175], a[92:95], v[212:215], a[172:175]\n"
    "ds_write_b64 v252, v[52:53] offset:4096\n"
    "v_mul_f32 v68, v247, v68\n"
    "v_mul_f32 v69, v247, v69\n"
    "v_mul_f32 v70, v247, v70\n"
    "v_mul_f32 v71, v247, v71\n"
    "v_mfma_f32_16x16x32_bf16 a[172:175], a[88:91], v[196:199], a[172:175]\n"
    "v_cvt_pk_bf16_f32 v68, v68, v69\n"
    "v_cvt_pk_bf16_f32 v69, v70, v71\n"
    "ds_write_b64 v242, v[68:69] offset:4224\n"
    "v_mul_f32 v84, v247, v84\n"
    "v_mul_f32 v85, v247, v85\n"
    "v_mul_f32 v86, v247, v86\n"
    "v_mfma_f32_16x16x32_bf16 a[128:131], a[144:147], v[192:195], a[128:131]\n"
    "v_mul_f32 v87, v247, v87\n"
    "v_cvt_pk_bf16_f32 v84, v84, v85\n"
    "v_cvt_pk_bf16_f32 v85, v86, v87\n"
    "ds_write_b64 v250, v[84:85] offset:4224\n"
    "v_mfma_f32_16x16x32_bf16 a[128:131], a[144:147], v[208:211], a[128:131]\n"
    "v_mul_f32 v100, v247, v100\n"
    "v_mul_f32 v101, v247, v101\n"
    "v_mul_f32 v102, v247, v102\n"
    "v_mul_f32 v103, v247, v103\n"
    "v_cvt_pk_bf16_f32 v100, v100, v101\n"
    "v_cvt_pk_bf16_f32 v101, v102, v103\n"
    "v_mfma_f32_16x16x32_bf16 v[0:3], a[96:99], v[192:195], v[0:3]\n"
    "ds_write_b64 v251, v[100:101] offset:4224\n"
    "v_mfma_f32_16x16x32_bf16 v[0:3], a[100:103], v[208:211], v[0:3]\n"
    "v_accvgpr_read_b32 v228, a172\n"
    "v_accvgpr_read_b32 v229, a173\n"
    "v_accvgpr_read_b32 v230, a174\n"
    "v_accvgpr_read_b32 v231, a175\n"
    "v_mul_f32 v228, v247, v228\n"
    "v_mul_f32 v229, v247, v229\n"
    "v_mul_f32 v230, v247, v230\n"
    "v_mfma_f32_16x16x32_bf16 v[16:19], a[108:111], v[208:211], v[16:19]\n"
    "v_mul_f32 v231, v247, v231\n"
    "v_cvt_pk_bf16_f32 v228, v228, v229\n"
    "v_cvt_pk_bf16_f32 v229, v230, v231\n"
    "ds_write_b64 v252, v[228:229] offset:4224\n"
    "v_mfma_f32_16x16x32_bf16 v[16:19], a[104:107], v[192:195], v[16:19]\n"
    "ds_read_b128 a[16:19], v243 offset:4096\n"
    "v_mfma_f32_16x16x32_bf16 v[32:35], a[152:155], v[192:195], v[32:35]\n"
    "ds_read_b128 a[20:23], v244 offset:5120\n"
    "v_mfma_f32_16x16x32_bf16 v[32:35], a[156:159], v[208:211], v[32:35]\n"
    "ds_read_b128 a[24:27], v243 offset:6144\n"
    "v_mfma_f32_16x16x32_bf16 v[48:51], a[164:167], v[208:211], v[48:51]\n"
    "ds_read_b128 a[28:31], v244 offset:7168\n"
    "v_accvgpr_read_b32 v246, a128\n"
    "v_cmp_ngt_f32 vcc, s67, v246\n"
    "v_rcp_f32 v246, v246\n"
    "s_cmp_lg_u64 vcc, 0\n"
    "s_cselect_b32 s38, s69, 0\n"
    "s_or_b32 s68, s68, s38\n"
    "v_mul_f32 v0, v246, v0\n"
    "v_mul_f32 v1, v246, v1\n"
    "v_mul_f32 v2, v246, v2\n"
    "v_mfma_f32_16x16x32_bf16 v[48:51], a[160:163], v[192:195], v[48:51]\n"
    "v_mul_f32 v3, v246, v3\n"
    "v_cvt_pk_bf16_f32 v0, v0, v1\n"
    "v_cvt_pk_bf16_f32 v1, v2, v3\n"
    "ds_write_b64 v242, v[0:1] offset:0\n"
    "v_mul_f32 v16, v246, v16\n"
    "v_mul_f32 v17, v246, v17\n"
    "v_mul_f32 v18, v246, v18\n"
    "v_mul_f32 v19, v246, v19\n"
    "v_mfma_f32_16x16x32_bf16 v[64:67], a[64:67], v[192:195], v[64:67]\n"
    "v_cvt_pk_bf16_f32 v16, v16, v17\n"
    "v_cvt_pk_bf16_f32 v17, v18, v19\n"
    "ds_write_b64 v250, v[16:17] offset:0\n"
    "v_mfma_f32_16x16x32_bf16 v[64:67], a[68:71], v[208:211], v[64:67]\n"
    "v_mul_f32 v32, v246, v32\n"
    "v_mul_f32 v33, v246, v33\n"
    "v_mul_f32 v34, v246, v34\n"
    "v_mul_f32 v35, v246, v35\n"
    "v_cvt_pk_bf16_f32 v32, v32, v33\n"
    "v_cvt_pk_bf16_f32 v33, v34, v35\n"
    "ds_write_b64 v251, v[32:33] offset:0\n"
    "s_add_u32 s37, s64, 4096\n"
    "s_waitcnt lgkmcnt(6)\n"
    "buffer_store_dwordx4 a[16:19], v245, s[52:55], s37 offen offset:0 sc1\n"
    "v_mfma_f32_16x16x32_bf16 v[80:83], a[76:79], v[208:211], v[80:83]\n"
    "s_waitcnt lgkmcnt(5)\n"
    "buffer_store_dwordx4 a[20:23], v245, s[52:55], s37 offen offset:1024 sc1\n"
    "s_waitcnt lgkmcnt(4)\n"
    "buffer_store_dwordx4 a[24:27], v245, s[52:55], s37 offen offset:2048 sc1\n"
    "s_waitcnt lgkmcnt(3)\n"
    "buffer_store_dwordx4 a[28:31], v245, s[52:55], s37 offen offset:3072 sc1\n"
    "v_mfma_f32_16x16x32_bf16 v[80:83], a[72:75], v[192:195], v[80:83]\n"
    "v_mul_f32 v48, v246, v48\n"
    "v_mul_f32 v49, v246, v49\n"
    "v_mul_f32 v50, v246, v50\n"
    "v_mul_f32 v51, v246, v51\n"
    "v_cvt_pk_bf16_f32 v48, v48, v49\n"
    "v_cvt_pk_bf16_f32 v49, v50, v51\n"
    "ds_write_b64 v252, v[48:49] offset:0\n"
    "v_mfma_f32_16x16x32_bf16 v[96:99], a[80:83], v[192:195], v[96:99]\n"
    "v_mfma_f32_16x16x32_bf16 v[96:99], a[84:87], v[208:211], v[96:99]\n"
    "v_mul_f32 v64, v246, v64\n"
    "v_mul_f32 v65, v246, v65\n"
    "v_mul_f32 v66, v246, v66\n"
    "v_mul_f32 v67, v246, v67\n"
    "v_cvt_pk_bf16_f32 v64, v64, v65\n"
    "v_cvt_pk_bf16_f32 v65, v66, v67\n"
    "ds_write_b64 v242, v[64:65] offset:128\n"
    "v_mfma_f32_16x16x32_bf16 a[168:171], a[92:95], v[208:211], a[168:171]\n"
    "v_mfma_f32_16x16x32_bf16 a[168:171], a[88:91], v[192:195], a[168:171]\n"
    "v_mul_f32 v80, v246, v80\n"
    "v_mul_f32 v81, v246, v81\n"
    "v_mul_f32 v82, v246, v82\n"
    "v_mul_f32 v83, v246, v83\n"
    "v_cvt_pk_bf16_f32 v80, v80, v81\n"
    "v_cvt_pk_bf16_f32 v81, v82, v83\n"
    "ds_write_b64 v250, v[80:81] offset:128\n"
    "v_mul_f32 v96, v246, v96\n"
    "v_mul_f32 v97, v246, v97\n"
    "v_mul_f32 v98, v246, v98\n"
    "v_mul_f32 v99, v246, v99\n"
    "v_cvt_pk_bf16_f32 v96, v96, v97\n"
    "v_cvt_pk_bf16_f32 v97, v98, v99\n"
    "ds_write_b64 v251, v[96:97] offset:128\n"
    "v_accvgpr_read_b32 v224, a168\n"
    "v_accvgpr_read_b32 v225, a169\n"
    "v_accvgpr_read_b32 v226, a170\n"
    "v_accvgpr_read_b32 v227, a171\n"
    "v_mul_f32 v224, v246, v224\n"
    "v_mul_f32 v225, v246, v225\n"
    "v_mul_f32 v226, v246, v226\n"
    "v_mul_f32 v227, v246, v227\n"
    "v_cvt_pk_bf16_f32 v224, v224, v225\n"
    "v_cvt_pk_bf16_f32 v225, v226, v227\n"
    "ds_write_b64 v252, v[224:225] offset:128\n"
    "ds_read_b128 a[0:3], v243 offset:0\n"
    "ds_read_b128 a[4:7], v244 offset:1024\n"
    "ds_read_b128 a[8:11], v243 offset:2048\n"
    "ds_read_b128 a[12:15], v244 offset:3072\n"
    "s_add_u32 s37, s64, 0\n"
    "s_waitcnt lgkmcnt(3)\n"
    "buffer_store_dwordx4 a[0:3], v245, s[52:55], s37 offen offset:0 sc1\n"
    "s_waitcnt lgkmcnt(2)\n"
    "buffer_store_dwordx4 a[4:7], v245, s[52:55], s37 offen offset:1024 sc1\n"
    "s_waitcnt lgkmcnt(1)\n"
    "buffer_store_dwordx4 a[8:11], v245, s[52:55], s37 offen offset:2048 sc1\n"
    "s_waitcnt lgkmcnt(0)\n"
    "buffer_store_dwordx4 a[12:15], v245, s[52:55], s37 offen offset:3072 sc1\n"
    "s_lshl_b32 s39, s39, 8\n"
    "s_or_b32 %0, s68, s39\n"
    : "=s"(flag), "+{v0}"(tid)
    : "s"(__builtin_amdgcn_kernarg_segment_ptr()), "s"(hb), "s"(ldsb)
    : "v1", "v2", "v3", "v4", "v5", "v6", "v7", "v8", "v9", "v10", "v11", "v12", "v13", "v14", "v15", "v16", "v17", "v18", "v19", "v20", "v21", "v22", "v23", "v24", "v25", "v26", "v27", "v28", "v29", "v30", "v31", "v32", "v33", "v34", "v35", "v36", "v37", "v38", "v39", "v40", "v41", "v42", "v43", "v44", "v45", "v46", "v47", "v48", "v49", "v50", "v51", "v52", "v53", "v54", "v55", "v56", "v57", "v58", "v59", "v60", "v61", "v62", "v63", "v64", "v65", "v66", "v67", "v68", "v69", "v70", "v71", "v72", "v73", "v74", "v75", "v76", "v77", "v78", "v79", "v80", "v81", "v82", "v83", "v84", "v85", "v86", "v87", "v88", "v89", "v90", "v91", "v92", "v93", "v94", "v95", "v96", "v97", "v98", "v99", "v100", "v101", "v102", "v103", "v104", "v105", "v106", "v107", "v108", "v109", "v110", "v111", "v112", "v113", "v114", "v115", "v116", "v117", "v118", "v119", "v120", "v121", "v122", "v123", "v124", "v125", "v126", "v127", "v128", "v129", "v130", "v131", "v132", "v133", "v134", "v135", "v136", "v137", "v138", "v139", "v140", "v141", "v142", "v143", "v144", "v145", "v146", "v147", "v148", "v149", "v150", "v151", "v152", "v153", "v154", "v155", "v156", "v157", "v158", "v159", "v160", "v161", "v162", "v163", "v164", "v165", "v166", "v167", "v168", "v169", "v170", "v171", "v172", "v173", "v174", "v175", "v176", "v177", "v178", "v179", "v180", "v181", "v182", "v183", "v184", "v185", "v186", "v187", "v188", "v189", "v190", "v191", "v192", "v193", "v194", "v195", "v196", "v197", "v198", "v199", "v200", "v201", "v202", "v203", "v204", "v205", "v206", "v207", "v208", "v209", "v210", "v211", "v212", "v213", "v214", "v215", "v216", "v217", "v218", "v219", "v220", "v221", "v222", "v223", "v224", "v225", "v226", "v227", "v228", "v229", "v230", "v231", "v232", "v233", "v234", "v235", "v236", "v237", "v238", "v239", "v240", "v241", "v242", "v243", "v244", "v245", "v246", "v247", "v248", "v249", "v250", "v251", "v252", "v253", "v254", "v255", "a0", "a1", "a2", "a3", "a4", "a5", "a6", "a7", "a8", "a9", "a10", "a11", "a12", "a13", "a14", "a15", "a16", "a17", "a18", "a19", "a20", "a21", "a22", "a23", "a24", "a25", "a26", "a27", "a28", "a29", "a30", "a31", "a32", "a33", "a34", "a35", "a36", "a37", "a38", "a39", "a40", "a41", "a42", "a43", "a44", "a45", "a46", "a47", "a48", "a49", "a50", "a51", "a52", "a53", "a54", "a55", "a56", "a57", "a58", "a59", "a60", "a61", "a62", "a63", "a64", "a65", "a66", "a67", "a68", "a69", "a70", "a71", "a72", "a73", "a74", "a75", "a76", "a77", "a78", "a79", "a80", "a81", "a82", "a83", "a84", "a85", "a86", "a87", "a88", "a89", "a90", "a91", "a92", "a93", "a94", "a95", "a96", "a97", "a98", "a99", "a100", "a101", "a102", "a103", "a104", "a105", "a106", "a107", "a108", "a109", "a110", "a111", "a112", "a113", "a114", "a115", "a116", "a117", "a118", "a119", "a120", "a121", "a122", "a123", "a124", "a125", "a126", "a127", "a128", "a129", "a130", "a131", "a132", "a133", "a134", "a135", "a136", "a137", "a138", "a139", "a140", "a141", "a142", "a143", "a144", "a145", "a146", "a147", "a148", "a149", "a150", "a151", "a152", "a153", "a154", "a155", "a156", "a157", "a158", "a159", "a160", "a161", "a162", "a163", "a164", "a165", "a166", "a167", "a168", "a169", "a170", "a171", "a172", "a173", "a174", "a175", "a176", "a177", "a178", "a179", "a180", "a181", "a182", "a183", "a184", "a185", "a186", "a187", "a188", "a189", "a190", "a191", "a192", "a193", "a194", "a195", "a196", "a197", "a198", "a199", "a200", "a201", "a202", "a203", "a204", "a205", "a206", "a207", "a208", "a209", "a210", "a211", "a212", "a213", "a214", "a215", "a216", "a217", "a218", "a219", "a220", "a221", "a222", "a223", "a224", "a225", "a226", "a227", "a228", "a229", "a230", "a231", "s36", "s37", "s38", "s39", "s40", "s41", "s42", "s43", "s44", "s45", "s46", "s47", "s48", "s49", "s50", "s51", "s52", "s53", "s54", "s55", "s56", "s57", "s58", "s59", "s60", "s61", "s62", "s63", "s64", "s65", "s66", "s67", "s68", "s69", "s70", "s71", "s72", "s73", "s74", "s75", "s76", "s77", "s78", "s79", "s80", "s81", "s82", "s83", "s84", "s85", "s86", "s87", "s88", "s89", "s90", "s91", "s92", "s93", "vcc", "scc", "memory");
  if (!0 && __builtin_expect((flag & 15u) != 0u, 0)) {
    unsigned lane;
    asm volatile("s_waitcnt vmcnt(0)\n\tv_mbcnt_lo_u32_b32 %0, -1, 0\n\tv_mbcnt_hi_u32_b32 %0, -1, %0" : "=v"(lane) :: "memory");
#pragma unroll 1
    for (unsigned h = 0; h < 4; ++h)
      if ((flag >> h) & 1u) {
        const size_t head = hb + 256u * h, row = head * 256u + (flag >> 8) * 64u + lane;
        slow_rows(Q + row * 256, K + head * 131072, V + head * 131072, O + row * 256);
      }
  }
}
extern "C" void kernel_launch(void* const* d_in, const int* in_sizes, int n_in, void* d_out, int out_size, void* d_ws, size_t ws_size, hipStream_t stream) {
  hipLaunchKernelGGL(attn_fwd, dim3(256), dim3(256), 0, stream, (const char*)d_in[0], (const char*)d_in[1], (const char*)d_in[2], (char*)d_out, (char*)d_ws);
}
```
